```python
import torch

def _softmax(x, axis=-1):
    return torch.softmax(x, dim=axis)
def _gelu(x):
    return torch.nn.functional.gelu(x, approximate="tanh")
def _silu(x):
    return torch.nn.functional.silu(x)
def _sigmoid(x):
    return torch.sigmoid(x)
def _layernorm(x, axis=-1, eps=1e-5):
    m = x.mean(dim=axis, keepdim=True)
    v = x.var(dim=axis, keepdim=True, unbiased=False)
    return (x - m) / torch.sqrt(v + eps)
def _scatter_add(tgt, idx, src, axis=0):
    return tgt.clone().index_add_(axis, idx, src)
def _segment_sum(src, seg, n):
    out = torch.zeros((n,) + src.shape[1:], dtype=src.dtype)
    return out.index_add_(0, seg, src)
def _pad(x, pw):
    flat = tuple(v for pair in reversed(pw) for v in pair)
    return torch.nn.functional.pad(x, flat)

kernel_name = 'sequential_preLN__ssm_conv_s4__glu_gated_bilinear__prompt'

def setup_inputs():

    _w = (lambda a: (lambda t: t.to(torch.bfloat16) if t.is_floating_point() else t)(torch.as_tensor(a)))
    def _A_setup():
        B, L, D, N = 1, 2048, 448, 256
        A_re = -(lambda _x: torch.exp(torch.as_tensor(_x)))((lambda *s: torch.randn(*s, dtype=torch.bfloat16) * 3.2)(D, N))
        A_im = torch.pi * (lambda *s: torch.randn(*s, dtype=torch.bfloat16) * 0.05)(D, N)
        B_re = (lambda *s: torch.randn(*s, dtype=torch.bfloat16) * 0.05)(D, N)
        B_im = (lambda *s: torch.randn(*s, dtype=torch.bfloat16) * 0.05)(D, N)
        C_re = (lambda *s: torch.randn(*s, dtype=torch.bfloat16) * 0.05)(D, N)
        C_im = (lambda *s: torch.randn(*s, dtype=torch.bfloat16) * 0.05)(D, N)
        log_dt = (lambda *s: torch.randn(*s, dtype=torch.bfloat16) * 0.05)(D) * 0.5 - 4.0
        Dskip = (lambda *s: torch.randn(*s, dtype=torch.bfloat16) * 0.05)(D)
        return {
            "u": _w((lambda *s: torch.randn(*s, dtype=torch.bfloat16) * 0.05)(B, L, D)),
            "A_re": _w(A_re),
            "A_im": _w(A_im),
            "B_re": _w(B_re),
            "B_im": _w(B_im),
            "C_re": _w(C_re),
            "C_im": _w(C_im),
            "log_dt": _w(log_dt),
            "Dskip": _w(Dskip),
        }
    _A_in = _A_setup()

    def _B_setup():
        B, S, D, F = 1, 2048, 448, 1792
        return {
            "x": _w((lambda *s: torch.randn(*s, dtype=torch.bfloat16) * 0.05)(B, S, D)),
            "w_gate": _w((lambda *s: torch.randn(*s, dtype=torch.bfloat16) * 0.4)(D, F) * (D ** -0.5)),
            "w_up": _w((lambda *s: torch.randn(*s, dtype=torch.bfloat16) * 0.4)(D, F) * (D ** -0.5)),
            "w_down": _w((lambda *s: torch.randn(*s, dtype=torch.bfloat16) * 0.4)(F, D) * (F ** -0.5)),
        }
    _B_in = _B_setup()

    return {
        "A_u": _A_in["u"],
        "A_A_re": _A_in["A_re"],
        "A_A_im": _A_in["A_im"],
        "A_B_re": _A_in["B_re"],
        "A_B_im": _A_in["B_im"],
        "A_C_re": _A_in["C_re"],
        "A_C_im": _A_in["C_im"],
        "A_log_dt": _A_in["log_dt"],
        "A_Dskip": _A_in["Dskip"],
        "B_x": _B_in["x"],
        "B_w_gate": _B_in["w_gate"],
        "B_w_up": _B_in["w_up"],
        "B_w_down": _B_in["w_down"],
    }

def reference(A_u, A_A_re, A_A_im, A_B_re, A_B_im, A_C_re, A_C_im, A_log_dt, A_Dskip, B_x, B_w_gate, B_w_up, B_w_down):
    def _A_ref(u, A_re, A_im, B_re, B_im, C_re, C_im, log_dt, Dskip):
        Bsz, L, D = u.shape
        dt = torch.exp(log_dt)[:, None]
        A = A_re + 1j * A_im
        Bc = B_re + 1j * B_im
        Cc = C_re + 1j * C_im
        logdA = dt * A
        dB = dt * Bc
        l = (lambda x, dt: x.to(dt))(torch.arange(L), A_re.dtype)
        P = torch.exp(logdA[:, :, None] * l[None, None, :])
        K = torch.einsum("dn,dnl,dn->dl", Cc, P, dB).real
        L2 = 2 * L
        Kp = _pad(K, ((0, 0), (0, L)))
        up = (lambda x, *d: x.permute(*d))(u, 0, 2, 1)
        up = _pad(up, ((0, 0), (0, 0), (0, L)))
        Kf = (lambda x, axes=(-2,-1): torch.fft.rfft2(x.float(), dim=axes))(Kp[:, None, :], axes=(-2, -1))
        Uf = (lambda x, axes=(-2,-1): torch.fft.rfft2(x.float(), dim=axes))(up[:, :, None, :], axes=(-2, -1))
        Yf = Uf * Kf[None, :, :, :]
        y = (lambda x, s, axes=(-2,-1): torch.fft.irfft2(x, s=s, dim=axes).to(torch.bfloat16))(Yf, (1, L2), axes=(-2, -1))[:, :, 0, :L]
        y = (lambda x, *d: x.permute(*d))(y, 0, 2, 1) + u * Dskip[None, None, :]
        return y

    def _B_ref(x, w_gate, w_up, w_down):
        g = torch.einsum("bsd,df->bsf", x, w_gate)
        u = torch.einsum("bsd,df->bsf", x, w_up)
        h = g * u
        return torch.einsum("bsf,fd->bsd", h, w_down)

    x = A_u
    h = x + _A_ref(_layernorm(x, axis=-1), A_A_re, A_A_im, A_B_re, A_B_im, A_C_re, A_C_im, A_log_dt, A_Dskip)
    h2 = h + _B_ref(_layernorm(h, axis=-1), B_w_gate, B_w_up, B_w_down)
    return h2
```

```cpp
#include <hip/hip_runtime.h>
#include <hip/hip_bf16.h>
#include <stdint.h>

typedef unsigned short u16;
__device__ __forceinline__ float bf2f(u16 v){ return __uint_as_float(((unsigned)v)<<16); }
__device__ __forceinline__ u16 f2bf(float f){ unsigned u=__float_as_uint(f); u += 0x7fffu + ((u>>16)&1u); return (u16)(u>>16); }
__device__ __forceinline__ float rbf(float f){ return bf2f(f2bf(f)); }

constexpr int L=2048, D=448, N=256, F=1792;

__global__ void k_ln(const u16* __restrict__ in, u16* __restrict__ out){
  int t=blockIdx.x; int lane=threadIdx.x;
  float v[7]; float s=0;
  for(int i=0;i<7;i++){ v[i]=bf2f(in[t*D+lane+64*i]); s+=v[i]; }
  for(int m=32;m>=1;m>>=1) s+=__shfl_xor(s,m);
  float mean=s/D; float q=0;
  for(int i=0;i<7;i++){ float d=v[i]-mean; q+=d*d; }
  for(int m=32;m>=1;m>>=1) q+=__shfl_xor(q,m);
  float var=q/D;
  float mb=rbf(mean), vb=rbf(var);
  float sd=rbf(sqrtf(rbf(vb+1e-5f)));
  for(int i=0;i<7;i++){ float d=rbf(v[i]-mb); out[t*D+lane+64*i]=f2bf(d/sd); }
}

__global__ void k_kgen(const u16* Are,const u16* Aim,const u16* Bre,const u16* Bim,const u16* Cre,const u16* Cim,const u16* logdt,float* K){
  int d=blockIdx.y; int l=blockIdx.x*256+threadIdx.x;
  float dt=rbf(expf(bf2f(logdt[d])));
  float lf=rbf((float)l);
  float acc=0;
  for(int n=0;n<N;n++){
    float are=dt*bf2f(Are[d*N+n]), aim=dt*bf2f(Aim[d*N+n]);
    float bre=dt*bf2f(Bre[d*N+n]), bim=dt*bf2f(Bim[d*N+n]);
    float cre=bf2f(Cre[d*N+n]), cim=bf2f(Cim[d*N+n]);
    float xr=cre*bre-cim*bim, xi=cre*bim+cim*bre;
    float e=expf(are*lf); float sn,cs; sincosf(aim*lf,&sn,&cs);
    acc+= e*(xr*cs-xi*sn);
  }
  K[d*L+l]=acc;
}

__global__ void k_conv(const u16* x,const u16* u,const float* K,const u16* Dskip,u16* h){
  int t=blockIdx.x; int d=blockIdx.y*64+threadIdx.x;
  float acc=0;
  for(int s=0;s<=t;s++) acc+=K[d*L+s]*bf2f(u[(t-s)*D+d]);
  float y=rbf(acc);
  float ud=rbf(bf2f(u[t*D+d])*bf2f(Dskip[d]));
  float y2=rbf(y+ud);
  h[t*D+d]=f2bf(bf2f(x[t*D+d])+y2);
}

__global__ void k_gemm1(const u16* n2,const u16* wg,const u16* wu,u16* H){
  int t=blockIdx.x; int f=blockIdx.y*256+threadIdx.x;
  float g=0,u=0;
  for(int k=0;k<D;k++){ float a=bf2f(n2[t*D+k]); g+=a*bf2f(wg[k*F+f]); u+=a*bf2f(wu[k*F+f]); }
  H[t*F+f]=f2bf(rbf(g)*rbf(u));
}
__global__ void k_gemm2(const u16* H,const u16* wd,const u16* h,u16* out){
  int t=blockIdx.x; int d=blockIdx.y*64+threadIdx.x;
  float a=0;
  for(int k=0;k<F;k++) a+=bf2f(H[t*F+k])*bf2f(wd[k*D+d]);
  out[t*D+d]=f2bf(bf2f(h[t*D+d])+rbf(a));
}

extern "C" void kernel_launch(void* const* d_in, const int* in_sizes, int n_in,
                              void* d_out, int out_size, void* d_ws, size_t ws_size,
                              hipStream_t stream) {
  const u16* x=(const u16*)d_in[0];
  const u16* Are=(const u16*)d_in[1]; const u16* Aim=(const u16*)d_in[2];
  const u16* Bre=(const u16*)d_in[3]; const u16* Bim=(const u16*)d_in[4];
  const u16* Cre=(const u16*)d_in[5]; const u16* Cim=(const u16*)d_in[6];
  const u16* logdt=(const u16*)d_in[7]; const u16* Dskip=(const u16*)d_in[8];
  const u16* wg=(const u16*)d_in[10]; const u16* wu=(const u16*)d_in[11]; const u16* wd=(const u16*)d_in[12];
  char* ws=(char*)d_ws;
  u16* u=(u16*)ws; ws+=L*D*2;
  u16* h=(u16*)ws; ws+=L*D*2;
  u16* n2=(u16*)ws; ws+=L*D*2;
  float* K=(float*)ws; ws+=D*L*4;
  u16* H=(u16*)ws; ws+=L*F*2;
  k_ln<<<L,64,0,stream>>>(x,u);
  k_kgen<<<dim3(L/256,D),256,0,stream>>>(Are,Aim,Bre,Bim,Cre,Cim,logdt,K);
  k_conv<<<dim3(L,D/64),64,0,stream>>>(x,u,K,Dskip,h);
  k_ln<<<L,64,0,stream>>>(h,n2);
  k_gemm1<<<dim3(L,F/256),256,0,stream>>>(n2,wg,wu,H);
  k_gemm2<<<dim3(L,D/64),64,0,stream>>>(H,wd,h,(u16*)d_out);
}
```

```cpp
#include <hip/hip_runtime.h>
#include <stdint.h>

typedef unsigned short u16;
typedef unsigned int u32;
typedef unsigned long long u64;
typedef float f32x2 __attribute__((ext_vector_type(2)));
typedef float f32x4 __attribute__((ext_vector_type(4)));
typedef float f32x16 __attribute__((ext_vector_type(16)));
typedef unsigned u32x2 __attribute__((ext_vector_type(2)));
typedef unsigned u32x4 __attribute__((ext_vector_type(4)));
typedef short s16x4 __attribute__((ext_vector_type(4)));
typedef short s16x8 __attribute__((ext_vector_type(8)));
typedef __bf16 bf16x8 __attribute__((ext_vector_type(8)));
typedef __bf16 bf16x2 __attribute__((ext_vector_type(2)));
typedef _Float16 f16x8 __attribute__((ext_vector_type(8)));
typedef _Float16 f16x2 __attribute__((ext_vector_type(2)));
#define DI __device__ __forceinline__
#define LAS __attribute__((address_space(3)))
#define GAS __attribute__((address_space(1)))
typedef GAS unsigned gu32;
typedef GAS unsigned long long gu64;
#define RLX_AGENT __ATOMIC_RELAXED, __HIP_MEMORY_SCOPE_AGENT

constexpr int SL = 2048, DM = 448, NS = 256, FF = 1792;

constexpr size_t WS_EPOCH = 0;
constexpr size_t WS_GRAN  = 4096;
constexpr size_t WS_CS    = 32768;
constexpr size_t WS_HT    = 65536;
constexpr size_t WS_H     = WS_HT + (size_t)DM * SL * 2;
constexpr size_t WS_DBG   = WS_H + (size_t)SL * FF * 2;

#define STAMP(k, i) do {} while (0)

DI float bf2f(u32 v) { return __uint_as_float(v << 16); }
DI u32 pk_bf16(float a, float b) { f32x2 v = {a, b}; return __builtin_bit_cast(u32, __builtin_convertvector(v, bf16x2)); }
DI u32 pk_f16(float a, float b) { f32x2 v = {a, b}; return __builtin_bit_cast(u32, __builtin_convertvector(v, f16x2)); }
DI float rbf(float a) { return __uint_as_float(pk_bf16(a, a) << 16); }

template <int CTRL, int RM> DI float dpp_add(float v) {
  return v + __int_as_float(__builtin_amdgcn_update_dpp(0, __float_as_int(v), CTRL, RM, 0xf, true));
}
template <int CTRL, int RM> DI float dpp_max(float v) {
  return fmaxf(v, __int_as_float(__builtin_amdgcn_update_dpp(0, __float_as_int(v), CTRL, RM, 0xf, true)));
}
DI float wave_sum(float v) {
  v = dpp_add<0x111, 0xf>(v); v = dpp_add<0x112, 0xf>(v); v = dpp_add<0x114, 0xf>(v); v = dpp_add<0x118, 0xf>(v);
  v = dpp_add<0x142, 0xa>(v); v = dpp_add<0x143, 0xc>(v);
  return __int_as_float(__builtin_amdgcn_readlane(__float_as_int(v), 63));
}
DI float wave_max(float v) {
  v = dpp_max<0x111, 0xf>(v); v = dpp_max<0x112, 0xf>(v); v = dpp_max<0x114, 0xf>(v); v = dpp_max<0x118, 0xf>(v);
  v = dpp_max<0x142, 0xa>(v); v = dpp_max<0x143, 0xc>(v);
  return __int_as_float(__builtin_amdgcn_readlane(__float_as_int(v), 63));
}

struct IdxTab { u16 v[2064]; };
constexpr int tap_entry(int l) {
  if (l < 0) return 21 * 32;
  int lam = l;
  if (l >= 256) {
    int sh = l >= 1024 ? 3 : (l >= 512 ? 2 : 1);
    int q = l >> sh, rem = l & ((1 << sh) - 1), half = 1 << (sh - 1);
    if (rem > half || (rem == half && (q & 1))) q += 1;
    lam = q << sh;
  }
  int hq = lam >> 8, lo = lam & 255, c = lo >> 3, r = 0;
  if (hq == 0) r = lo & 7;
  else if (hq == 1) r = 8 + ((lo & 7) >> 1);
  else if (hq == 2) r = 12 + ((lo & 7) >> 2);
  else if (hq == 3) r = 14 + ((lo & 7) >> 2);
  else if (hq < 8) r = 16 + hq - 4;
  else { r = 20; c = 0; }
  return r * 32 + c;
}
constexpr IdxTab make_tab() { IdxTab t{}; for (int y = 0; y < 2064; ++y) t.v[y] = (u16)(2 * tap_entry(2047 - y)); return t; }
__device__ const IdxTab RIDX __attribute__((aligned(16))) = make_tab();

struct cpx { float r, i; };
DI cpx cmul(cpx a, cpx b) { return {a.r * b.r - a.i * b.i, a.r * b.i + a.i * b.r}; }
DI cpx csq(cpx a) { return {a.r * a.r - a.i * a.i, 2.f * a.r * a.i}; }
DI cpx cexp_s(float zr, float zi, float s) {
  float m = __builtin_amdgcn_exp2f(zr * (s * 1.44269504088896341f));
  float a = __builtin_amdgcn_fractf(zi * (s * 0.15915494309189535f));
  return {m * __builtin_amdgcn_cosf(a), m * __builtin_amdgcn_sinf(a)};
}
DI float pow2_for(float mx, int target) {
  int e = (int)(__float_as_uint(mx) >> 23);
  int be = 127 + target - (e - 126);
  be = be > 254 ? 254 : (be < 1 ? 1 : be);
  return __uint_as_float((u32)be << 23);
}
DI float pow2_inv(float p) { return __uint_as_float((254u - (__float_as_uint(p) >> 23)) << 23); }

constexpr int A_SCR = 0, A_SCRW = 9216;
constexpr int A_CP = 73728, A_CPS = 4160, A_CPC = 8 * A_CPS;
constexpr int A_UU = A_CP + 2 * A_CPC, A_UUC = 95 * 80;
constexpr int A_TB = A_UU + 2 * A_UUC, A_TBC = 22 * 32 * 2;
constexpr int A_SW = A_TB + 2 * A_TBC;
constexpr int A_LDS = A_SW + 64;
static_assert(A_LDS <= 163840, "lds");

__global__ __launch_bounds__(512, 2) void ka_conv(const u16* __restrict__ x, const u16* __restrict__ Are, const u16* __restrict__ Aim,
    const u16* __restrict__ Bre, const u16* __restrict__ Bim, const u16* __restrict__ Cre, const u16* __restrict__ Cim,
    const u16* __restrict__ logdt, const u16* __restrict__ Dskip, const u16* __restrict__ wg, const u16* __restrict__ wu,
    char* ws) {
  extern __shared__ __attribute__((aligned(16))) char lds[];
  char* dbg = ws + WS_DBG; (void)dbg;
  const int tid = threadIdx.x, lane = tid & 63;
  const int wv = __builtin_amdgcn_readfirstlane(tid >> 6);
  const int b = blockIdx.x, xcd = b & 7, j = b >> 3;
  const bool worker = j < 28;
  STAMP(0, 0);
  gu32* epoch_p = (gu32*)(ws + WS_EPOCH);
  gu64* gran = (gu64*)(ws + WS_GRAN);
  const u32 tag = __hip_atomic_load(epoch_p, RLX_AGENT) + 1u;

  const int trow = 8 * b + wv;
  u32x4 xr = *(const u32x4*)(x + trow * DM + (lane < 56 ? lane : 0) * 8);
  const int w = worker ? xcd * 28 + j : 0, c0 = 2 * w;
  const int ch = tid >> 8, n = tid & 255;
  const int idx = (c0 + ch) * NS + n;
  const u32 p_are = Are[idx], p_aim = Aim[idx], p_bre = Bre[idx], p_bim = Bim[idx], p_cre = Cre[idx], p_cim = Cim[idx];
  const u32 p_ldt = logdt[c0 + ch], p_d01 = *(const u32*)(Dskip + c0);
  u32 xc[4];
#pragma unroll
  for (int i = 0; i < 4; ++i) xc[i] = *(const u32*)(x + (4 * tid + i) * DM + c0);
  const u32x4 ix0 = *(const u32x4*)(RIDX.v + 8 * (tid & 255)), ix1 = *(const u32x4*)(RIDX.v + 8 * (tid & 255) + 8);
  {
    float v[8];
#pragma unroll
    for (int i = 0; i < 4; ++i) { const u32 xw = lane < 56 ? xr[i] : 0u; v[2 * i] = bf2f(xw & 0xffffu); v[2 * i + 1] = __uint_as_float(xw & 0xffff0000u); }
    float s = 0.f;
#pragma unroll
    for (int i = 0; i < 8; ++i) s += v[i];
    const float mean = wave_sum(s) * (1.f / DM);
    float q = 0.f;
    if (lane < 56) {
#pragma unroll
      for (int i = 0; i < 8; ++i) { float d = v[i] - mean; q += d * d; }
    }
    const float var = wave_sum(q) * (1.f / DM);
    const float sd = rbf(__builtin_sqrtf(rbf(rbf(var) + 1e-5f)));
    const u32 val = (pk_bf16(mean, sd));
    if (lane == 0) __hip_atomic_store(gran + trow, ((u64)tag << 32) | val, RLX_AGENT);
  }
  STAMP(0, 1);
  if (!worker) {
    const int s = xcd * 4 + (j - 28);
    const u16* W = (s >> 4) ? wu : wg;
    const int colb = (s & 15) * 112;
    float a[8] = {0, 0, 0, 0, 0, 0, 0, 0};
    const int cc = tid % 14, rg = tid / 14;
    if (tid < 448) {
      u32x4 v[14];
#pragma unroll
      for (int r = 0; r < 14; ++r) v[r] = *(const u32x4*)(W + (rg * 14 + r) * FF + colb + cc * 8);
#pragma unroll
      for (int r = 0; r < 14; ++r)
#pragma unroll
        for (int i = 0; i < 4; ++i) { a[2 * i] += bf2f(v[r][i] & 0xffffu); a[2 * i + 1] += __uint_as_float(v[r][i] & 0xffff0000u); }
      float* red = (float*)lds;
#pragma unroll
      for (int i = 0; i < 8; ++i) red[rg * 112 + cc * 8 + i] = a[i];
    }
    __syncthreads();
    if (tid < 112) {
      float t = 0.f;
      for (int r = 0; r < 32; ++r) t += ((float*)lds)[r * 112 + tid];
      ((float*)(ws + WS_CS))[(s >> 4) * FF + colb + tid] = t;
    }
    return;
  }
  if (tid < 48) { int cc = tid / 24, r = tid % 24; *(u32x4*)(lds + A_CP + cc * A_CPC + (r / 3) * A_CPS + (256 + r % 3) * 16) = (u32x4){0, 0, 0, 0}; }
  if (tid < 310) { int cc = tid / 155, r = tid % 155; *(u32x4*)(lds + A_UU + cc * A_UUC + r * 16) = (u32x4){0, 0, 0, 0}; }
  if (tid < 8) { int cc = tid >> 2; *(u32x4*)(lds + A_TB + cc * A_TBC + 21 * 64 + (tid & 3) * 16) = (u32x4){0, 0, 0, 0}; }

  {
    const float dt = rbf(__builtin_amdgcn_exp2f(bf2f(p_ldt) * 1.44269504088896341f));
    const float zr = dt * bf2f(p_are), zi = dt * bf2f(p_aim);
    const float br = dt * bf2f(p_bre), bi = dt * bf2f(p_bim);
    const float cr = bf2f(p_cre), ci = bf2f(p_cim);
    cpx CB = {cr * br - ci * bi, cr * bi + ci * br};
    const float mx = wave_max(fmaxf(fabsf(CB.r), fabsf(CB.i)));
    const float sabs = wave_sum(fabsf(CB.r) + fabsf(CB.i));
    const float scA = pow2_for(mx, 14);
    if (lane == 0) { ((float*)(lds + A_SW))[wv] = pow2_inv(scA) * (1.f / 16384.f); ((float*)(lds + A_SW))[8 + wv] = sabs; }
    char* scr = lds + A_SCR + wv * A_SCRW;
    const cpx E1 = cexp_s(zr, zi, 1.f), E8 = cexp_s(zr, zi, 8.f), F = cexp_s(zr, zi, 256.f);
    {
      cpx p = {16384.f, 0.f};
#pragma unroll
      for (int c = 0; c < 32; ++c) {
        *(u32*)(scr + c * 272 + lane * 4) = pk_f16(p.r, p.i);
        if (c < 31) p = cmul(p, E8);
      }
    }
    f16x8 bf[8];
#pragma unroll
    for (int ks = 0; ks < 8; ++ks) bf[ks] = *(const f16x8*)(scr + (lane & 31) * 272 + ks * 32 + (lane >> 5) * 16);
    {
      auto put = [&](int r, cpx v) { *(u32*)(scr + r * 272 + lane * 4) = pk_f16(v.r, -v.i); };
      const cpx E2 = csq(E1), E4 = csq(E2);
      cpx X = {CB.r * scA, CB.i * scA};
      cpx G = cmul(X, F);
      put(0, X);
#pragma unroll
      for (int r = 1; r < 8; ++r) { X = cmul(X, E1); put(r, X); }
      put(8, G); X = cmul(G, E2); put(9, X); X = cmul(X, E2); put(10, X); X = cmul(X, E2); put(11, X);
      G = cmul(G, F); put(12, G); put(13, cmul(G, E4));
      G = cmul(G, F); put(14, G); put(15, cmul(G, E4));
#pragma unroll
      for (int r = 16; r < 21; ++r) { G = cmul(G, F); put(r, G); }
    }
    f32x16 acc = {};
#pragma unroll
    for (int ks = 0; ks < 8; ++ks) {
      f16x8 af = *(const f16x8*)(scr + (lane & 31) * 272 + ks * 32 + (lane >> 5) * 16);
      acc = __builtin_amdgcn_mfma_f32_32x32x16_f16(af, bf[ks], acc, 0, 0, 0);
    }
#pragma unroll
    for (int r = 0; r < 12; ++r) {
      int row = (r & 3) + 8 * (r >> 2) + 4 * (lane >> 5);
      *(float*)(scr + row * 128 + (lane & 31) * 4) = acc[r];
    }
  }
  STAMP(0, 2);
  __syncthreads();
  float kinv;
  {
    const float* sw = (const float*)(lds + A_SW);
    const float S = sw[8 + 4 * ch] + sw[8 + 4 * ch + 1] + sw[8 + 4 * ch + 2] + sw[8 + 4 * ch + 3];
    const float ks = pow2_for(S, 15);
    const float i0 = sw[4 * ch] * ks, i1 = sw[4 * ch + 1] * ks, i2 = sw[4 * ch + 2] * ks, i3 = sw[4 * ch + 3] * ks;
    const float S0 = sw[8] + sw[9] + sw[10] + sw[11], S1 = sw[12] + sw[13] + sw[14] + sw[15];
    kinv = 0.f; (void)S0; (void)S1;
#pragma unroll
    for (int it = 0; it < 3; ++it) {
      int e = (tid & 255) + 256 * it;
      if (e < 672) {
        const char* p = lds + A_SCR + (4 * ch) * A_SCRW + e * 4;
        float v = *(const float*)p * i0 + *(const float*)(p + A_SCRW) * i1 + *(const float*)(p + 2 * A_SCRW) * i2 + *(const float*)(p + 3 * A_SCRW) * i3;
        *(_Float16*)(lds + A_TB + ch * A_TBC + e * 2) = (_Float16)v;
      }
    }
  }
  float kinv0, kinv1;
  {
    const float* sw = (const float*)(lds + A_SW);
    kinv0 = pow2_inv(pow2_for(sw[8] + sw[9] + sw[10] + sw[11], 15));
    kinv1 = pow2_inv(pow2_for(sw[12] + sw[13] + sw[14] + sw[15], 15));
    (void)kinv;
  }
  __syncthreads();
  {
    const char* tb = lds + A_TB + ch * A_TBC;
    u32 P[8];
#pragma unroll
    for (int k = 0; k < 8; ++k) {
      u32 iw = k < 4 ? ix0[k] : ix1[k - 4];
      u32 lo = *(const u16*)(tb + (iw & 0xffffu));
      u32 hi = (k < 7) ? *(const u16*)(tb + (iw >> 16)) : 0u;
      P[k] = lo | (hi << 16);
    }
    u32 Q[7];
#pragma unroll
    for (int k = 0; k < 7; ++k) Q[k] = __builtin_amdgcn_alignbit(P[k + 1], P[k], 16);
    char* cp = lds + A_CP + ch * A_CPC + (tid & 255) * 16;
#pragma unroll
    for (int s = 0; s < 8; ++s) {
      const int off = 7 - s;
      u32x4 v;
      if ((off & 1) == 0) v = (u32x4){P[off / 2], P[off / 2 + 1], P[off / 2 + 2], P[off / 2 + 3]};
      else v = (u32x4){Q[off / 2], Q[off / 2 + 1], Q[off / 2 + 2], Q[off / 2 + 3]};
      *(u32x4*)(cp + s * A_CPS) = v;
    }
  }
  STAMP(0, 3);
  float uu[2][4], xv[2][4];
  {
    __amdgpu_buffer_rsrc_t rs = __builtin_amdgcn_make_buffer_rsrc((void*)(ws + WS_GRAN), 0, SL * 8, 0x00020000);
    u32x4 g0, g1;
    for (int spin = 0; spin < (1 << 16); ++spin) {
      g0 = __builtin_amdgcn_raw_buffer_load_b128(rs, tid * 32, 0, 16);
      g1 = __builtin_amdgcn_raw_buffer_load_b128(rs, tid * 32 + 16, 0, 16);
      bool ok = g0[1] == tag && g0[3] == tag && g1[1] == tag && g1[3] == tag;
      if (__all(ok)) break;
      __builtin_amdgcn_s_sleep(2);
    }
    const u32 st[4] = {g0[0], g0[2], g1[0], g1[2]};
#pragma unroll
    for (int i = 0; i < 4; ++i) {
      const float m = bf2f(st[i] & 0xffffu), sd = __uint_as_float(st[i] & 0xffff0000u);
      const float rs_ = __builtin_amdgcn_rcpf(sd);
      xv[0][i] = bf2f(xc[i] & 0xffffu); xv[1][i] = __uint_as_float(xc[i] & 0xffff0000u);
      uu[0][i] = rbf(rbf(xv[0][i] - m) * rs_); uu[1][i] = rbf(rbf(xv[1][i] - m) * rs_);
    }
#pragma unroll
    for (int c = 0; c < 2; ++c)
      *(u32x2*)(lds + A_UU + c * A_UUC + ((tid >> 3) + 31) * 80 + (tid & 7) * 8) = (u32x2){pk_f16(uu[c][0], uu[c][1]), pk_f16(uu[c][2], uu[c][3])};
  }
  STAMP(0, 4);
  __syncthreads();
  if (b == 0 && tid == 0) __hip_atomic_store(epoch_p, tag, RLX_AGENT);
  {
    const int cw = wv >> 2, w4 = wv & 3;
    const int q = lane & 31, h = lane >> 5, a = q >> 3, s = q & 7;
    const char* abase = lds + A_CP + cw * A_CPC + s * A_CPS + 4080 - 16 * a + 16 * h - 32 * (16 * w4 + 14) - 2048;
    const char* bbase = lds + A_UU + cw * A_UUC + (q + 31 - (8 * w4 + 7)) * 80 + 16 * h;
    f32x16 y0 = {}, y1 = {};
#pragma unroll
    for (int i = 0; i < 8; ++i) {
#pragma unroll
      for (int kk = 0; kk < 2; ++kk) {
        const int erel = 14 - (2 * i - kk);
        f16x8 alo = *(const f16x8*)(abase + 2048 + 32 * erel);
        f16x8 ahi = *(const f16x8*)(abase + 32 * erel);
        f16x8 b0 = *(const f16x8*)(bbase + (7 - i) * 80 + kk * 32);
        f16x8 b1 = *(const f16x8*)(bbase + (7 - i + 32) * 80 + kk * 32);
        y0 = __builtin_amdgcn_mfma_f32_32x32x16_f16(alo, b0, y0, 0, 0, 0);
        y1 = __builtin_amdgcn_mfma_f32_32x32x16_f16(alo, b1, y1, 0, 0, 0);
        y1 = __builtin_amdgcn_mfma_f32_32x32x16_f16(ahi, b0, y1, 0, 0, 0);
      }
    }
    char* pp = lds + A_SCR + wv * A_SCRW + q * 144 + h * 16;
#pragma unroll
    for (int g = 0; g < 4; ++g) {
      *(f32x4*)(pp + g * 32) = (f32x4){y0[4 * g], y0[4 * g + 1], y0[4 * g + 2], y0[4 * g + 3]};
      *(f32x4*)(pp + 32 * 144 + g * 32) = (f32x4){y1[4 * g], y1[4 * g + 1], y1[4 * g + 2], y1[4 * g + 3]};
    }
  }
  STAMP(0, 5);
  __syncthreads();
  {
    u16* hT = (u16*)(ws + WS_HT);
#pragma unroll
    for (int c = 0; c < 2; ++c) {
      const char* pp = lds + A_SCR + (4 * c) * A_SCRW + (tid >> 3) * 144 + (tid & 7) * 16;
      f32x4 y = *(const f32x4*)pp + *(const f32x4*)(pp + A_SCRW) + *(const f32x4*)(pp + 2 * A_SCRW) + *(const f32x4*)(pp + 3 * A_SCRW);
      const float ki = c ? kinv1 : kinv0, dk = c ? __uint_as_float(p_d01 & 0xffff0000u) : bf2f(p_d01 & 0xffffu);
      float hv[4];
#pragma unroll
      for (int i = 0; i < 4; ++i) hv[i] = xv[c][i] + rbf(rbf(y[i] * ki) + rbf(uu[c][i] * dk));
      *(u32x2*)(hT + (c0 + c) * SL + 4 * tid) = (u32x2){pk_bf16(hv[0], hv[1]), pk_bf16(hv[2], hv[3])};
    }
  }
  STAMP(0, 6);
}

constexpr int B_G = 0, B_U = 14464, B_H = 28928, B_STG = 45312, B_CS = 3 * B_STG, B_ST = B_CS + 224 * 4, B_LDS = B_ST + 4 * 32 * 8;

DI s16x8 tr_frag(const char* p0, const char* p1) {
  s16x4 a = __builtin_amdgcn_ds_read_tr16_b64_v4i16((LAS s16x4*)p0);
  s16x4 b = __builtin_amdgcn_ds_read_tr16_b64_v4i16((LAS s16x4*)p1);
  return __builtin_shufflevector(a, b, 0, 1, 2, 3, 4, 5, 6, 7);
}
DI void glds16(const void* g, u32 lds_dst) {
  u32 keep;
  asm volatile("s_mov_b32 %0, m0\n\ts_mov_b32 m0, %2\n\ts_nop 0\n\tglobal_load_lds_dwordx4 %1, off\n\ts_mov_b32 m0, %0"
               : "=&s"(keep) : "v"(g), "s"(lds_dst) : "memory");
}
DI u32 lds_addr(const void* p) { return (u32)(size_t)(LAS const char*)p; }

template <int NT, int T0, bool STATS, int NI>
DI void kb_body(char* lds, const u16* const (&src)[6], int wv, int lane, int m, int n, char* ws) {
  const int tt = wv & 3;
  const int grp = lane >> 4, q = (lane & 15) >> 2, p = lane & 3, h = grp >> 1;
  const int aoff = ((grp & 1) ? B_U : B_G) + (8 * h + q) * 224 + (16 * T0 + 4 * p) * 2;
  const int bchunk = 4 * tt + 2 * (grp & 1) + (p >> 1);
  const int boff = B_H + (8 * h + q) * 256 + ((bchunk ^ (q << 2)) * 16) + (p & 1) * 8;
  f32x16 acc[NT];
#pragma unroll
  for (int i = 0; i < NT; ++i) acc[i] = (f32x16){};
  float ssum = 0.f, ssq = 0.f;
  const u16* sp[6];
#pragma unroll
  for (int r = 0; r < 6; ++r) sp[r] = src[r];
  u32 dst[6]; int step[6];
#pragma unroll
  for (int r = 0; r < 6; ++r) {
    const int qi = wv + 8 * r;
    dst[r] = lds_addr(lds) + B_G + qi * 1024 + (qi >= 14 ? B_U - B_G - 14 * 1024 : 0) + (qi >= 28 ? B_H - B_U - 14 * 1024 : 0);
    step[r] = qi < 28 ? 64 * FF : 64 * SL;
  }
  auto issue = [&](int buf) {
#pragma unroll
    for (int r = 0; r < NI; ++r) { glds16(sp[r], dst[r] + buf * B_STG); sp[r] += step[r]; }
  };
  issue(0); issue(1);
#pragma unroll 1
  for (int s = 0; s < 7; ++s) {
    if (s < 6) asm volatile("s_waitcnt vmcnt(%0)" :: "n"(NI) : "memory");
    else asm volatile("s_waitcnt vmcnt(0)" ::: "memory");
    asm volatile("s_waitcnt lgkmcnt(0)" ::: "memory");
    __builtin_amdgcn_s_barrier();
    const int buf = s % 3;
    if (s + 2 < 7) issue((s + 2) % 3);
    const char* st = lds + buf * B_STG;
#pragma unroll
    for (int ks = 0; ks < 4; ++ks) {
      s16x8 bfr = tr_frag(st + boff + ks * 16 * 256, st + boff + (ks * 16 + 4) * 256);
      if (STATS) {
        u32x4 bw = __builtin_bit_cast(u32x4, bfr);
#pragma unroll
        for (int i = 0; i < 4; ++i) {
          const u32 wi = bw[i]; bf16x2 v = __builtin_bit_cast(bf16x2, wi);
          ssum = __builtin_amdgcn_fdot2_f32_bf16(v, __builtin_bit_cast(bf16x2, 0x3f803f80u), ssum, false);
          ssq = __builtin_amdgcn_fdot2_f32_bf16(v, v, ssq, false);
        }
      }
#pragma unroll
      for (int i = 0; i < NT; ++i) {
        s16x8 afr = tr_frag(st + aoff + ks * 16 * 224 + i * 32, st + aoff + (ks * 16 + 4) * 224 + i * 32);
        acc[i] = __builtin_amdgcn_mfma_f32_32x32x16_bf16(__builtin_bit_cast(bf16x8, afr), __builtin_bit_cast(bf16x8, bfr), acc[i], 0, 0, 0);
      }
    }
  }
  float* stat = (float*)(lds + B_ST);
  if (STATS) {
    ssum += __shfl_xor(ssum, 32); ssq += __shfl_xor(ssq, 32);
    const float mean = ssum * (1.f / DM);
    float var = ssq * (1.f / DM) - mean * mean; var = var < 0.f ? 0.f : var;
    const float sd = rbf(__builtin_sqrtf(rbf(rbf(var) + 1e-5f)));
    if (lane < 32) { stat[(tt * 32 + lane) * 2] = rbf(mean); stat[(tt * 32 + lane) * 2 + 1] = __builtin_amdgcn_rcpf(sd); }
  }
  __syncthreads();
  const float mb = stat[(tt * 32 + (lane & 31)) * 2], rs = stat[(tt * 32 + (lane & 31)) * 2 + 1];
  const float* cs = (const float*)(lds + B_CS);
  const int hh = lane >> 5;
  char* Hf = ws + WS_H;
#pragma unroll
  for (int i = 0; i < NT; ++i) {
    const int f0 = 16 * (T0 + i) + 4 * hh;
    const f32x4 g0 = *(const f32x4*)(cs + f0), g1 = *(const f32x4*)(cs + f0 + 8);
    const f32x4 u0 = *(const f32x4*)(cs + 112 + f0), u1 = *(const f32x4*)(cs + 112 + f0 + 8);
    float hv[8];
#pragma unroll
    for (int r = 0; r < 4; ++r) {
      hv[r] = ((acc[i][r] - mb * g0[r]) * rs) * ((acc[i][8 + r] - mb * u0[r]) * rs);
      hv[4 + r] = ((acc[i][4 + r] - mb * g1[r]) * rs) * ((acc[i][12 + r] - mb * u1[r]) * rs);
    }
    u32x4 o = {pk_bf16(hv[0], hv[1]), pk_bf16(hv[2], hv[3]), pk_bf16(hv[4], hv[5]), pk_bf16(hv[6], hv[7])};
    *(u32x4*)(Hf + ((size_t)((4 * m + tt) * 112 + 7 * n + T0 + i) * 64 + lane) * 16) = o;
  }
}

__global__ __launch_bounds__(512, 2) void kb_gemm1(const u16* __restrict__ wg, const u16* __restrict__ wu, char* ws) {
  extern __shared__ __attribute__((aligned(16))) char lds[];
  char* dbg = ws + WS_DBG; (void)dbg;
  const int tid = threadIdx.x, lane = tid & 63;
  const int wv = __builtin_amdgcn_readfirstlane(tid >> 6);
  const int b = blockIdx.x, xcd = b & 7, j = b >> 3;
  const int m = 2 * xcd + (j >> 4), n = j & 15;
  STAMP(1, 0);
  const u16* hT = (const u16*)(ws + WS_HT);
  const u16* src[6];
#pragma unroll
  for (int r = 0; r < 6; ++r) {
    const int qi = wv + 8 * r;
    if (qi < 28) {
      const int c = (qi < 14 ? qi : qi - 14) * 64 + lane, row = c / 14, col = c % 14;
      src[r] = (qi < 14 ? wg : wu) + row * FF + 112 * n + col * 8;
    } else {
      const int c = ((qi - 28) & 15) * 64 + lane, row = c >> 4, cp = c & 15;
      src[r] = hT + row * SL + 128 * m + (cp ^ ((row & 3) << 2)) * 8;
    }
  }
  if (tid < 224) ((float*)(lds + B_CS))[tid] = ((const float*)(ws + WS_CS))[(tid / 112) * FF + 112 * n + tid % 112];
  if (wv < 4) kb_body<4, 0, false, 6>(lds, src, wv, lane, m, n, ws);
  else kb_body<3, 4, true, 5>(lds, src, wv, lane, m, n, ws);
  STAMP(1, 1);
}

constexpr int C_STG = 16384, C_NB = 8, C_LDS = C_NB * C_STG;

__global__ __launch_bounds__(512, 2) void kc_gemm2(const u16* __restrict__ wd, char* ws, u16* __restrict__ out) {
  extern __shared__ __attribute__((aligned(16))) char lds[];
  char* dbg = ws + WS_DBG; (void)dbg;
  const int tid = threadIdx.x, lane = tid & 63;
  const int wv = __builtin_amdgcn_readfirstlane(tid >> 6);
  const int b = blockIdx.x, xcd = b & 7, j = b >> 3;
  if (j >= 28) return;
  STAMP(2, 0);
  const int mm = 4 * xcd + j / 7, nn = j % 7;
  const int dt = wv & 1, tt = (wv >> 1) & 1, kp = wv >> 2;
  const char* hs = ws + WS_H + ((size_t)((2 * mm + (wv >> 2)) * 112 + (wv & 3)) * 64 + lane) * 16;
  const u16* wsrc;
  { const int row = 8 * wv + (lane >> 3), cp = lane & 7; wsrc = wd + row * DM + 64 * nn + (cp ^ (((row >> 1) & 1) << 2)) * 8; }
  const u32 ldsb = lds_addr(lds) + wv * 1024;
  auto issue = [&](int buf) {
    glds16(hs, ldsb + buf * C_STG); hs += 4 * 1024;
    glds16(wsrc, ldsb + buf * C_STG + 8192); wsrc += 64 * DM;
  };
  const int hh = lane >> 5;
  const int tok = 64 * mm + 32 * tt + (lane & 31);
  const u16* hT = (const u16*)(ws + WS_HT);
  u32 res[8];
#pragma unroll
  for (int i = 0; i < 8; ++i) res[i] = hT[(64 * nn + 32 * dt + 16 * kp + 8 * (i >> 2) + 4 * hh + (i & 3)) * SL + tok];
#pragma unroll
  for (int s = 0; s < C_NB - 1; ++s) issue(s);
  const int grp = lane >> 4, q = (lane & 15) >> 2, p = lane & 3, h = grp >> 1;
  const int achunk = 4 * dt + 2 * (grp & 1) + (p >> 1);
  const int aoff = 8192 + (4 * h + q) * 128 + ((achunk ^ (((q >> 1) & 1) << 2)) * 16) + (p & 1) * 8;
  const int boff = tt * 4096 + lane * 16;
  f32x16 acc = {};
#pragma unroll 1
  for (int s = 0; s < 28; ++s) {
    if (s < 28 - (C_NB - 2)) asm volatile("s_waitcnt vmcnt(12)" ::: "memory");
    else asm volatile("s_waitcnt vmcnt(0)" ::: "memory");
    asm volatile("s_waitcnt lgkmcnt(0)" ::: "memory");
    __builtin_amdgcn_s_barrier();
    if (s + C_NB - 1 < 28) issue((s + C_NB - 1) % C_NB);
    const char* st = lds + (s % C_NB) * C_STG;
#pragma unroll
    for (int k2 = 0; k2 < 2; ++k2) {
      const int ks = 2 * kp + k2;
      s16x8 afr = tr_frag(st + aoff + ks * 16 * 128, st + aoff + (ks * 16 + 8) * 128);
      bf16x8 bfr = *(const bf16x8*)(st + boff + ks * 1024);
      acc = __builtin_amdgcn_mfma_f32_32x32x16_bf16(__builtin_bit_cast(bf16x8, afr), bfr, acc, 0, 0, 0);
    }
  }
  __syncthreads();
  {
    char* ex = lds + wv * 2048 + lane * 16;
    const int o = 8 * (kp ^ 1);
    f32x4 s0, s1;
    if (kp == 0) { s0 = (f32x4){acc[8], acc[9], acc[10], acc[11]}; s1 = (f32x4){acc[12], acc[13], acc[14], acc[15]}; }
    else { s0 = (f32x4){acc[0], acc[1], acc[2], acc[3]}; s1 = (f32x4){acc[4], acc[5], acc[6], acc[7]}; }
    (void)o;
    *(f32x4*)ex = s0; *(f32x4*)(ex + 1024) = s1;
    __syncthreads();
    const char* ey = lds + (wv ^ 4) * 2048 + lane * 16;
    f32x4 r0 = *(const f32x4*)ey, r1 = *(const f32x4*)(ey + 1024);
    f32x4 k0, k1;
    if (kp == 0) { k0 = (f32x4){acc[0], acc[1], acc[2], acc[3]}; k1 = (f32x4){acc[4], acc[5], acc[6], acc[7]}; }
    else { k0 = (f32x4){acc[8], acc[9], acc[10], acc[11]}; k1 = (f32x4){acc[12], acc[13], acc[14], acc[15]}; }
    k0 += r0; k1 += r1;
    u16* op = out + tok * DM + 64 * nn + 32 * dt + 16 * kp + 4 * hh;
    *(u32x2*)op = (u32x2){pk_bf16(bf2f(res[0]) + rbf(k0[0]), bf2f(res[1]) + rbf(k0[1])), pk_bf16(bf2f(res[2]) + rbf(k0[2]), bf2f(res[3]) + rbf(k0[3]))};
    *(u32x2*)(op + 8) = (u32x2){pk_bf16(bf2f(res[4]) + rbf(k1[0]), bf2f(res[5]) + rbf(k1[1])), pk_bf16(bf2f(res[6]) + rbf(k1[2]), bf2f(res[7]) + rbf(k1[3]))};
  }
  STAMP(2, 1);
}

extern "C" void kernel_launch(void* const* d_in, const int* in_sizes, int n_in, void* d_out, int out_size, void* d_ws, size_t ws_size,
                              hipStream_t stream) {
  (void)in_sizes; (void)n_in; (void)out_size; (void)ws_size;
  const u16* x = (const u16*)d_in[0];
  hipFuncSetAttribute((const void*)ka_conv, hipFuncAttributeMaxDynamicSharedMemorySize, A_LDS);
  hipFuncSetAttribute((const void*)kb_gemm1, hipFuncAttributeMaxDynamicSharedMemorySize, B_LDS);
  hipFuncSetAttribute((const void*)kc_gemm2, hipFuncAttributeMaxDynamicSharedMemorySize, C_LDS);
  ka_conv<<<256, 512, A_LDS, stream>>>(x, (const u16*)d_in[1], (const u16*)d_in[2], (const u16*)d_in[3], (const u16*)d_in[4],
                                       (const u16*)d_in[5], (const u16*)d_in[6], (const u16*)d_in[7], (const u16*)d_in[8],
                                       (const u16*)d_in[10], (const u16*)d_in[11], (char*)d_ws);
  kb_gemm1<<<256, 512, B_LDS, stream>>>((const u16*)d_in[10], (const u16*)d_in[11], (char*)d_ws);
  kc_gemm2<<<256, 512, C_LDS, stream>>>((const u16*)d_in[12], (char*)d_ws, (u16*)d_out);
}
```

```cpp
#include <hip/hip_runtime.h>
#include <stdint.h>

typedef unsigned short u16;
typedef unsigned int u32;
typedef unsigned long long u64;
typedef float f32x2 __attribute__((ext_vector_type(2)));
typedef float f32x4 __attribute__((ext_vector_type(4)));
typedef float f32x16 __attribute__((ext_vector_type(16)));
typedef unsigned u32x2 __attribute__((ext_vector_type(2)));
typedef unsigned u32x4 __attribute__((ext_vector_type(4)));
typedef short s16x4 __attribute__((ext_vector_type(4)));
typedef short s16x8 __attribute__((ext_vector_type(8)));
typedef __bf16 bf16x8 __attribute__((ext_vector_type(8)));
typedef __bf16 bf16x2 __attribute__((ext_vector_type(2)));
typedef _Float16 f16x8 __attribute__((ext_vector_type(8)));
typedef _Float16 f16x4 __attribute__((ext_vector_type(4)));
typedef _Float16 f16x2 __attribute__((ext_vector_type(2)));
#define DI __device__ __forceinline__
#define LAS __attribute__((address_space(3)))
#define GAS __attribute__((address_space(1)))
typedef GAS unsigned gu32;
typedef GAS unsigned long long gu64;
#define RLX_AGENT __ATOMIC_RELAXED, __HIP_MEMORY_SCOPE_AGENT
#define FENCE() __builtin_amdgcn_sched_barrier(0)

constexpr int SL = 2048, DM = 448, NS = 256, FF = 1792;
constexpr int HS = SL + 64;

constexpr size_t WS_EPOCH = 0;
constexpr size_t WS_GRAN  = 4096;
constexpr size_t WS_CS    = 32768;
constexpr size_t WS_XG    = 65536;
constexpr size_t WS_HT    = WS_XG + (size_t)DM * 8192;
constexpr size_t WS_H     = WS_HT + (size_t)DM * HS * 2;
constexpr size_t WS_DBG   = WS_H + (size_t)SL * FF * 2;

#define STAMP_DECL do {} while (0)
#define STAMP(k, i) do {} while (0)
#define STAMP_FLUSH(k) do {} while (0)

DI float bf2f(u32 v) { return __uint_as_float(v << 16); }
DI float bfhi(u32 v) { return __uint_as_float(v & 0xffff0000u); }
DI u32 pk_bf16(float a, float b) { f32x2 v = {a, b}; return __builtin_bit_cast(u32, __builtin_convertvector(v, bf16x2)); }
DI u32 pk_f16(float a, float b) { f32x2 v = {a, b}; return __builtin_bit_cast(u32, __builtin_convertvector(v, f16x2)); }
DI u32 pk2(f32x2 v) { return __builtin_bit_cast(u32, __builtin_convertvector(v, f16x2)); }
DI float rbf(float a) { return __uint_as_float(pk_bf16(a, a) << 16); }

template <int CTRL, int RM> DI float dpp0(float v) { return __int_as_float(__builtin_amdgcn_update_dpp(0, __float_as_int(v), CTRL, RM, 0xf, true)); }
DI float lane63(float v) { return __int_as_float(__builtin_amdgcn_readlane(__float_as_int(v), 63)); }
DI void wave_sum2(float& a, float& b) {
  a += dpp0<0x111, 0xf>(a); b += dpp0<0x111, 0xf>(b); a += dpp0<0x112, 0xf>(a); b += dpp0<0x112, 0xf>(b);
  a += dpp0<0x114, 0xf>(a); b += dpp0<0x114, 0xf>(b); a += dpp0<0x118, 0xf>(a); b += dpp0<0x118, 0xf>(b);
  a += dpp0<0x142, 0xa>(a); b += dpp0<0x142, 0xa>(b); a += dpp0<0x143, 0xc>(a); b += dpp0<0x143, 0xc>(b);
  a = lane63(a); b = lane63(b);
}
DI void wave_max_sum(float& m, float& s) {
  m = fmaxf(m, dpp0<0x111, 0xf>(m)); s += dpp0<0x111, 0xf>(s); m = fmaxf(m, dpp0<0x112, 0xf>(m)); s += dpp0<0x112, 0xf>(s);
  m = fmaxf(m, dpp0<0x114, 0xf>(m)); s += dpp0<0x114, 0xf>(s); m = fmaxf(m, dpp0<0x118, 0xf>(m)); s += dpp0<0x118, 0xf>(s);
  m = fmaxf(m, dpp0<0x142, 0xa>(m)); s += dpp0<0x142, 0xa>(s); m = fmaxf(m, dpp0<0x143, 0xc>(m)); s += dpp0<0x143, 0xc>(s);
  m = lane63(m); s = lane63(s);
}

DI void glds16(const void* g, u32 lds_dst) {
  u32 keep;
  asm volatile("s_mov_b32 %0, m0\n\ts_mov_b32 m0, %2\n\ts_nop 0\n\tglobal_load_lds_dwordx4 %1, off\n\ts_mov_b32 m0, %0"
               : "=&s"(keep) : "v"(g), "s"(lds_dst) : "memory");
}
typedef __amdgpu_buffer_rsrc_t rsrc_t;
DI rsrc_t mkrsrc(const void* p) { return __builtin_amdgcn_make_buffer_rsrc((void*)p, 0, 0x7fffffff, 0x00020000); }
DI void bdma16(rsrc_t rs, u32 voff, u32 soff, u32 lds_dst) {
  asm volatile("s_mov_b32 m0, %0\n\ts_nop 0\n\tbuffer_load_dwordx4 %1, %2, %3 offen lds" :: "s"(lds_dst), "v"(voff), "s"(rs), "s"(soff) : "memory");
}
DI void bdma4(rsrc_t rs, u32 voff, u32 soff, u32 lds_dst) {
  asm volatile("s_mov_b32 m0, %0\n\ts_nop 0\n\tbuffer_load_dword %1, %2, %3 offen lds" :: "s"(lds_dst), "v"(voff), "s"(rs), "s"(soff) : "memory");
}
DI u32 lds_addr(const void* p) { return (u32)(size_t)(LAS const char*)p; }
DI s16x8 tr_frag(const char* p0, const char* p1) {
  s16x4 a = __builtin_amdgcn_ds_read_tr16_b64_v4i16((LAS s16x4*)p0);
  s16x4 b = __builtin_amdgcn_ds_read_tr16_b64_v4i16((LAS s16x4*)p1);
  return __builtin_shufflevector(a, b, 0, 1, 2, 3, 4, 5, 6, 7);
}

struct IdxTab { u16 v[2064]; };
constexpr int tap_entry(int l) {
  if (l < 0) return 21 * 32;
  int lam = l;
  if (l >= 256) {
    int sh = l >= 1024 ? 3 : (l >= 512 ? 2 : 1);
    int q = l >> sh, rem = l & ((1 << sh) - 1), half = 1 << (sh - 1);
    if (rem > half || (rem == half && (q & 1))) q += 1;
    lam = q << sh;
  }
  int hq = lam >> 8, lo = lam & 255, c = lo >> 3, r = 0;
  if (hq == 0) r = lo & 7;
  else if (hq == 1) r = 8 + ((lo & 7) >> 1);
  else if (hq == 2) r = 12 + ((lo & 7) >> 2);
  else if (hq == 3) r = 14 + ((lo & 7) >> 2);
  else if (hq < 8) r = 16 + hq - 4;
  else { r = 20; c = 0; }
  return r * 32 + c;
}
constexpr IdxTab make_tab() { IdxTab t{}; for (int y = 0; y < 2064; ++y) t.v[y] = (u16)(2 * tap_entry(2047 - y)); return t; }
__device__ const IdxTab RIDX __attribute__((aligned(16))) = make_tab();

DI float pow2_for(float mx, int target) {
  int e = (int)(__float_as_uint(mx) >> 23);
  int be = 127 + target - (e - 126);
  be = be > 254 ? 254 : (be < 1 ? 1 : be);
  return __uint_as_float((u32)be << 23);
}
DI float pow2_inv(float p) { return __uint_as_float((254u - (__float_as_uint(p) >> 23)) << 23); }

struct CM { f32x2 a, b; };
DI CM mk(f32x2 e) { return {e, (f32x2){-e.y, e.x}}; }
DI f32x2 cmul(f32x2 p, CM m) {
  f32x2 t = __builtin_shufflevector(p, p, 0, 0) * m.a;
  return __builtin_elementwise_fma(__builtin_shufflevector(p, p, 1, 1), m.b, t);
}
DI f32x2 cexp_s(float zr, float zi, float s) {
  float m = __builtin_amdgcn_exp2f(zr * (s * 1.44269504088896341f));
  float a = __builtin_amdgcn_fractf(zi * (s * 0.15915494309189535f));
  return (f32x2){m * __builtin_amdgcn_cosf(a), m * __builtin_amdgcn_sinf(a)};
}
template <int OFF>
DI void put4(u32 base, u32 v0, u32 v1, u32 v2, u32 v3) {
  asm volatile("s_mov_b32 m0, %0\n\ts_nop 0\n\tds_write_addtid_b32 %1 offset:%5\n\tds_write_addtid_b32 %2 offset:%6\n\t"
               "ds_write_addtid_b32 %3 offset:%7\n\tds_write_addtid_b32 %4 offset:%8"
               :: "s"(base), "v"(v0), "v"(v1), "v"(v2), "v"(v3), "n"(OFF), "n"(OFF + 272), "n"(OFF + 544), "n"(OFF + 816) : "memory");
}

constexpr int A_T1 = 0, A_T2 = 8192;
constexpr int A_SCR = 25088, A_SCRW = 32 * 272 + 21 * 272;
constexpr int A_CP = 0, A_CPS = 4160, A_CPC = 8 * A_CPS;
constexpr int A_EX = 2 * A_CPC, A_XX = A_EX + 16384, A_HO = A_XX + 10240, A_DUM = A_HO + 10240;
constexpr int A_UU = A_SCR + 8 * A_SCRW, A_UUC = 95 * 80;
constexpr int A_TB = A_UU + 2 * A_UUC, A_TBC = 22 * 32 * 2;
constexpr int A_SW = A_TB + 2 * A_TBC;
constexpr int A_LDS = A_SW + 64;
static_assert(A_LDS <= 163840 && A_DUM + 4096 <= A_UU && (A_SCR % 16) == 0 && (A_SCRW % 16) == 0 && (A_UU % 16) == 0, "lds");

__global__ __launch_bounds__(512, 2) void ka_conv(const u16* __restrict__ x, const u16* __restrict__ Are, const u16* __restrict__ Aim,
    const u16* __restrict__ Bre, const u16* __restrict__ Bim, const u16* __restrict__ Cre, const u16* __restrict__ Cim,
    const u16* __restrict__ logdt, const u16* __restrict__ Dskip, const u16* __restrict__ wg, const u16* __restrict__ wu,
    const u16* __restrict__ wd, char* ws) {
  extern __shared__ __attribute__((aligned(16))) char lds[];
  char* dbg = ws + WS_DBG; (void)dbg;
  STAMP_DECL;
  const int tid = threadIdx.x, lane = tid & 63;
  const int wv = __builtin_amdgcn_readfirstlane(tid >> 6);
  const int b = blockIdx.x, xcd = b & 7, j = b >> 3;
  const bool worker = j < 28;
  STAMP(0, 0);
  gu32* epoch_p = (gu32*)(ws + WS_EPOCH);
  gu64* gran = (gu64*)(ws + WS_GRAN);
  const u32 tag = __hip_atomic_load(epoch_p, RLX_AGENT) + 1u;

  const int w = worker ? xcd * 28 + j : 0, c0 = 2 * w;
  const int trow = 8 * b + wv;
  const u32x4 xr = *(const u32x4*)(x + trow * DM + (lane < 56 ? lane : 0) * 8);
  const u32x4 xt = *(const u32x4*)(x + (128 * (w & 15) + (tid >> 2)) * DM + 32 * (w >> 4) + (tid & 3) * 8);
  const int ch = tid >> 8, n = tid & 255;
  const int idx = (c0 + ch) * NS + n;
  const u32 p_ldt = logdt[c0 + ch];
  const u32 p_are = Are[idx], p_aim = Aim[idx], p_bre = Bre[idx], p_bim = Bim[idx], p_cre = Cre[idx], p_cim = Cim[idx];
  const u32 p_d01 = *(const u32*)(Dskip + c0);
  const u32x4 ix0 = *(const u32x4*)(RIDX.v + 8 * (tid & 255)), ix1 = *(const u32x4*)(RIDX.v + 8 * (tid & 255) + 8);
  {
    const float piv = __uint_as_float((u32)__builtin_amdgcn_readfirstlane(xr[0]) << 16);
    float s = 0.f, q = 0.f, s2 = 0.f, q2 = 0.f;
#pragma unroll
    for (int i = 0; i < 4; ++i) {
      const float lo = bf2f(xr[i] & 0xffffu) - piv, hi = bfhi(xr[i]) - piv;
      s += lo; s2 += hi; q = fmaf(lo, lo, q); q2 = fmaf(hi, hi, q2);
    }
    s = lane < 56 ? s + s2 : 0.f; q = lane < 56 ? q + q2 : 0.f;
    wave_sum2(s, q);
    const float ms = s * (1.f / DM);
    float var = q * (1.f / DM) - ms * ms; var = var < 0.f ? 0.f : var;
    const float sd = rbf(__builtin_sqrtf(rbf(rbf(var) + 1e-5f)));
    if (lane == 0) __hip_atomic_store(gran + trow, ((u64)tag << 32) | pk_bf16(piv + ms, sd), RLX_AGENT);
  }
  STAMP(0, 1);
  if (!worker) {
    const int s = xcd * 4 + (j - 28);
    const u16* W = (s >> 4) ? wu : wg;
    const int colb = (s & 15) * 112;
    float a[8] = {0, 0, 0, 0, 0, 0, 0, 0};
    const int cc = tid % 14, rg = tid / 14;
    u32 warm = 0;
    {
      u32x4 wv4[7];
#pragma unroll
      for (int i = 0; i < 7; ++i) { const int c = tid + 512 * i; wv4[i] = *(const u32x4*)(wd + (size_t)s * 25088 + (c < 3136 ? c : 0) * 8); }
#pragma unroll
      for (int i = 0; i < 7; ++i) warm |= wv4[i][0] & wv4[i][1] & wv4[i][2] & wv4[i][3];
      asm volatile("" :: "v"(warm));
    }
    if (tid < 448) {
      u32x4 v[14];
#pragma unroll
      for (int r = 0; r < 14; ++r) v[r] = *(const u32x4*)(W + (rg * 14 + r) * FF + colb + cc * 8);
#pragma unroll
      for (int r = 0; r < 14; ++r)
#pragma unroll
        for (int i = 0; i < 4; ++i) { a[2 * i] += bf2f(v[r][i] & 0xffffu); a[2 * i + 1] += bfhi(v[r][i]); }
      float* red = (float*)lds;
#pragma unroll
      for (int i = 0; i < 8; ++i) red[rg * 112 + cc * 8 + i] = a[i];
    }
    __syncthreads();
    if (tid < 112) {
      float t = 0.f;
      for (int r = 0; r < 32; ++r) t += ((float*)lds)[r * 112 + tid];
      ((float*)(ws + WS_CS))[(s >> 4) * FF + colb + tid] = t;
    }
    STAMP_FLUSH(0);
    return;
  }
  *(u32x4*)(lds + A_T1 + tid * 16) = xt;
  const float dt = rbf(__builtin_amdgcn_exp2f(bf2f(p_ldt) * 1.44269504088896341f));
  const float zr = dt * bf2f(p_are), zi = dt * bf2f(p_aim);
  const f32x2 E8 = cexp_s(zr, zi, 8.f);
  const f32x2 E1 = cexp_s(zr, -zi, 1.f), F = cexp_s(zr, -zi, 256.f);
  __syncthreads();
  {
    const int grp = lane >> 4, q = (lane & 15) >> 2, p = lane & 3;
#pragma unroll
    for (int i = 0; i < 2; ++i) {
      const int tb = 16 * wv + 8 * i + 4 * (grp >> 1);
      const s16x4 v = __builtin_amdgcn_ds_read_tr16_b64_v4i16((LAS s16x4*)(lds + A_T1 + (tb + q) * 64 + (grp & 1) * 32 + p * 8));
      const u32x2 vw = __builtin_bit_cast(u32x2, v);
      *(u32x4*)(lds + A_T2 + (16 * (grp & 1) + (lane & 15)) * 528 + tb * 4) = (u32x4){vw[0], tag, vw[1], tag};
    }
  }
  const u32 scb = lds_addr(lds) + A_SCR + wv * A_SCRW;
  const CM M8 = mk(E8);
  f32x2 p0 = {16384.f, 0.f}, p1 = cmul(p0, M8), p2 = cmul(p1, M8), p3 = cmul(p2, M8);
  const CM M32 = mk(cmul(p2, mk(p2 * (1.f / 16384.f))) * (1.f / 16384.f));
  __syncthreads();
  {
    __amdgpu_buffer_rsrc_t rs = __builtin_amdgcn_make_buffer_rsrc((void*)(ws + WS_XG), 0, DM * 8192, 0x00020000);
#pragma unroll
    for (int k = 0; k < 2; ++k) {
      const int id = tid + 512 * k, c = id >> 5, pos = id & 31;
      const u32x4 v = *(const u32x4*)(lds + A_T2 + c * 528 + pos * 16);
      __builtin_amdgcn_raw_buffer_store_b128(v, rs, (32 * (w >> 4) + c) * 8192 + (w & 15) * 512 + pos * 16, 0, 16);
    }
  }
  STAMP(0, 2);
  {
    const float br = dt * bf2f(p_bre), bi = dt * bf2f(p_bim);
    const float cr = bf2f(p_cre), ci = bf2f(p_cim);
    const float CBr = cr * br - ci * bi, CBi = cr * bi + ci * br;
    float mx = fmaxf(fabsf(CBr), fabsf(CBi)), sabs = fabsf(CBr) + fabsf(CBi);
    wave_max_sum(mx, sabs);
    const float scA = pow2_for(mx, 14);
    if (lane == 0) { ((float*)(lds + A_SW))[wv] = pow2_inv(scA) * (1.f / 16384.f); ((float*)(lds + A_SW))[8 + wv] = sabs; }
#define COL4(R) { put4<(R) * 272>(scb, pk2(p0), pk2(p1), pk2(p2), pk2(p3)); \
                  if ((R) < 28) { p0 = cmul(p0, M32); p1 = cmul(p1, M32); p2 = cmul(p2, M32); p3 = cmul(p3, M32); } }
    COL4(0) COL4(4) COL4(8) COL4(12) COL4(16) COL4(20) COL4(24) COL4(28)
#undef COL4
    const CM M1 = mk(E1), MF = mk(F);
    const f32x2 E2 = cmul(E1, M1);
    const CM M2 = mk(E2), M4 = mk(cmul(E2, M2));
    const f32x2 X0 = {CBr * scA, -CBi * scA};
    const f32x2 X1 = cmul(X0, M1), G1 = cmul(X0, MF);
    const f32x2 X2 = cmul(X0, M2), X3 = cmul(X1, M2), G2 = cmul(G1, MF), Y1 = cmul(G1, M2);
    put4<(32 + 0) * 272>(scb, pk2(X0), pk2(X1), pk2(X2), pk2(X3));
    const f32x2 X4 = cmul(X0, M4), X5 = cmul(X1, M4), X6 = cmul(X2, M4), X7 = cmul(X3, M4);
    const f32x2 G3 = cmul(G2, MF), Y2 = cmul(G1, M4), Y3 = cmul(Y1, M4);
    put4<(32 + 4) * 272>(scb, pk2(X4), pk2(X5), pk2(X6), pk2(X7));
    put4<(32 + 8) * 272>(scb, pk2(G1), pk2(Y1), pk2(Y2), pk2(Y3));
    const f32x2 G4 = cmul(G3, MF), Z2 = cmul(G2, M4), Z3 = cmul(G3, M4);
    put4<(32 + 12) * 272>(scb, pk2(G2), pk2(Z2), pk2(G3), pk2(Z3));
    const CM MF2 = mk(cmul(F, MF));
    const f32x2 G5 = cmul(G3, MF2), G6 = cmul(G4, MF2);
    const f32x2 G7 = cmul(G5, MF2), G8 = cmul(G6, MF2);
    put4<(32 + 16) * 272>(scb, pk2(G4), pk2(G5), pk2(G6), pk2(G7));
    {
      const u32 g8 = pk2(G8);
      asm volatile("s_mov_b32 m0, %0\n\ts_nop 0\n\tds_write_addtid_b32 %1 offset:%2" :: "s"(scb), "v"(g8), "n"((32 + 20) * 272) : "memory");
    }
    char* scr = lds + A_SCR + wv * A_SCRW;
    const char* fr = scr + (lane & 31) * 272 + (lane >> 5) * 16;
    f16x8 bf[8], af[8];
#pragma unroll
    for (int ks = 0; ks < 8; ++ks) { bf[ks] = *(const f16x8*)(fr + ks * 32); af[ks] = *(const f16x8*)(fr + 32 * 272 + ks * 32); }
    f32x16 acc0 = {}, acc1 = {};
#pragma unroll
    for (int ks = 0; ks < 8; ks += 2) {
      acc0 = __builtin_amdgcn_mfma_f32_32x32x16_f16(af[ks], bf[ks], acc0, 0, 0, 0);
      acc1 = __builtin_amdgcn_mfma_f32_32x32x16_f16(af[ks + 1], bf[ks + 1], acc1, 0, 0, 0);
    }
#pragma unroll
    for (int r = 0; r < 12; ++r) {
      int row = (r & 3) + 8 * (r >> 2) + 4 * (lane >> 5);
      *(float*)(scr + row * 128 + (lane & 31) * 4) = acc0[r] + acc1[r];
    }
  }
  STAMP(0, 3);
  __syncthreads();
  float kinv0, kinv1;
  {
    const float* sw = (const float*)(lds + A_SW);
    const float k0 = pow2_for(sw[8] + sw[9] + sw[10] + sw[11], 15), k1 = pow2_for(sw[12] + sw[13] + sw[14] + sw[15], 15);
    kinv0 = pow2_inv(k0); kinv1 = pow2_inv(k1);
    const float ks = ch ? k1 : k0;
    const float i0 = sw[4 * ch] * ks, i1 = sw[4 * ch + 1] * ks, i2 = sw[4 * ch + 2] * ks, i3 = sw[4 * ch + 3] * ks;
    const char* p = lds + A_SCR + (4 * ch) * A_SCRW + (tid & 255) * 4;
    float v[3][4];
#pragma unroll
    for (int it = 0; it < 3; ++it)
#pragma unroll
      for (int k = 0; k < 4; ++k) v[it][k] = *(const float*)(p + it * 1024 + k * A_SCRW);
#pragma unroll
    for (int it = 0; it < 3; ++it) {
      const int e = (tid & 255) + 256 * it;
      const float t = v[it][0] * i0 + v[it][1] * i1 + v[it][2] * i2 + v[it][3] * i3;
      if (it < 2 || e < 672) *(_Float16*)(lds + A_TB + ch * A_TBC + e * 2) = (_Float16)t;
    }
    if (tid < 8) { int cc = tid >> 2; *(u32x4*)(lds + A_TB + cc * A_TBC + 21 * 64 + (tid & 3) * 16) = (u32x4){0, 0, 0, 0}; }
    if (tid < 310) { int cc = tid / 155, r = tid % 155; *(u32x4*)(lds + A_UU + cc * A_UUC + r * 16) = (u32x4){0, 0, 0, 0}; }
  }
  __syncthreads();
  {
    const char* tb = lds + A_TB + ch * A_TBC;
    u32 P[8];
#pragma unroll
    for (int k = 0; k < 8; ++k) {
      u32 iw = k < 4 ? ix0[k] : ix1[k - 4];
      u32 lo = *(const u16*)(tb + (iw & 0xffffu));
      u32 hi = (k < 7) ? *(const u16*)(tb + (iw >> 16)) : 0u;
      P[k] = lo | (hi << 16);
    }
    u32 Q[7];
#pragma unroll
    for (int k = 0; k < 7; ++k) Q[k] = __builtin_amdgcn_alignbit(P[k + 1], P[k], 16);
    char* cp = lds + A_CP + ch * A_CPC + (tid & 255) * 16;
#pragma unroll
    for (int s = 0; s < 8; ++s) {
      const int off = 7 - s;
      u32x4 v;
      if ((off & 1) == 0) v = (u32x4){P[off / 2], P[off / 2 + 1], P[off / 2 + 2], P[off / 2 + 3]};
      else v = (u32x4){Q[off / 2], Q[off / 2 + 1], Q[off / 2 + 2], Q[off / 2 + 3]};
      *(u32x4*)(cp + s * A_CPS) = v;
    }
    if (tid < 48) { int cc = tid / 24, r = tid % 24; *(u32x4*)(lds + A_CP + cc * A_CPC + (r / 3) * A_CPS + (256 + r % 3) * 16) = (u32x4){0, 0, 0, 0}; }
  }
  STAMP(0, 4);
  if (wv < 4) {
    __amdgpu_buffer_rsrc_t rs = __builtin_amdgcn_make_buffer_rsrc((void*)(ws + WS_GRAN), 0, SL * 8, 0x00020000);
    __amdgpu_buffer_rsrc_t rx = __builtin_amdgcn_make_buffer_rsrc((void*)(ws + WS_XG + (size_t)c0 * 8192), 0, 2 * 8192, 0x00020000);
    u32x4 g[4], xg[4];
    for (int spin = 0; spin < (1 << 16); ++spin) {
#pragma unroll
      for (int i = 0; i < 4; ++i) g[i] = __builtin_amdgcn_raw_buffer_load_b128(rs, tid * 64 + i * 16, 0, 16);
#pragma unroll
      for (int i = 0; i < 4; ++i) xg[i] = __builtin_amdgcn_raw_buffer_load_b128(rx, (i >> 1) * 8192 + tid * 32 + (i & 1) * 16, 0, 16);
      u32 bad = 0;
#pragma unroll
      for (int i = 0; i < 4; ++i) bad |= (g[i][1] ^ tag) | (g[i][3] ^ tag) | (xg[i][1] ^ tag) | (xg[i][3] ^ tag);
      if (__all(bad == 0)) break;
      __builtin_amdgcn_s_sleep(2);
    }
    float uu[2][8];
#pragma unroll
    for (int i = 0; i < 8; ++i) {
      const u32 st = g[i >> 1][(i & 1) * 2];
      const float m = bf2f(st & 0xffffu), rs_ = __builtin_amdgcn_rcpf(bfhi(st));
#pragma unroll
      for (int c = 0; c < 2; ++c) {
        const u32 xw = xg[2 * c + (i >> 2)][(i & 2)];
        const float xv = (i & 1) ? bfhi(xw) : bf2f(xw & 0xffffu);
        uu[c][i] = rbf(rbf(xv - m) * rs_);
      }
    }
#pragma unroll
    for (int c = 0; c < 2; ++c) {
      *(u32x4*)(lds + A_UU + c * A_UUC + ((tid >> 2) + 31) * 80 + (tid & 3) * 16) =
          (u32x4){pk_f16(uu[c][0], uu[c][1]), pk_f16(uu[c][2], uu[c][3]), pk_f16(uu[c][4], uu[c][5]), pk_f16(uu[c][6], uu[c][7])};
      *(u32x4*)(lds + A_XX + c * 5120 + (tid >> 2) * 80 + (tid & 3) * 16) = (u32x4){xg[2 * c][0], xg[2 * c][2], xg[2 * c + 1][0], xg[2 * c + 1][2]};
    }
  }
  STAMP(0, 5);
  __syncthreads();
  if (b == 0 && tid == 0) __hip_atomic_store(epoch_p, tag, RLX_AGENT);
  const int cw = (wv >> 1) & 1, hf = wv & 1;
  const int q = lane & 31, h = lane >> 5;
  f32x16 keep;
  if (wv < 4) {
    const int a = q >> 3, s = q & 7;
    const char* abase = lds + A_CP + cw * A_CPC + s * A_CPS + 4080 - 16 * a + 16 * h - 32 * (32 * hf + 30) - 2048;
    const char* bbase = lds + A_UU + cw * A_UUC + (q + 31 - (16 * hf + 15)) * 80 + 16 * h;
    f32x16 y0 = {}, y1 = {};
    f16x8 fa[2][8];
    auto ld = [&](int i, f16x8 (&f)[8]) {
#pragma unroll
      for (int kk = 0; kk < 2; ++kk) {
        const int erel = 30 - (2 * i - kk);
        f[4 * kk] = *(const f16x8*)(abase + 2048 + 32 * erel);
        f[4 * kk + 1] = *(const f16x8*)(bbase + (15 - i) * 80 + kk * 32);
        f[4 * kk + 2] = *(const f16x8*)(bbase + (15 - i + 32) * 80 + kk * 32);
        f[4 * kk + 3] = *(const f16x8*)(abase + 32 * erel);
      }
    };
    ld(0, fa[0]);
#pragma unroll
    for (int i = 0; i < 16; ++i) {
      const f16x8 (&f)[8] = fa[i & 1];
      f16x8 (&g)[8] = fa[(i + 1) & 1];
#pragma unroll
      for (int kk = 0; kk < 2; ++kk) {
        const int erel = 30 - (2 * (i + 1) - kk);
        y0 = __builtin_amdgcn_mfma_f32_32x32x16_f16(f[4 * kk], f[4 * kk + 1], y0, 0, 0, 0);
        FENCE();
        y1 = __builtin_amdgcn_mfma_f32_32x32x16_f16(f[4 * kk], f[4 * kk + 2], y1, 0, 0, 0);
        if (i + 1 < 16) { g[4 * kk] = *(const f16x8*)(abase + 2048 + 32 * erel); g[4 * kk + 2] = *(const f16x8*)(bbase + (15 - i - 1 + 32) * 80 + kk * 32); }
        FENCE();
        y1 = __builtin_amdgcn_mfma_f32_32x32x16_f16(f[4 * kk + 3], f[4 * kk + 1], y1, 0, 0, 0);
        if (i + 1 < 16) { g[4 * kk + 1] = *(const f16x8*)(bbase + (15 - i - 1) * 80 + kk * 32); g[4 * kk + 3] = *(const f16x8*)(abase + 32 * erel); }
        FENCE();
      }
    }
    const f32x16 give = hf ? y0 : y1;
    keep = hf ? y1 : y0;
    char* pp = lds + A_EX + wv * 4096 + lane * 16;
#pragma unroll
    for (int g = 0; g < 4; ++g) *(f32x4*)(pp + g * 1024) = (f32x4){give[4 * g], give[4 * g + 1], give[4 * g + 2], give[4 * g + 3]};
  }
  STAMP(0, 6);
  __syncthreads();
  if (wv < 4) {
    const char* pp = lds + A_EX + (wv ^ 1) * 4096 + lane * 16;
    const float ki = cw ? kinv1 : kinv0, dk = cw ? bfhi(p_d01) : bf2f(p_d01 & 0xffffu);
    const int row = 32 * hf + q;
#pragma unroll
    for (int g = 0; g < 4; ++g) {
      const f32x4 o = *(const f32x4*)(pp + g * 1024);
      const f16x4 u4 = *(const f16x4*)(lds + A_UU + cw * A_UUC + (row + 31) * 80 + g * 16 + h * 8);
      const u32x2 x2 = *(const u32x2*)(lds + A_XX + cw * 5120 + row * 80 + g * 16 + h * 8);
      float hv[4];
#pragma unroll
      for (int i = 0; i < 4; ++i) {
        const u32 xw = x2[i >> 1];
        const float xv = (i & 1) ? bfhi(xw) : bf2f(xw & 0xffffu);
        hv[i] = xv + rbf(rbf((keep[4 * g + i] + o[i]) * ki) + rbf((float)u4[i] * dk));
      }
      *(u32x2*)(lds + A_HO + cw * 5120 + row * 80 + g * 16 + h * 8) = (u32x2){pk_bf16(hv[0], hv[1]), pk_bf16(hv[2], hv[3])};
    }
  }
  __syncthreads();
  {
    const int c = tid >> 8, t8 = tid & 255;
    const u32x4 v = *(const u32x4*)(lds + A_HO + c * 5120 + (t8 >> 2) * 80 + (t8 & 3) * 16);
    *(u32x4*)((u16*)(ws + WS_HT) + (c0 + c) * HS + 8 * t8) = v;
  }
  STAMP(0, 7);
  STAMP_FLUSH(0);
}

constexpr int B_G = 0, B_U = 8192, B_H = 16384, B_STG = 24576, B_NB = 6, B_NST = 14;
constexpr int B_CS = B_NB * B_STG, B_ST = B_CS + 1024, B_SR = 3 * B_STG  , B_LDS = B_ST + 1024;
static_assert(B_LDS <= 163840, "lds");

template <int V> struct IC { static constexpr int value = V; };
template <int N> DI void wait_vm() { asm volatile("s_waitcnt vmcnt(%0)" :: "n"(N) : "memory"); }
DI void glds4(const void* g, u32 lds_dst) {
  u32 keep;
  asm volatile("s_mov_b32 %0, m0\n\ts_mov_b32 m0, %2\n\ts_nop 0\n\tglobal_load_lds_dword %1, off\n\ts_mov_b32 m0, %0"
               : "=&s"(keep) : "v"(g), "s"(lds_dst) : "memory");
}
DI void bar() { __builtin_amdgcn_s_barrier(); }

__global__ __launch_bounds__(512, 2) void kb_gemm1(const u16* __restrict__ wg, const u16* __restrict__ wu, char* ws) {
  extern __shared__ __attribute__((aligned(16))) char lds[];
  char* dbg = ws + WS_DBG; (void)dbg;
  STAMP_DECL;
  const int tid = threadIdx.x, lane = tid & 63;
  const int wv = __builtin_amdgcn_readfirstlane(tid >> 6);
  const int b = blockIdx.x, xcd = b & 7, j = b >> 3;
  if (j >= 28) return;
  const int m = 4 * (xcd >> 1) + j / 7, n = 7 * (xcd & 1) + j % 7;
  STAMP(1, 0);
  if (wv >= 4) {
    const int lw = wv - 4;
    bdma4(mkrsrc((const float*)(ws + WS_CS) + (lw >> 1) * FF + 128 * n + (lw & 1) * 64), lane * 4, 0, lds_addr(lds) + B_CS + lw * 256);
    const int row = 4 * lw + (lane >> 4), cp = lane & 15;
    const rsrc_t rg = mkrsrc(wg + 128 * n), ru = mkrsrc(wu + 128 * n), rh = mkrsrc((const u16*)(ws + WS_HT) + 128 * m);
    const u32 vg = (row * FF + (cp ^ ((row & 3) << 2)) * 8) * 2, vu = (row * FF + (cp ^ (((row & 3) << 2) ^ 2)) * 8) * 2;
    const u32 vh = (row * HS + (cp ^ ((row & 3) << 2)) * 8) * 2;
    const u32 dl = lds_addr(lds) + lw * 1024;
    int slot = 0; u32 sw = 0, sh = 0;
    auto issue = [&]() {
      const u32 d = dl + slot * B_STG;
      bdma16(rh, vh, sh, d + B_H); bdma16(rh, vh, sh + 16 * HS * 2, d + B_H + 4096);
      bdma16(rg, vg, sw, d + B_G); bdma16(rg, vg, sw + 16 * FF * 2, d + B_G + 4096);
      bdma16(ru, vu, sw, d + B_U); bdma16(ru, vu, sw + 16 * FF * 2, d + B_U + 4096);
      sw += 32 * FF * 2; sh += 32 * HS * 2;
      slot = slot == B_NB - 1 ? 0 : slot + 1;
    };
    const int soff = B_H + (lw + 4 * (lane >> 4)) * 256 + (((lane & 15) ^ (lw << 2)) * 16);
    float sa[8], sq[8];
#pragma unroll
    for (int i = 0; i < 8; ++i) { sa[i] = 0.f; sq[i] = 0.f; }
    int buf = 0;
    auto consume = [&]() {
      const char* st = lds + buf * B_STG + soff;
      const u32x4 v0 = *(const u32x4*)st, v1 = *(const u32x4*)(st + 16 * 256);
#pragma unroll
      for (int r = 0; r < 2; ++r)
#pragma unroll
        for (int i = 0; i < 4; ++i) {
          const u32 wi = r ? v1[i] : v0[i];
          const float lo = bf2f(wi & 0xffffu), hi = bfhi(wi);
          asm("v_add_f32 %0, %0, %1" : "+v"(sa[2 * i]) : "v"(lo)); asm("v_add_f32 %0, %0, %1" : "+v"(sa[2 * i + 1]) : "v"(hi));
          asm("v_fmac_f32 %0, %1, %1" : "+v"(sq[2 * i]) : "v"(lo)); asm("v_fmac_f32 %0, %1, %1" : "+v"(sq[2 * i + 1]) : "v"(hi));
        }
      buf = buf == B_NB - 1 ? 0 : buf + 1;
    };
    issue(); issue();
    wait_vm<6 * 1>(); bar(); issue(); issue(); consume();
    wait_vm<6 * 2>(); bar(); issue(); issue(); consume();
#pragma unroll 1
    for (int s = 2; s < 10; ++s) { wait_vm<6 * 3>(); bar(); issue(); consume(); }
    wait_vm<6 * 3>(); bar(); consume();
    wait_vm<6 * 2>(); bar(); consume();
    wait_vm<6 * 1>(); bar(); consume();
    wait_vm<0>(); bar(); consume();
    STAMP(1, 5);
    {
      f32x4* sr = (f32x4*)(lds + B_SR) + ((lw * 4 + (lane >> 4)) * 16 + (lane & 15)) * 4;
#pragma unroll
      for (int i = 0; i < 4; ++i) sr[i] = (f32x4){sa[2 * i], sq[2 * i], sa[2 * i + 1], sq[2 * i + 1]};
    }
    asm volatile("s_waitcnt lgkmcnt(0)" ::: "memory");
    bar();
    if (lw < 2) {
      const int t = lw * 64 + lane;
      const f32x2* sr = (const f32x2*)(lds + B_SR) + t;
      f32x2 tot = {0.f, 0.f};
#pragma unroll
      for (int g = 0; g < 16; ++g) tot += sr[g * 128];
      const float mean = tot[0] * (1.f / DM);
      float var = tot[1] * (1.f / DM) - mean * mean; var = var < 0.f ? 0.f : var;
      const float sd = rbf(__builtin_sqrtf(rbf(rbf(var) + 1e-5f)));
      const float rs = __builtin_amdgcn_rcpf(sd);
      ((f32x2*)(lds + B_ST))[t] = (f32x2){-rbf(mean), rs * rs};
    }
    STAMP(1, 6);
    STAMP_FLUSH(1);
    asm volatile("s_waitcnt lgkmcnt(0)" ::: "memory");
    bar();
    return;
  }
  const int tp = wv & 1, ag = wv >> 1;
  const int grp = lane >> 4, q = (lane & 15) >> 2, p = lane & 3, h = grp >> 1;
  const int aoff = ((grp & 1) ? B_U : B_G) + (8 * h + q) * 256 + (p & 1) * 8;
  const int akey = (q << 2) ^ ((grp & 1) << 1);
  const int boff = B_H + (8 * h + q) * 256 + (p & 1) * 8;
  int ao[4], bo[2];
#pragma unroll
  for (int i = 0; i < 4; ++i) ao[i] = aoff + (((2 * (4 * ag + i) + (p >> 1)) ^ akey) * 16);
#pragma unroll
  for (int t = 0; t < 2; ++t) bo[t] = boff + (((4 * (2 * tp + t) + 2 * (grp & 1) + (p >> 1)) ^ (q << 2)) * 16);
  f32x16 acc[4][2];
#pragma unroll
  for (int i = 0; i < 4; ++i) { acc[i][0] = (f32x16){}; acc[i][1] = (f32x16){}; }
  struct KF { s16x8 a[4], b[2]; };
  KF X, Y;
  auto kstep = [&](auto havecur, const KF& cur, KF& nxt, const char* st) {
#pragma unroll
    for (int i = 0; i < 4; ++i) {
#pragma unroll
      for (int t = 0; t < 2; ++t) {
        if (decltype(havecur)::value)
          acc[i][t] = __builtin_amdgcn_mfma_f32_32x32x16_bf16(__builtin_bit_cast(bf16x8, cur.a[i]), __builtin_bit_cast(bf16x8, cur.b[t]), acc[i][t], 0, 0, 0);
        const int g = 2 * i + t;
        if (g < 2) nxt.b[g] = tr_frag(st + bo[g], st + bo[g] + 4 * 256);
        else if (g < 6) nxt.a[g - 2] = tr_frag(st + ao[g - 2], st + ao[g - 2] + 4 * 256);
        FENCE();
      }
    }
  };
  int buf = 0;
  auto stage = [&](auto first) {
    bar();
    const char* st = lds + buf * B_STG;
    kstep(IC<!decltype(first)::value>{}, Y, X, st);
    kstep(IC<1>{}, X, Y, st + 16 * 256);
    buf = buf == B_NB - 1 ? 0 : buf + 1;
  };
  stage(IC<1>{});
  STAMP(1, 3);
#pragma unroll 1
  for (int s = 1; s < B_NST; ++s) stage(IC<0>{});
#pragma unroll
  for (int i = 0; i < 4; ++i)
#pragma unroll
    for (int t = 0; t < 2; ++t)
      acc[i][t] = __builtin_amdgcn_mfma_f32_32x32x16_bf16(__builtin_bit_cast(bf16x8, Y.a[i]), __builtin_bit_cast(bf16x8, Y.b[t]), acc[i][t], 0, 0, 0);
  STAMP(1, 4);
  bar();
  bar();
  const float* cs = (const float*)(lds + B_CS);
  const int hh = lane >> 5;
  char* Hf = ws + WS_H;
#pragma unroll
  for (int t = 0; t < 2; ++t) {
    const f32x2 st2 = ((const f32x2*)(lds + B_ST))[(2 * tp + t) * 32 + (lane & 31)];
    const f32x2 nm = {st2[0], st2[0]}, r2 = {st2[1], st2[1]};
#pragma unroll
    for (int i = 0; i < 4; ++i) {
      const int f0 = 16 * (4 * ag + i) + 4 * hh;
      const f32x4 g0 = *(const f32x4*)(cs + f0), g1 = *(const f32x4*)(cs + f0 + 8);
      const f32x4 u0 = *(const f32x4*)(cs + 128 + f0), u1 = *(const f32x4*)(cs + 128 + f0 + 8);
      u32x4 o;
#pragma unroll
      for (int r = 0; r < 4; ++r) {
        const f32x4 cg = r < 2 ? g0 : g1, cu = r < 2 ? u0 : u1;
        const int e = 2 * (r & 1);
        const f32x2 g = __builtin_elementwise_fma(nm, (f32x2){cg[e], cg[e + 1]}, (f32x2){acc[i][t][2 * r], acc[i][t][2 * r + 1]});
        const f32x2 u = __builtin_elementwise_fma(nm, (f32x2){cu[e], cu[e + 1]}, (f32x2){acc[i][t][8 + 2 * r], acc[i][t][8 + 2 * r + 1]});
        const f32x2 hv = (g * r2) * u;
        o[r] = pk_bf16(hv[0], hv[1]);
      }
      *(u32x4*)(Hf + ((size_t)((4 * m + 2 * tp + t) * 112 + 8 * n + 4 * ag + i) * 64 + lane) * 16) = o;
    }
  }
  STAMP(1, 1);
  STAMP_FLUSH(1);
}

constexpr int C_STG = 16384, C_NB = 10, C_NST = 28, C_LDS = C_NB * C_STG;

__global__ __launch_bounds__(512, 2) void kc_gemm2(const u16* __restrict__ wd, char* ws, u16* __restrict__ out) {
  extern __shared__ __attribute__((aligned(16))) char lds[];
  char* dbg = ws + WS_DBG; (void)dbg;
  STAMP_DECL;
  const int tid = threadIdx.x, lane = tid & 63;
  const int wv = __builtin_amdgcn_readfirstlane(tid >> 6);
  const int b = blockIdx.x, xcd = b & 7, j = b >> 3;
  if (j >= 28) return;
  STAMP(2, 0);
  const int mm = 4 * xcd + j / 7, nn = j % 7;
  if (wv >= 4) {
    const int lw = wv - 4;
    const rsrc_t rh = mkrsrc(ws + WS_H + ((size_t)((2 * mm + (lw >> 1)) * 112 + 2 * (lw & 1)) * 64) * 16);
    const rsrc_t rw = mkrsrc(wd + 64 * nn);
    const int row = 16 * lw + (lane >> 3), cp = lane & 7;
    const u32 vh = lane * 16, vw = (row * DM + (cp ^ (((row >> 1) & 1) << 2)) * 8) * 2;
    const u32 dl = lds_addr(lds) + lw * 2048;
    int slot = 0; u32 sh = 0, sw = 0;
    auto issue = [&]() {
      const u32 d = dl + slot * C_STG;
      bdma16(rh, vh, sh, d); bdma16(rh, vh, sh + 1024, d + 1024);
      bdma16(rw, vw, sw, d + 8192); bdma16(rw, vw, sw + 8 * DM * 2, d + 8192 + 1024);
      sh += 4 * 1024; sw += 64 * DM * 2;
      slot = slot == C_NB - 1 ? 0 : slot + 1;
    };
    issue(); issue();
    wait_vm<4 * 1>(); bar(); issue(); issue(); issue();
    wait_vm<4 * 3>(); bar(); issue(); issue(); issue();
    wait_vm<4 * 5>(); bar(); issue(); issue(); issue();
#pragma unroll 1
    for (int s = 3; s < 20; ++s) { wait_vm<4 * 7>(); bar(); issue(); }
    wait_vm<4 * 7>(); bar(); wait_vm<4 * 6>(); bar(); wait_vm<4 * 5>(); bar(); wait_vm<4 * 4>(); bar();
    wait_vm<4 * 3>(); bar(); wait_vm<4 * 2>(); bar(); wait_vm<4 * 1>(); bar(); wait_vm<0>(); bar();
    STAMP(2, 5); STAMP_FLUSH(2);
    return;
  }
  const int dt = wv & 1, tt = wv >> 1;
  const int hh = lane >> 5;
  const int tok = 64 * mm + 32 * tt + (lane & 31);
  const u16* hT = (const u16*)(ws + WS_HT);
  u32 res[16];
#pragma unroll
  for (int i = 0; i < 16; ++i) res[i] = hT[(64 * nn + 32 * dt + 8 * (i >> 2) + 4 * hh + (i & 3)) * HS + tok];
  const int grp = lane >> 4, q = (lane & 15) >> 2, p = lane & 3, h = grp >> 1;
  const int achunk = 4 * dt + 2 * (grp & 1) + (p >> 1);
  const int aoff = 8192 + (4 * h + q) * 128 + ((achunk ^ (((q >> 1) & 1) << 2)) * 16) + (p & 1) * 8;
  const int boff = tt * 4096 + lane * 16;
  f32x16 acc = {};
  struct F4 { s16x8 a[4]; bf16x8 b[4]; };
  F4 X, Y;
  int buf = 0;
  auto stage = [&](auto first, F4& cur, const F4& prev) {
    bar();
    const char* st = lds + buf * C_STG;
#pragma unroll
    for (int k = 0; k < 4; ++k) {
      if (!decltype(first)::value) acc = __builtin_amdgcn_mfma_f32_32x32x16_bf16(__builtin_bit_cast(bf16x8, prev.a[k]), prev.b[k], acc, 0, 0, 0);
      cur.a[k] = tr_frag(st + aoff + k * 16 * 128, st + aoff + (k * 16 + 8) * 128);
      cur.b[k] = *(const bf16x8*)(st + boff + k * 1024);
      FENCE();
    }
    buf = buf == C_NB - 1 ? 0 : buf + 1;
  };
  stage(IC<1>{}, X, Y);
  STAMP(2, 3);
  stage(IC<0>{}, Y, X);
#pragma unroll 1
  for (int s = 2; s < C_NST; s += 2) { stage(IC<0>{}, X, Y); stage(IC<0>{}, Y, X); }
#pragma unroll
  for (int k = 0; k < 4; ++k) acc = __builtin_amdgcn_mfma_f32_32x32x16_bf16(__builtin_bit_cast(bf16x8, Y.a[k]), Y.b[k], acc, 0, 0, 0);
  STAMP(2, 4);
  u16* op = out + tok * DM + 64 * nn + 32 * dt + 4 * hh;
#pragma unroll
  for (int g = 0; g < 4; ++g)
    *(u32x2*)(op + 8 * g) = (u32x2){pk_bf16(bf2f(res[4 * g]) + rbf(acc[4 * g]), bf2f(res[4 * g + 1]) + rbf(acc[4 * g + 1])),
                                    pk_bf16(bf2f(res[4 * g + 2]) + rbf(acc[4 * g + 2]), bf2f(res[4 * g + 3]) + rbf(acc[4 * g + 3]))};
  STAMP(2, 1);
  STAMP_FLUSH(2);
}

extern "C" void kernel_launch(void* const* d_in, const int* in_sizes, int n_in, void* d_out, int out_size, void* d_ws, size_t ws_size,
                              hipStream_t stream) {
  (void)in_sizes; (void)n_in; (void)out_size; (void)ws_size;
  const u16* x = (const u16*)d_in[0];
  (void)hipFuncSetAttribute((const void*)ka_conv, hipFuncAttributeMaxDynamicSharedMemorySize, A_LDS);
  (void)hipFuncSetAttribute((const void*)kb_gemm1, hipFuncAttributeMaxDynamicSharedMemorySize, B_LDS);
  (void)hipFuncSetAttribute((const void*)kc_gemm2, hipFuncAttributeMaxDynamicSharedMemorySize, C_LDS);
  ka_conv<<<256, 512, A_LDS, stream>>>(x, (const u16*)d_in[1], (const u16*)d_in[2], (const u16*)d_in[3], (const u16*)d_in[4],
                                       (const u16*)d_in[5], (const u16*)d_in[6], (const u16*)d_in[7], (const u16*)d_in[8],
                                       (const u16*)d_in[10], (const u16*)d_in[11], (const u16*)d_in[12], (char*)d_ws);
  kb_gemm1<<<256, 512, B_LDS, stream>>>((const u16*)d_in[10], (const u16*)d_in[11], (char*)d_ws);
  kc_gemm2<<<256, 512, C_LDS, stream>>>((const u16*)d_in[12], (char*)d_ws, (u16*)d_out);
}
```

```cpp
#include <hip/hip_runtime.h>
#include <stdint.h>

typedef unsigned short u16;
typedef unsigned int u32;
typedef unsigned long long u64;
typedef float f32x2 __attribute__((ext_vector_type(2)));
typedef float f32x4 __attribute__((ext_vector_type(4)));
typedef float f32x16 __attribute__((ext_vector_type(16)));
typedef unsigned u32x2 __attribute__((ext_vector_type(2)));
typedef unsigned u32x4 __attribute__((ext_vector_type(4)));
typedef short s16x4 __attribute__((ext_vector_type(4)));
typedef short s16x8 __attribute__((ext_vector_type(8)));
typedef __bf16 bf16x8 __attribute__((ext_vector_type(8)));
typedef __bf16 bf16x2 __attribute__((ext_vector_type(2)));
typedef _Float16 f16x8 __attribute__((ext_vector_type(8)));
typedef _Float16 f16x4 __attribute__((ext_vector_type(4)));
typedef _Float16 f16x2 __attribute__((ext_vector_type(2)));
#define DI __device__ __forceinline__
#define LAS __attribute__((address_space(3)))
#define GAS __attribute__((address_space(1)))
typedef GAS unsigned gu32;
typedef GAS unsigned long long gu64;
#define RLX_AGENT __ATOMIC_RELAXED, __HIP_MEMORY_SCOPE_AGENT
#define FENCE() __builtin_amdgcn_sched_barrier(0)

constexpr int SL = 2048, DM = 448, NS = 256, FF = 1792;
constexpr int HS = SL + 64;

constexpr size_t WS_EPOCH = 0;
constexpr size_t WS_GRAN  = 4096;
constexpr size_t WS_CS    = 32768;
constexpr size_t WS_FLA   = 65536;
constexpr size_t WS_FLB   = WS_FLA + 32768;
constexpr size_t WS_XG    = 131072;
constexpr size_t WS_HT    = WS_XG + (size_t)DM * 8192;
constexpr size_t WS_H     = WS_HT + (size_t)DM * HS * 2;
constexpr size_t WS_DBG   = WS_H + (size_t)SL * FF * 2;

#define STAMP_DECL do {} while (0)
#define STAMP(k, i) do {} while (0)
#define STAMP_FLUSH(k) do {} while (0)

DI float bf2f(u32 v) { return __uint_as_float(v << 16); }
DI float bfhi(u32 v) { return __uint_as_float(v & 0xffff0000u); }
DI u32 pk_bf16(float a, float b) { f32x2 v = {a, b}; return __builtin_bit_cast(u32, __builtin_convertvector(v, bf16x2)); }
DI u32 pk_f16(float a, float b) { f32x2 v = {a, b}; return __builtin_bit_cast(u32, __builtin_convertvector(v, f16x2)); }
DI u32 pk2(f32x2 v) { return __builtin_bit_cast(u32, __builtin_convertvector(v, f16x2)); }
DI float rbf(float a) { return __uint_as_float(pk_bf16(a, a) << 16); }

template <int CTRL, int RM> DI float dpp0(float v) { return __int_as_float(__builtin_amdgcn_update_dpp(0, __float_as_int(v), CTRL, RM, 0xf, true)); }
DI float lane63(float v) { return __int_as_float(__builtin_amdgcn_readlane(__float_as_int(v), 63)); }
DI void wave_sum2(float& a, float& b) {
  a += dpp0<0x111, 0xf>(a); b += dpp0<0x111, 0xf>(b); a += dpp0<0x112, 0xf>(a); b += dpp0<0x112, 0xf>(b);
  a += dpp0<0x114, 0xf>(a); b += dpp0<0x114, 0xf>(b); a += dpp0<0x118, 0xf>(a); b += dpp0<0x118, 0xf>(b);
  a += dpp0<0x142, 0xa>(a); b += dpp0<0x142, 0xa>(b); a += dpp0<0x143, 0xc>(a); b += dpp0<0x143, 0xc>(b);
  a = lane63(a); b = lane63(b);
}
DI void wave_max_sum(float& m, float& s) {
  m = fmaxf(m, dpp0<0x111, 0xf>(m)); s += dpp0<0x111, 0xf>(s); m = fmaxf(m, dpp0<0x112, 0xf>(m)); s += dpp0<0x112, 0xf>(s);
  m = fmaxf(m, dpp0<0x114, 0xf>(m)); s += dpp0<0x114, 0xf>(s); m = fmaxf(m, dpp0<0x118, 0xf>(m)); s += dpp0<0x118, 0xf>(s);
  m = fmaxf(m, dpp0<0x142, 0xa>(m)); s += dpp0<0x142, 0xa>(s); m = fmaxf(m, dpp0<0x143, 0xc>(m)); s += dpp0<0x143, 0xc>(s);
  m = lane63(m); s = lane63(s);
}

DI void glds16(const void* g, u32 lds_dst) {
  u32 keep;
  asm volatile("s_mov_b32 %0, m0\n\ts_mov_b32 m0, %2\n\ts_nop 0\n\tglobal_load_lds_dwordx4 %1, off\n\ts_mov_b32 m0, %0"
               : "=&s"(keep) : "v"(g), "s"(lds_dst) : "memory");
}
typedef __amdgpu_buffer_rsrc_t rsrc_t;
DI rsrc_t mkrsrc(const void* p) { return __builtin_amdgcn_make_buffer_rsrc((void*)p, 0, 0x7fffffff, 0x00020000); }
DI void bdma16(rsrc_t rs, u32 voff, u32 soff, u32 lds_dst) {
  asm volatile("s_mov_b32 m0, %0\n\ts_nop 0\n\tbuffer_load_dwordx4 %1, %2, %3 offen lds" :: "s"(lds_dst), "v"(voff), "s"(rs), "s"(soff) : "memory");
}
DI void bdma16_dev(rsrc_t rs, u32 voff, u32 soff, u32 lds_dst) {
  asm volatile("s_mov_b32 m0, %0\n\ts_nop 0\n\tbuffer_load_dwordx4 %1, %2, %3 offen sc1 lds" :: "s"(lds_dst), "v"(voff), "s"(rs), "s"(soff) : "memory");
}
DI void bdma4(rsrc_t rs, u32 voff, u32 soff, u32 lds_dst) {
  asm volatile("s_mov_b32 m0, %0\n\ts_nop 0\n\tbuffer_load_dword %1, %2, %3 offen lds" :: "s"(lds_dst), "v"(voff), "s"(rs), "s"(soff) : "memory");
}
DI u32 lds_addr(const void* p) { return (u32)(size_t)(LAS const char*)p; }
DI s16x8 tr_frag(const char* p0, const char* p1) {
  s16x4 a = __builtin_amdgcn_ds_read_tr16_b64_v4i16((LAS s16x4*)p0);
  s16x4 b = __builtin_amdgcn_ds_read_tr16_b64_v4i16((LAS s16x4*)p1);
  return __builtin_shufflevector(a, b, 0, 1, 2, 3, 4, 5, 6, 7);
}

struct IdxTab { u16 v[2064]; };
constexpr int tap_entry(int l) {
  if (l < 0) return 21 * 32;
  int lam = l;
  if (l >= 256) {
    int sh = l >= 1024 ? 3 : (l >= 512 ? 2 : 1);
    int q = l >> sh, rem = l & ((1 << sh) - 1), half = 1 << (sh - 1);
    if (rem > half || (rem == half && (q & 1))) q += 1;
    lam = q << sh;
  }
  int hq = lam >> 8, lo = lam & 255, c = lo >> 3, r = 0;
  if (hq == 0) r = lo & 7;
  else if (hq == 1) r = 8 + ((lo & 7) >> 1);
  else if (hq == 2) r = 12 + ((lo & 7) >> 2);
  else if (hq == 3) r = 14 + ((lo & 7) >> 2);
  else if (hq < 8) r = 16 + hq - 4;
  else { r = 20; c = 0; }
  return r * 32 + c;
}
constexpr IdxTab make_tab() { IdxTab t{}; for (int y = 0; y < 2064; ++y) t.v[y] = (u16)(2 * tap_entry(2047 - y)); return t; }
__device__ const IdxTab RIDX __attribute__((aligned(16))) = make_tab();

DI float pow2_for(float mx, int target) {
  int e = (int)(__float_as_uint(mx) >> 23);
  int be = 127 + target - (e - 126);
  be = be > 254 ? 254 : (be < 1 ? 1 : be);
  return __uint_as_float((u32)be << 23);
}
DI float pow2_inv(float p) { return __uint_as_float((254u - (__float_as_uint(p) >> 23)) << 23); }

struct CM { f32x2 a, b; };
DI CM mk(f32x2 e) { return {e, (f32x2){-e.y, e.x}}; }
DI f32x2 cmul(f32x2 p, CM m) {
  f32x2 t = __builtin_shufflevector(p, p, 0, 0) * m.a;
  return __builtin_elementwise_fma(__builtin_shufflevector(p, p, 1, 1), m.b, t);
}
DI f32x2 cexp_s(float zr, float zi, float s) {
  float m = __builtin_amdgcn_exp2f(zr * (s * 1.44269504088896341f));
  float a = __builtin_amdgcn_fractf(zi * (s * 0.15915494309189535f));
  return (f32x2){m * __builtin_amdgcn_cosf(a), m * __builtin_amdgcn_sinf(a)};
}
template <int OFF>
DI void put4(u32 base, u32 v0, u32 v1, u32 v2, u32 v3) {
  asm volatile("s_mov_b32 m0, %0\n\ts_nop 0\n\tds_write_addtid_b32 %1 offset:%5\n\tds_write_addtid_b32 %2 offset:%6\n\t"
               "ds_write_addtid_b32 %3 offset:%7\n\tds_write_addtid_b32 %4 offset:%8"
               :: "s"(base), "v"(v0), "v"(v1), "v"(v2), "v"(v3), "n"(OFF), "n"(OFF + 272), "n"(OFF + 544), "n"(OFF + 816) : "memory");
}

template <int V> struct IC { static constexpr int value = V; };
template <int N> DI void wait_vm() { asm volatile("s_waitcnt vmcnt(%0)" :: "n"(N) : "memory"); }
DI void glds4(const void* g, u32 lds_dst) {
  u32 keep;
  asm volatile("s_mov_b32 %0, m0\n\ts_mov_b32 m0, %2\n\ts_nop 0\n\tglobal_load_lds_dword %1, off\n\ts_mov_b32 m0, %0"
               : "=&s"(keep) : "v"(g), "s"(lds_dst) : "memory");
}
DI void bar() { __builtin_amdgcn_s_barrier(); }

constexpr int A_T1 = 0, A_T2 = 8192;
constexpr int A_SCR = 25088, A_SCRW = 32 * 272 + 21 * 272;
constexpr int A_CP = 0, A_CPS = 4160, A_CPC = 8 * A_CPS;
constexpr int A_EX = 2 * A_CPC, A_XX = A_EX + 16384, A_HO = A_XX + 10240, A_DUM = A_HO + 10240;
constexpr int A_UU = A_SCR + 8 * A_SCRW, A_UUC = 95 * 80;
constexpr int A_TB = A_UU + 2 * A_UUC, A_TBC = 22 * 32 * 2;
constexpr int A_SW = A_TB + 2 * A_TBC;
constexpr int A_LDS = A_SW + 64;
static_assert(A_LDS <= 163840 && A_DUM + 4096 <= A_UU && (A_SCR % 16) == 0 && (A_SCRW % 16) == 0 && (A_UU % 16) == 0, "lds");

constexpr int B_G = 0, B_U = 8192, B_H = 16384, B_STG = 24576, B_NB = 6, B_NST = 14;
constexpr int B_CS = B_NB * B_STG, B_ST = B_CS + 1024, B_SR = 3 * B_STG  , B_CNT = 163840 - 16, B_LDS = B_ST + 1024;
static_assert(B_LDS <= 163840, "lds");

constexpr int C_STG = 16384, C_NB = 10, C_NST = 28, C_LDS = C_NB * C_STG;

constexpr int K_LDS = 163840;
static_assert(A_LDS <= K_LDS - 16 && B_LDS <= K_LDS && C_LDS <= K_LDS, "lds");

__global__ __launch_bounds__(512, 2) void fused_kernel(const u16* __restrict__ x, const u16* __restrict__ Are, const u16* __restrict__ Aim,
    const u16* __restrict__ Bre, const u16* __restrict__ Bim, const u16* __restrict__ Cre, const u16* __restrict__ Cim,
    const u16* __restrict__ logdt, const u16* __restrict__ Dskip, const u16* __restrict__ wg, const u16* __restrict__ wu,
    const u16* __restrict__ wd, char* ws, u16* __restrict__ out) {
  extern __shared__ __attribute__((aligned(16))) char lds[];
  char* dbg = ws + WS_DBG; (void)dbg;
  STAMP_DECL;
  const int tid = threadIdx.x, lane = tid & 63;
  const int wv = __builtin_amdgcn_readfirstlane(tid >> 6);
  const int b = blockIdx.x, xcd = b & 7, j = b >> 3;
  const bool worker = j < 28;
  STAMP(0, 0);
  gu32* epoch_p = (gu32*)(ws + WS_EPOCH);
  gu64* gran = (gu64*)(ws + WS_GRAN);
  const u32 tag = __hip_atomic_load(epoch_p, RLX_AGENT) + 1u;
  if (tid == 0) *(u32*)(lds + B_CNT) = 0u;

  const int w = worker ? xcd * 28 + j : 0, c0 = 2 * w;
  const int trow = 8 * b + wv;
  const u32x4 xr = *(const u32x4*)(x + trow * DM + (lane < 56 ? lane : 0) * 8);
  const u32x4 xt = *(const u32x4*)(x + (128 * (w & 15) + (tid >> 2)) * DM + 32 * (w >> 4) + (tid & 3) * 8);
  const int ch = tid >> 8, n = tid & 255;
  const int idx = (c0 + ch) * NS + n;
  const u32 p_ldt = logdt[c0 + ch];
  const u32 p_are = Are[idx], p_aim = Aim[idx], p_bre = Bre[idx], p_bim = Bim[idx], p_cre = Cre[idx], p_cim = Cim[idx];
  const u32 p_d01 = *(const u32*)(Dskip + c0);
  const u32x4 ix0 = *(const u32x4*)(RIDX.v + 8 * (tid & 255)), ix1 = *(const u32x4*)(RIDX.v + 8 * (tid & 255) + 8);
  {
    const float piv = __uint_as_float((u32)__builtin_amdgcn_readfirstlane(xr[0]) << 16);
    float s = 0.f, q = 0.f, s2 = 0.f, q2 = 0.f;
#pragma unroll
    for (int i = 0; i < 4; ++i) {
      const float lo = bf2f(xr[i] & 0xffffu) - piv, hi = bfhi(xr[i]) - piv;
      s += lo; s2 += hi; q = fmaf(lo, lo, q); q2 = fmaf(hi, hi, q2);
    }
    s = lane < 56 ? s + s2 : 0.f; q = lane < 56 ? q + q2 : 0.f;
    wave_sum2(s, q);
    const float ms = s * (1.f / DM);
    float var = q * (1.f / DM) - ms * ms; var = var < 0.f ? 0.f : var;
    const float sd = rbf(__builtin_sqrtf(rbf(rbf(var) + 1e-5f)));
    if (lane == 0) __hip_atomic_store(gran + trow, ((u64)tag << 32) | pk_bf16(piv + ms, sd), RLX_AGENT);
  }
  STAMP(0, 1);
  if (!worker) {
    const int s = xcd * 4 + (j - 28);
    const u16* W = (s >> 4) ? wu : wg;
    const int colb = (s & 15) * 112;
    float a[8] = {0, 0, 0, 0, 0, 0, 0, 0};
    const int cc = tid % 14, rg = tid / 14;
    u32 warm = 0;
    {
      u32x4 wv4[7];
#pragma unroll
      for (int i = 0; i < 7; ++i) { const int c = tid + 512 * i; wv4[i] = *(const u32x4*)(wd + (size_t)s * 25088 + (c < 3136 ? c : 0) * 8); }
#pragma unroll
      for (int i = 0; i < 7; ++i) warm |= wv4[i][0] & wv4[i][1] & wv4[i][2] & wv4[i][3];
      asm volatile("" :: "v"(warm));
    }
    if (tid < 448) {
      u32x4 v[14];
#pragma unroll
      for (int r = 0; r < 14; ++r) v[r] = *(const u32x4*)(W + (rg * 14 + r) * FF + colb + cc * 8);
#pragma unroll
      for (int r = 0; r < 14; ++r)
#pragma unroll
        for (int i = 0; i < 4; ++i) { a[2 * i] += bf2f(v[r][i] & 0xffffu); a[2 * i + 1] += bfhi(v[r][i]); }
      float* red = (float*)lds;
#pragma unroll
      for (int i = 0; i < 8; ++i) red[rg * 112 + cc * 8 + i] = a[i];
    }
    __syncthreads();
    if (tid < 112) {
      float t = 0.f;
      for (int r = 0; r < 32; ++r) t += ((float*)lds)[r * 112 + tid];
      __hip_atomic_store((gu64*)(ws + WS_CS) + (s >> 4) * FF + colb + tid, ((u64)tag << 32) | __float_as_uint(t), RLX_AGENT);
    }
    STAMP_FLUSH(0);
    return;
  }
  *(u32x4*)(lds + A_T1 + tid * 16) = xt;
  const float dt = rbf(__builtin_amdgcn_exp2f(bf2f(p_ldt) * 1.44269504088896341f));
  const float zr = dt * bf2f(p_are), zi = dt * bf2f(p_aim);
  const f32x2 E8 = cexp_s(zr, zi, 8.f);
  const f32x2 E1 = cexp_s(zr, -zi, 1.f), F = cexp_s(zr, -zi, 256.f);
  {
    const int grp = lane >> 4, q = (lane & 15) >> 2, p = lane & 3;
#pragma unroll
    for (int i = 0; i < 2; ++i) {
      const int tb = 16 * wv + 8 * i + 4 * (grp >> 1);
      const s16x4 v = __builtin_amdgcn_ds_read_tr16_b64_v4i16((LAS s16x4*)(lds + A_T1 + (tb + q) * 64 + (grp & 1) * 32 + p * 8));
      const u32x2 vw = __builtin_bit_cast(u32x2, v);
      *(u32x4*)(lds + A_T2 + (16 * (grp & 1) + (lane & 15)) * 528 + tb * 4) = (u32x4){vw[0], tag, vw[1], tag};
    }
  }
  const u32 scb = lds_addr(lds) + A_SCR + wv * A_SCRW;
  const CM M8 = mk(E8);
  f32x2 p0 = {16384.f, 0.f}, p1 = cmul(p0, M8), p2 = cmul(p1, M8), p3 = cmul(p2, M8);
  const CM M32 = mk(cmul(p2, mk(p2 * (1.f / 16384.f))) * (1.f / 16384.f));
  __syncthreads();
  {
    __amdgpu_buffer_rsrc_t rs = __builtin_amdgcn_make_buffer_rsrc((void*)(ws + WS_XG), 0, DM * 8192, 0x00020000);
#pragma unroll
    for (int k = 0; k < 2; ++k) {
      const int id = tid + 512 * k, c = id >> 5, pos = id & 31;
      const u32x4 v = *(const u32x4*)(lds + A_T2 + c * 528 + pos * 16);
      __builtin_amdgcn_raw_buffer_store_b128(v, rs, (32 * (w >> 4) + c) * 8192 + (w & 15) * 512 + pos * 16, 0, 16);
    }
  }
  STAMP(0, 2);
  {
    const float br = dt * bf2f(p_bre), bi = dt * bf2f(p_bim);
    const float cr = bf2f(p_cre), ci = bf2f(p_cim);
    const float CBr = cr * br - ci * bi, CBi = cr * bi + ci * br;
    float mx = fmaxf(fabsf(CBr), fabsf(CBi)), sabs = fabsf(CBr) + fabsf(CBi);
    wave_max_sum(mx, sabs);
    const float scA = pow2_for(mx, 14);
    if (lane == 0) { ((float*)(lds + A_SW))[wv] = pow2_inv(scA) * (1.f / 16384.f); ((float*)(lds + A_SW))[8 + wv] = sabs; }
#define COL4(R) { put4<(R) * 272>(scb, pk2(p0), pk2(p1), pk2(p2), pk2(p3)); \
                  if ((R) < 28) { p0 = cmul(p0, M32); p1 = cmul(p1, M32); p2 = cmul(p2, M32); p3 = cmul(p3, M32); } }
    COL4(0) COL4(4) COL4(8) COL4(12) COL4(16) COL4(20) COL4(24) COL4(28)
#undef COL4
    const CM M1 = mk(E1), MF = mk(F);
    const f32x2 E2 = cmul(E1, M1);
    const CM M2 = mk(E2), M4 = mk(cmul(E2, M2));
    const f32x2 X0 = {CBr * scA, -CBi * scA};
    const f32x2 X1 = cmul(X0, M1), G1 = cmul(X0, MF);
    const f32x2 X2 = cmul(X0, M2), X3 = cmul(X1, M2), G2 = cmul(G1, MF), Y1 = cmul(G1, M2);
    put4<(32 + 0) * 272>(scb, pk2(X0), pk2(X1), pk2(X2), pk2(X3));
    const f32x2 X4 = cmul(X0, M4), X5 = cmul(X1, M4), X6 = cmul(X2, M4), X7 = cmul(X3, M4);
    const f32x2 G3 = cmul(G2, MF), Y2 = cmul(G1, M4), Y3 = cmul(Y1, M4);
    put4<(32 + 4) * 272>(scb, pk2(X4), pk2(X5), pk2(X6), pk2(X7));
    put4<(32 + 8) * 272>(scb, pk2(G1), pk2(Y1), pk2(Y2), pk2(Y3));
    const f32x2 G4 = cmul(G3, MF), Z2 = cmul(G2, M4), Z3 = cmul(G3, M4);
    put4<(32 + 12) * 272>(scb, pk2(G2), pk2(Z2), pk2(G3), pk2(Z3));
    const CM MF2 = mk(cmul(F, MF));
    const f32x2 G5 = cmul(G3, MF2), G6 = cmul(G4, MF2);
    const f32x2 G7 = cmul(G5, MF2), G8 = cmul(G6, MF2);
    put4<(32 + 16) * 272>(scb, pk2(G4), pk2(G5), pk2(G6), pk2(G7));
    {
      const u32 g8 = pk2(G8);
      asm volatile("s_mov_b32 m0, %0\n\ts_nop 0\n\tds_write_addtid_b32 %1 offset:%2" :: "s"(scb), "v"(g8), "n"((32 + 20) * 272) : "memory");
    }
    char* scr = lds + A_SCR + wv * A_SCRW;
    const char* fr = scr + (lane & 31) * 272 + (lane >> 5) * 16;
    f16x8 bf[8], af[8];
#pragma unroll
    for (int ks = 0; ks < 8; ++ks) { bf[ks] = *(const f16x8*)(fr + ks * 32); af[ks] = *(const f16x8*)(fr + 32 * 272 + ks * 32); }
    f32x16 acc0 = {}, acc1 = {};
#pragma unroll
    for (int ks = 0; ks < 8; ks += 2) {
      acc0 = __builtin_amdgcn_mfma_f32_32x32x16_f16(af[ks], bf[ks], acc0, 0, 0, 0);
      acc1 = __builtin_amdgcn_mfma_f32_32x32x16_f16(af[ks + 1], bf[ks + 1], acc1, 0, 0, 0);
    }
#pragma unroll
    for (int r = 0; r < 12; ++r) {
      int row = (r & 3) + 8 * (r >> 2) + 4 * (lane >> 5);
      *(float*)(scr + row * 128 + (lane & 31) * 4) = acc0[r] + acc1[r];
    }
  }
  STAMP(0, 3);
  const __amdgpu_buffer_rsrc_t rs_g = __builtin_amdgcn_make_buffer_rsrc((void*)(ws + WS_GRAN), 0, SL * 8, 0x00020000);
  const __amdgpu_buffer_rsrc_t rs_x = __builtin_amdgcn_make_buffer_rsrc((void*)(ws + WS_XG + (size_t)c0 * 8192), 0, 2 * 8192, 0x00020000);
  u32x4 g[4], xg[4];
  const int t8 = tid & 255;
  auto fetch = [&]() {
#pragma unroll
    for (int i = 0; i < 4; ++i) g[i] = __builtin_amdgcn_raw_buffer_load_b128(rs_g, t8 * 64 + i * 16, 0, 16);
#pragma unroll
    for (int i = 0; i < 4; ++i) xg[i] = __builtin_amdgcn_raw_buffer_load_b128(rs_x, (i >> 1) * 8192 + t8 * 32 + (i & 1) * 16, 0, 16);
  };
  if (wv < 4) fetch();
  asm volatile("s_waitcnt lgkmcnt(0)" ::: "memory");
  bar();
  float kinv0, kinv1;
  {
    const float* sw = (const float*)(lds + A_SW);
    const float k0 = pow2_for(sw[8] + sw[9] + sw[10] + sw[11], 15), k1 = pow2_for(sw[12] + sw[13] + sw[14] + sw[15], 15);
    kinv0 = pow2_inv(k0); kinv1 = pow2_inv(k1);
    const float ks = ch ? k1 : k0;
    const float i0 = sw[4 * ch] * ks, i1 = sw[4 * ch + 1] * ks, i2 = sw[4 * ch + 2] * ks, i3 = sw[4 * ch + 3] * ks;
    const char* p = lds + A_SCR + (4 * ch) * A_SCRW + (tid & 255) * 4;
    float v[3][4];
#pragma unroll
    for (int it = 0; it < 3; ++it)
#pragma unroll
      for (int k = 0; k < 4; ++k) v[it][k] = *(const float*)(p + it * 1024 + k * A_SCRW);
#pragma unroll
    for (int it = 0; it < 3; ++it) {
      const int e = (tid & 255) + 256 * it;
      const float t = v[it][0] * i0 + v[it][1] * i1 + v[it][2] * i2 + v[it][3] * i3;
      if (it < 2 || e < 672) *(_Float16*)(lds + A_TB + ch * A_TBC + e * 2) = (_Float16)t;
    }
    if (tid < 8) { int cc = tid >> 2; *(u32x4*)(lds + A_TB + cc * A_TBC + 21 * 64 + (tid & 3) * 16) = (u32x4){0, 0, 0, 0}; }
    if (tid < 310) { int cc = tid / 155, r = tid % 155; *(u32x4*)(lds + A_UU + cc * A_UUC + r * 16) = (u32x4){0, 0, 0, 0}; }
  }
  asm volatile("s_waitcnt lgkmcnt(0)" ::: "memory");
  bar();
  if (wv >= 4) {
#pragma unroll
   for (int ch = 0; ch < 2; ++ch) {
    const char* tb = lds + A_TB + ch * A_TBC;
    u32 P[8];
#pragma unroll
    for (int k = 0; k < 8; ++k) {
      u32 iw = k < 4 ? ix0[k] : ix1[k - 4];
      u32 lo = *(const u16*)(tb + (iw & 0xffffu));
      u32 hi = (k < 7) ? *(const u16*)(tb + (iw >> 16)) : 0u;
      P[k] = lo | (hi << 16);
    }
    u32 Q[7];
#pragma unroll
    for (int k = 0; k < 7; ++k) Q[k] = __builtin_amdgcn_alignbit(P[k + 1], P[k], 16);
    char* cp = lds + A_CP + ch * A_CPC + (tid & 255) * 16;
#pragma unroll
    for (int s = 0; s < 8; ++s) {
      const int off = 7 - s;
      u32x4 v;
      if ((off & 1) == 0) v = (u32x4){P[off / 2], P[off / 2 + 1], P[off / 2 + 2], P[off / 2 + 3]};
      else v = (u32x4){Q[off / 2], Q[off / 2 + 1], Q[off / 2 + 2], Q[off / 2 + 3]};
      *(u32x4*)(cp + s * A_CPS) = v;
    }
   }
    if (t8 < 48) { int cc = t8 / 24, r = t8 % 24; *(u32x4*)(lds + A_CP + cc * A_CPC + (r / 3) * A_CPS + (256 + r % 3) * 16) = (u32x4){0, 0, 0, 0}; }
    STAMP(0, 4);
  } else {
    for (int spin = 0; spin < (1 << 16); ++spin) {
      u32 bad = 0;
#pragma unroll
      for (int i = 0; i < 4; ++i) bad |= (g[i][1] ^ tag) | (g[i][3] ^ tag) | (xg[i][1] ^ tag) | (xg[i][3] ^ tag);
      if (__all(bad == 0)) break;
      __builtin_amdgcn_s_sleep(2);
      fetch();
    }
    STAMP(0, 4);
    float uu[2][8];
#pragma unroll
    for (int i = 0; i < 8; ++i) {
      const u32 st = g[i >> 1][(i & 1) * 2];
      const float m = bf2f(st & 0xffffu), rs_ = __builtin_amdgcn_rcpf(bfhi(st));
#pragma unroll
      for (int c = 0; c < 2; ++c) {
        const u32 xw = xg[2 * c + (i >> 2)][(i & 2)];
        const float xv = (i & 1) ? bfhi(xw) : bf2f(xw & 0xffffu);
        uu[c][i] = rbf(rbf(xv - m) * rs_);
      }
    }
#pragma unroll
    for (int c = 0; c < 2; ++c) {
      *(u32x4*)(lds + A_UU + c * A_UUC + ((tid >> 2) + 31) * 80 + (tid & 3) * 16) =
          (u32x4){pk_f16(uu[c][0], uu[c][1]), pk_f16(uu[c][2], uu[c][3]), pk_f16(uu[c][4], uu[c][5]), pk_f16(uu[c][6], uu[c][7])};
      *(u32x4*)(lds + A_XX + c * 5120 + (tid >> 2) * 80 + (tid & 3) * 16) = (u32x4){xg[2 * c][0], xg[2 * c][2], xg[2 * c + 1][0], xg[2 * c + 1][2]};
    }
  }
  STAMP(0, 5);
  asm volatile("s_waitcnt lgkmcnt(0)" ::: "memory");
  bar();
  if (b == 0 && tid == 0) __hip_atomic_store(epoch_p, tag, RLX_AGENT);
  const int cw = (wv >> 1) & 1, hf = wv & 1;
  const int q = lane & 31, h = lane >> 5;
  f32x16 keep;
  if (wv < 4) {
    const int a = q >> 3, s = q & 7;
    const char* abase = lds + A_CP + cw * A_CPC + s * A_CPS + 4080 - 16 * a + 16 * h - 32 * (32 * hf + 30) - 2048;
    const char* bbase = lds + A_UU + cw * A_UUC + (q + 31 - (16 * hf + 15)) * 80 + 16 * h;
    f32x16 y0 = {}, y1 = {};
    f16x8 fa[2][8];
    auto ld = [&](int i, f16x8 (&f)[8]) {
#pragma unroll
      for (int kk = 0; kk < 2; ++kk) {
        const int erel = 30 - (2 * i - kk);
        f[4 * kk] = *(const f16x8*)(abase + 2048 + 32 * erel);
        f[4 * kk + 1] = *(const f16x8*)(bbase + (15 - i) * 80 + kk * 32);
        f[4 * kk + 2] = *(const f16x8*)(bbase + (15 - i + 32) * 80 + kk * 32);
        f[4 * kk + 3] = *(const f16x8*)(abase + 32 * erel);
      }
    };
    ld(0, fa[0]);
#pragma unroll
    for (int i = 0; i < 16; ++i) {
      const f16x8 (&f)[8] = fa[i & 1];
      f16x8 (&g)[8] = fa[(i + 1) & 1];
#pragma unroll
      for (int kk = 0; kk < 2; ++kk) {
        const int erel = 30 - (2 * (i + 1) - kk);
        y0 = __builtin_amdgcn_mfma_f32_32x32x16_f16(f[4 * kk], f[4 * kk + 1], y0, 0, 0, 0);
        FENCE();
        y1 = __builtin_amdgcn_mfma_f32_32x32x16_f16(f[4 * kk], f[4 * kk + 2], y1, 0, 0, 0);
        if (i + 1 < 16) { g[4 * kk] = *(const f16x8*)(abase + 2048 + 32 * erel); g[4 * kk + 2] = *(const f16x8*)(bbase + (15 - i - 1 + 32) * 80 + kk * 32); }
        FENCE();
        y1 = __builtin_amdgcn_mfma_f32_32x32x16_f16(f[4 * kk + 3], f[4 * kk + 1], y1, 0, 0, 0);
        if (i + 1 < 16) { g[4 * kk + 1] = *(const f16x8*)(bbase + (15 - i - 1) * 80 + kk * 32); g[4 * kk + 3] = *(const f16x8*)(abase + 32 * erel); }
        FENCE();
      }
    }
    const f32x16 give = hf ? y0 : y1;
    keep = hf ? y1 : y0;
    char* pp = lds + A_EX + wv * 4096 + lane * 16;
#pragma unroll
    for (int g = 0; g < 4; ++g) *(f32x4*)(pp + g * 1024) = (f32x4){give[4 * g], give[4 * g + 1], give[4 * g + 2], give[4 * g + 3]};
  }
  STAMP(0, 6);
  asm volatile("s_waitcnt lgkmcnt(0)" ::: "memory");
  bar();
  const int m = 4 * (xcd >> 1) + j / 7, nb = 7 * (xcd & 1) + j % 7;
  const int lw = wv & 3;
  const int brow = 4 * lw + (lane >> 4), bcp = lane & 15;
  const rsrc_t rg = mkrsrc(wg + 128 * nb), ru = mkrsrc(wu + 128 * nb), rh = mkrsrc((const u16*)(ws + WS_HT) + 128 * m);
  const u32 vg = (brow * FF + (bcp ^ ((brow & 3) << 2)) * 8) * 2, vu = (brow * FF + (bcp ^ (((brow & 3) << 2) ^ 2)) * 8) * 2;
  const u32 vh = (brow * HS + (bcp ^ ((brow & 3) << 2)) * 8) * 2;
  const u32 bdl = lds_addr(lds) + lw * 1024;
  auto issueW = [&](int s, int slot) {
    const u32 d = bdl + slot * B_STG, sw = s * 32 * FF * 2;
    bdma16(rg, vg, sw, d + B_G); bdma16(rg, vg, sw + 16 * FF * 2, d + B_G + 4096);
    bdma16(ru, vu, sw, d + B_U); bdma16(ru, vu, sw + 16 * FF * 2, d + B_U + 4096);
  };
  auto issueH = [&](int s, int slot) {
    const u32 d = bdl + slot * B_STG, sh = s * 32 * HS * 2;
    bdma16_dev(rh, vh, sh, d + B_H); bdma16_dev(rh, vh, sh + 16 * HS * 2, d + B_H + 4096);
  };
  if (wv >= 4) { issueW(0, 0); issueW(1, 1); issueW(2, 2); }
  if (wv < 4) {
    const char* pp = lds + A_EX + (wv ^ 1) * 4096 + lane * 16;
    const float ki = cw ? kinv1 : kinv0, dk = cw ? bfhi(p_d01) : bf2f(p_d01 & 0xffffu);
    const int row = 32 * hf + q;
#pragma unroll
    for (int g = 0; g < 4; ++g) {
      const f32x4 o = *(const f32x4*)(pp + g * 1024);
      const f16x4 u4 = *(const f16x4*)(lds + A_UU + cw * A_UUC + (row + 31) * 80 + g * 16 + h * 8);
      const u32x2 x2 = *(const u32x2*)(lds + A_XX + cw * 5120 + row * 80 + g * 16 + h * 8);
      float hv[4];
#pragma unroll
      for (int i = 0; i < 4; ++i) {
        const u32 xw = x2[i >> 1];
        const float xv = (i & 1) ? bfhi(xw) : bf2f(xw & 0xffffu);
        hv[i] = xv + rbf(rbf((keep[4 * g + i] + o[i]) * ki) + rbf((float)u4[i] * dk));
      }
      *(u32x2*)(lds + A_HO + cw * 5120 + row * 80 + g * 16 + h * 8) = (u32x2){pk_bf16(hv[0], hv[1]), pk_bf16(hv[2], hv[3])};
    }
  }
  asm volatile("s_waitcnt lgkmcnt(0)" ::: "memory");
  bar();
  if (wv < 4) {
#pragma unroll
    for (int c = 0; c < 2; ++c) {
      const u32x4 v = *(const u32x4*)(lds + A_HO + c * 5120 + (t8 >> 2) * 80 + (t8 & 3) * 16);
      __builtin_amdgcn_raw_buffer_store_b128(v, mkrsrc(ws + WS_HT), ((c0 + c) * HS + 8 * t8) * 2, 0, 16);
    }
    asm volatile("s_waitcnt vmcnt(0)" ::: "memory");
  } else issueW(3, 3);
  bar();
  if (tid == 0) __hip_atomic_store((gu32*)(ws + WS_FLA) + w * 32, tag, RLX_AGENT);
  STAMP(0, 7);
  if (wv >= 4) {
    {
      gu32* fl = (gu32*)(ws + WS_FLA) + (lw * 56 + (lane < 56 ? lane : 0)) * 32;
      for (int spin = 0; spin < (1 << 16); ++spin) {
        if (__all(__hip_atomic_load(fl, RLX_AGENT) == tag)) break;
        __builtin_amdgcn_s_sleep(2);
      }
      LAS u32* cnt = (LAS u32*)(lds + B_CNT);
      if (lane == 0) __hip_atomic_fetch_add(cnt, 1u, __ATOMIC_RELAXED, __HIP_MEMORY_SCOPE_WORKGROUP);
      for (int spin = 0; spin < (1 << 20); ++spin) {
        if (__hip_atomic_load(cnt, __ATOMIC_RELAXED, __HIP_MEMORY_SCOPE_WORKGROUP) >= 4u) break;
        __builtin_amdgcn_s_sleep(1);
      }
    }
    STAMP(0, 8);
    issueH(0, 0); issueH(1, 1); issueH(2, 2); issueH(3, 3);
    int slot = 4, sn = 4;
    auto issue = [&]() { issueH(sn, slot); issueW(sn, slot); ++sn; slot = slot == B_NB - 1 ? 0 : slot + 1; };
    const int soff = B_H + (lw + 4 * (lane >> 4)) * 256 + (((lane & 15) ^ (lw << 2)) * 16);
    float sa[8], sq[8];
#pragma unroll
    for (int i = 0; i < 8; ++i) { sa[i] = 0.f; sq[i] = 0.f; }
    int buf = 0;
    auto consume = [&]() {
      const char* st = lds + buf * B_STG + soff;
      const u32x4 v0 = *(const u32x4*)st, v1 = *(const u32x4*)(st + 16 * 256);
#pragma unroll
      for (int r = 0; r < 2; ++r)
#pragma unroll
        for (int i = 0; i < 4; ++i) {
          const u32 wi = r ? v1[i] : v0[i];
          const float lo = bf2f(wi & 0xffffu), hi = bfhi(wi);
          asm("v_add_f32 %0, %0, %1" : "+v"(sa[2 * i]) : "v"(lo)); asm("v_add_f32 %0, %0, %1" : "+v"(sa[2 * i + 1]) : "v"(hi));
          asm("v_fmac_f32 %0, %1, %1" : "+v"(sq[2 * i]) : "v"(lo)); asm("v_fmac_f32 %0, %1, %1" : "+v"(sq[2 * i + 1]) : "v"(hi));
        }
      buf = buf == B_NB - 1 ? 0 : buf + 1;
    };
    wait_vm<6>(); bar(); issue(); consume();
    wait_vm<10>(); bar(); issue(); consume();
    wait_vm<14>(); bar(); issue(); consume();
#pragma unroll 1
    for (int s = 3; s < 10; ++s) { wait_vm<6 * 3>(); bar(); issue(); consume(); }
    wait_vm<6 * 3>(); bar(); consume();
    wait_vm<6 * 2>(); bar(); consume();
    wait_vm<6 * 1>(); bar(); consume();
    wait_vm<0>(); bar(); consume();
    STAMP(0, 9);
    {
      f32x4* sr = (f32x4*)(lds + B_SR) + ((lw * 4 + (lane >> 4)) * 16 + (lane & 15)) * 4;
#pragma unroll
      for (int i = 0; i < 4; ++i) sr[i] = (f32x4){sa[2 * i], sq[2 * i], sa[2 * i + 1], sq[2 * i + 1]};
    }
    if (lw >= 2) {
      const rsrc_t rc = mkrsrc(ws + WS_CS + ((lw - 2) * FF + 128 * nb) * 8);
      u32x4 cg;
      for (int spin = 0; spin < (1 << 16); ++spin) {
        cg = __builtin_amdgcn_raw_buffer_load_b128(rc, lane * 16, 0, 16);
        if (__all(cg[1] == tag && cg[3] == tag)) break;
        __builtin_amdgcn_s_sleep(2);
      }
      *(u32x2*)(lds + B_CS + (lw - 2) * 512 + lane * 8) = (u32x2){cg[0], cg[2]};
    }
    asm volatile("s_waitcnt lgkmcnt(0)" ::: "memory");
    bar();
    if (lw < 2) {
      const int t = lw * 64 + lane;
      const f32x2* sr = (const f32x2*)(lds + B_SR) + t;
      f32x2 tot = {0.f, 0.f};
#pragma unroll
      for (int g = 0; g < 16; ++g) tot += sr[g * 128];
      const float mean = tot[0] * (1.f / DM);
      float var = tot[1] * (1.f / DM) - mean * mean; var = var < 0.f ? 0.f : var;
      const float sd = rbf(__builtin_sqrtf(rbf(rbf(var) + 1e-5f)));
      const float rs = __builtin_amdgcn_rcpf(sd);
      ((f32x2*)(lds + B_ST))[t] = (f32x2){-rbf(mean), rs * rs};
    }
    asm volatile("s_waitcnt lgkmcnt(0)" ::: "memory");
    bar();
  } else {
  const int tp = wv & 1, ag = wv >> 1;
  const int grp = lane >> 4, q = (lane & 15) >> 2, p = lane & 3, h = grp >> 1;
  const int aoff = ((grp & 1) ? B_U : B_G) + (8 * h + q) * 256 + (p & 1) * 8;
  const int akey = (q << 2) ^ ((grp & 1) << 1);
  const int boff = B_H + (8 * h + q) * 256 + (p & 1) * 8;
  int ao[4], bo[2];
#pragma unroll
  for (int i = 0; i < 4; ++i) ao[i] = aoff + (((2 * (4 * ag + i) + (p >> 1)) ^ akey) * 16);
#pragma unroll
  for (int t = 0; t < 2; ++t) bo[t] = boff + (((4 * (2 * tp + t) + 2 * (grp & 1) + (p >> 1)) ^ (q << 2)) * 16);
  f32x16 acc[4][2];
#pragma unroll
  for (int i = 0; i < 4; ++i) { acc[i][0] = (f32x16){}; acc[i][1] = (f32x16){}; }
  struct KF { s16x8 a[4], b[2]; };
  KF X, Y;
  auto kstep = [&](auto havecur, const KF& cur, KF& nxt, const char* st) {
#pragma unroll
    for (int i = 0; i < 4; ++i) {
#pragma unroll
      for (int t = 0; t < 2; ++t) {
        if (decltype(havecur)::value)
          acc[i][t] = __builtin_amdgcn_mfma_f32_32x32x16_bf16(__builtin_bit_cast(bf16x8, cur.a[i]), __builtin_bit_cast(bf16x8, cur.b[t]), acc[i][t], 0, 0, 0);
        const int g = 2 * i + t;
        if (g < 2) nxt.b[g] = tr_frag(st + bo[g], st + bo[g] + 4 * 256);
        else if (g < 6) nxt.a[g - 2] = tr_frag(st + ao[g - 2], st + ao[g - 2] + 4 * 256);
        FENCE();
      }
    }
  };
  int buf = 0;
  auto stage = [&](auto first) {
    bar();
    const char* st = lds + buf * B_STG;
    kstep(IC<!decltype(first)::value>{}, Y, X, st);
    kstep(IC<1>{}, X, Y, st + 16 * 256);
    buf = buf == B_NB - 1 ? 0 : buf + 1;
  };
  stage(IC<1>{});
#pragma unroll 1
  for (int s = 1; s < B_NST; ++s) stage(IC<0>{});
  STAMP(0, 9);
  bar();
#pragma unroll
  for (int i = 0; i < 4; ++i)
#pragma unroll
    for (int t = 0; t < 2; ++t)
      acc[i][t] = __builtin_amdgcn_mfma_f32_32x32x16_bf16(__builtin_bit_cast(bf16x8, Y.a[i]), __builtin_bit_cast(bf16x8, Y.b[t]), acc[i][t], 0, 0, 0);
  bar();
  const float* cs = (const float*)(lds + B_CS);
  const int hh = lane >> 5;
  const rsrc_t rH = mkrsrc(ws + WS_H);
  f32x2 st2[2];
#pragma unroll
  for (int t = 0; t < 2; ++t) st2[t] = ((const f32x2*)(lds + B_ST))[(2 * tp + t) * 32 + (lane & 31)];
#pragma unroll
  for (int i = 0; i < 4; ++i) {
    const int f0 = 16 * (4 * ag + i) + 4 * hh;
    const f32x4 g0 = *(const f32x4*)(cs + f0), g1 = *(const f32x4*)(cs + f0 + 8);
    const f32x4 u0 = *(const f32x4*)(cs + 128 + f0), u1 = *(const f32x4*)(cs + 128 + f0 + 8);
#pragma unroll
    for (int t = 0; t < 2; ++t) {
      const f32x2 nm = {st2[t][0], st2[t][0]}, r2 = {st2[t][1], st2[t][1]};
      u32x4 o;
#pragma unroll
      for (int r = 0; r < 4; ++r) {
        const f32x4 cg = r < 2 ? g0 : g1, cu = r < 2 ? u0 : u1;
        const int e = 2 * (r & 1);
        const f32x2 g = __builtin_elementwise_fma(nm, (f32x2){cg[e], cg[e + 1]}, (f32x2){acc[i][t][2 * r], acc[i][t][2 * r + 1]});
        const f32x2 u = __builtin_elementwise_fma(nm, (f32x2){cu[e], cu[e + 1]}, (f32x2){acc[i][t][8 + 2 * r], acc[i][t][8 + 2 * r + 1]});
        const f32x2 hv = (g * r2) * u;
        o[r] = pk_bf16(hv[0], hv[1]);
      }
      __builtin_amdgcn_raw_buffer_store_b128(o, rH, (((4 * m + 2 * tp + t) * 112 + 8 * nb + 4 * ag + i) * 64 + lane) * 16, 0, 16);
    }
  }
  STAMP(0, 10);
  asm volatile("s_waitcnt vmcnt(0)" ::: "memory");
  }
  const int mm = 4 * xcd + j / 7, nn = j % 7;
  const rsrc_t rHc = mkrsrc(ws + WS_H + ((size_t)((2 * mm + (lw >> 1)) * 112 + 2 * (lw & 1)) * 64) * 16);
  const rsrc_t rw = mkrsrc(wd + 64 * nn);
  const int crow = 16 * lw + (lane >> 3), ccp = lane & 7;
  const u32 cvh = lane * 16, cvw = (crow * DM + (ccp ^ (((crow >> 1) & 1) << 2)) * 8) * 2;
  const u32 cdl = lds_addr(lds) + lw * 2048;
  auto cW = [&](int s, int slot) { const u32 d = cdl + slot * C_STG + 8192, sw = s * 64 * DM * 2; bdma16(rw, cvw, sw, d); bdma16(rw, cvw, sw + 8 * DM * 2, d + 1024); };
  auto cH = [&](int s, int slot) { const u32 d = cdl + slot * C_STG, sh = s * 4096; bdma16_dev(rHc, cvh, sh, d); bdma16_dev(rHc, cvh, sh + 1024, d + 1024); };
  if (wv >= 4) {
#pragma unroll
    for (int s = 0; s < 8; ++s) cW(s, s);
  }
  bar();
  if (tid == 0) __hip_atomic_store((gu32*)(ws + WS_FLB) + (m * 14 + nb) * 32, tag, RLX_AGENT);
  STAMP(0, 11);
  if (wv >= 4) {
    {
      gu32* fl = (gu32*)(ws + WS_FLB) + ((mm >> 1) * 14 + (lane < 14 ? lane : 0)) * 32;
      for (int spin = 0; spin < (1 << 16); ++spin) {
        if (__all(__hip_atomic_load(fl, RLX_AGENT) == tag)) break;
        __builtin_amdgcn_s_sleep(2);
      }
    }
    STAMP(0, 12);
#pragma unroll
    for (int s = 0; s < 8; ++s) cH(s, s);
    int slot = 8, sn = 8;
    auto issue = [&]() { cH(sn, slot); cW(sn, slot); ++sn; slot = slot == C_NB - 1 ? 0 : slot + 1; };
    wait_vm<14>(); bar(); issue();
    wait_vm<16>(); bar(); issue();
    wait_vm<18>(); bar(); issue();
    wait_vm<20>(); bar(); issue();
    wait_vm<22>(); bar(); issue();
    wait_vm<24>(); bar(); issue();
    wait_vm<26>(); bar(); issue();
#pragma unroll 1
    for (int s = 7; s < 20; ++s) { wait_vm<4 * 7>(); bar(); issue(); }
    wait_vm<4 * 7>(); bar(); wait_vm<4 * 6>(); bar(); wait_vm<4 * 5>(); bar(); wait_vm<4 * 4>(); bar();
    wait_vm<4 * 3>(); bar(); wait_vm<4 * 2>(); bar(); wait_vm<4 * 1>(); bar(); wait_vm<0>(); bar();
    STAMP(0, 13);
    STAMP_FLUSH(0);
    return;
  }
  const int dtl = wv & 1, tt = wv >> 1;
  const int tok = 64 * mm + 32 * tt + (lane & 31);
  const rsrc_t rres = mkrsrc(ws + WS_HT);
  u32 res[16];
#pragma unroll
  for (int i = 0; i < 16; ++i) res[i] = __builtin_amdgcn_raw_buffer_load_b16(rres, ((64 * nn + 32 * dtl + 8 * (i >> 2) + 4 * (lane >> 5) + (i & 3)) * HS + tok) * 2, 0, 16);
  const int cgrp = lane >> 4, cq = (lane & 15) >> 2, cp_ = lane & 3, chh = cgrp >> 1;
  const int achunk = 4 * dtl + 2 * (cgrp & 1) + (cp_ >> 1);
  const int caoff = 8192 + (4 * chh + cq) * 128 + ((achunk ^ (((cq >> 1) & 1) << 2)) * 16) + (cp_ & 1) * 8;
  const int cboff = tt * 4096 + lane * 16;
  f32x16 cacc = {};
  struct F4 { s16x8 a[4]; bf16x8 b[4]; };
  F4 Xc, Yc;
  int cbuf = 0;
  auto cstage = [&](auto first, F4& cur, const F4& prev) {
    bar();
    const char* st = lds + cbuf * C_STG;
#pragma unroll
    for (int k = 0; k < 4; ++k) {
      if (!decltype(first)::value) cacc = __builtin_amdgcn_mfma_f32_32x32x16_bf16(__builtin_bit_cast(bf16x8, prev.a[k]), prev.b[k], cacc, 0, 0, 0);
      cur.a[k] = tr_frag(st + caoff + k * 16 * 128, st + caoff + (k * 16 + 8) * 128);
      cur.b[k] = *(const bf16x8*)(st + cboff + k * 1024);
      FENCE();
    }
    cbuf = cbuf == C_NB - 1 ? 0 : cbuf + 1;
  };
  cstage(IC<1>{}, Xc, Yc);
  cstage(IC<0>{}, Yc, Xc);
#pragma unroll 1
  for (int s = 2; s < C_NST; s += 2) { cstage(IC<0>{}, Xc, Yc); cstage(IC<0>{}, Yc, Xc); }
#pragma unroll
  for (int k = 0; k < 4; ++k) cacc = __builtin_amdgcn_mfma_f32_32x32x16_bf16(__builtin_bit_cast(bf16x8, Yc.a[k]), Yc.b[k], cacc, 0, 0, 0);
  STAMP(0, 13);
  const int hh = lane >> 5;
  u16* op = out + tok * DM + 64 * nn + 32 * dtl + 4 * hh;
#pragma unroll
  for (int g = 0; g < 4; ++g)
    *(u32x2*)(op + 8 * g) = (u32x2){pk_bf16(bf2f(res[4 * g]) + rbf(cacc[4 * g]), bf2f(res[4 * g + 1]) + rbf(cacc[4 * g + 1])),
                                    pk_bf16(bf2f(res[4 * g + 2]) + rbf(cacc[4 * g + 2]), bf2f(res[4 * g + 3]) + rbf(cacc[4 * g + 3]))};
  STAMP(0, 14);
  STAMP_FLUSH(0);
}

extern "C" void kernel_launch(void* const* d_in, const int* in_sizes, int n_in, void* d_out, int out_size, void* d_ws, size_t ws_size,
                              hipStream_t stream) {
  (void)in_sizes; (void)n_in; (void)out_size; (void)ws_size;
  (void)hipFuncSetAttribute((const void*)fused_kernel, hipFuncAttributeMaxDynamicSharedMemorySize, K_LDS);
  fused_kernel<<<256, 512, K_LDS, stream>>>((const u16*)d_in[0], (const u16*)d_in[1], (const u16*)d_in[2], (const u16*)d_in[3], (const u16*)d_in[4],
                                            (const u16*)d_in[5], (const u16*)d_in[6], (const u16*)d_in[7], (const u16*)d_in[8],
                                            (const u16*)d_in[10], (const u16*)d_in[11], (const u16*)d_in[12], (char*)d_ws, (u16*)d_out);
}
```

```cpp
#include <hip/hip_runtime.h>
#include <stdint.h>

typedef unsigned short u16;
typedef unsigned int u32;
typedef unsigned long long u64;
typedef float f32x2 __attribute__((ext_vector_type(2)));
typedef float f32x4 __attribute__((ext_vector_type(4)));
typedef float f32x16 __attribute__((ext_vector_type(16)));
typedef unsigned u32x2 __attribute__((ext_vector_type(2)));
typedef unsigned u32x4 __attribute__((ext_vector_type(4)));
typedef short s16x4 __attribute__((ext_vector_type(4)));
typedef short s16x8 __attribute__((ext_vector_type(8)));
typedef __bf16 bf16x8 __attribute__((ext_vector_type(8)));
typedef __bf16 bf16x2 __attribute__((ext_vector_type(2)));
typedef _Float16 f16x8 __attribute__((ext_vector_type(8)));
typedef _Float16 f16x4 __attribute__((ext_vector_type(4)));
typedef _Float16 f16x2 __attribute__((ext_vector_type(2)));
#define DI __device__ __forceinline__
#define LAS __attribute__((address_space(3)))
#define GAS __attribute__((address_space(1)))
typedef GAS unsigned gu32;
typedef GAS unsigned long long gu64;
#define RLX_AGENT __ATOMIC_RELAXED, __HIP_MEMORY_SCOPE_AGENT
#define FENCE() __builtin_amdgcn_sched_barrier(0)

constexpr int SL = 2048, DM = 448, NS = 256, FF = 1792;
constexpr int HS = SL + 64;

constexpr size_t WS_EPOCH = 0;
constexpr size_t WS_GRAN  = 4096;
constexpr size_t WS_CS    = 32768;
constexpr size_t WS_FLA   = 65536;
constexpr size_t WS_FLB   = WS_FLA + 32768;
constexpr size_t WS_XG    = 131072;
constexpr size_t WS_HT    = WS_XG + (size_t)DM * 8192;
constexpr size_t WS_H     = WS_HT + (size_t)DM * HS * 2;
constexpr size_t WS_DBG   = WS_H + (size_t)SL * FF * 2;

#define STAMP_DECL do {} while (0)
#define STAMP(k, i) do {} while (0)
#define STAMP_FLUSH(k) do {} while (0)

DI float bf2f(u32 v) { return __uint_as_float(v << 16); }
DI float bfhi(u32 v) { return __uint_as_float(v & 0xffff0000u); }
DI u32 pk_bf16(float a, float b) { f32x2 v = {a, b}; return __builtin_bit_cast(u32, __builtin_convertvector(v, bf16x2)); }
DI u32 pk_f16(float a, float b) { f32x2 v = {a, b}; return __builtin_bit_cast(u32, __builtin_convertvector(v, f16x2)); }
DI u32 pk2(f32x2 v) { return __builtin_bit_cast(u32, __builtin_convertvector(v, f16x2)); }
DI float rbf(float a) { return __uint_as_float(pk_bf16(a, a) << 16); }

template <int CTRL, int RM> DI float dpp0(float v) { return __int_as_float(__builtin_amdgcn_update_dpp(0, __float_as_int(v), CTRL, RM, 0xf, true)); }
DI float lane63(float v) { return __int_as_float(__builtin_amdgcn_readlane(__float_as_int(v), 63)); }
DI void wave_sum2(float& a, float& b) {
  a += dpp0<0x111, 0xf>(a); b += dpp0<0x111, 0xf>(b); a += dpp0<0x112, 0xf>(a); b += dpp0<0x112, 0xf>(b);
  a += dpp0<0x114, 0xf>(a); b += dpp0<0x114, 0xf>(b); a += dpp0<0x118, 0xf>(a); b += dpp0<0x118, 0xf>(b);
  a += dpp0<0x142, 0xa>(a); b += dpp0<0x142, 0xa>(b); a += dpp0<0x143, 0xc>(a); b += dpp0<0x143, 0xc>(b);
  a = lane63(a); b = lane63(b);
}
DI void wave_max_sum(float& m, float& s) {
  m = fmaxf(m, dpp0<0x111, 0xf>(m)); s += dpp0<0x111, 0xf>(s); m = fmaxf(m, dpp0<0x112, 0xf>(m)); s += dpp0<0x112, 0xf>(s);
  m = fmaxf(m, dpp0<0x114, 0xf>(m)); s += dpp0<0x114, 0xf>(s); m = fmaxf(m, dpp0<0x118, 0xf>(m)); s += dpp0<0x118, 0xf>(s);
  m = fmaxf(m, dpp0<0x142, 0xa>(m)); s += dpp0<0x142, 0xa>(s); m = fmaxf(m, dpp0<0x143, 0xc>(m)); s += dpp0<0x143, 0xc>(s);
  m = lane63(m); s = lane63(s);
}

DI void glds16(const void* g, u32 lds_dst) {
  u32 keep;
  asm volatile("s_mov_b32 %0, m0\n\ts_mov_b32 m0, %2\n\ts_nop 0\n\tglobal_load_lds_dwordx4 %1, off\n\ts_mov_b32 m0, %0"
               : "=&s"(keep) : "v"(g), "s"(lds_dst) : "memory");
}
typedef __amdgpu_buffer_rsrc_t rsrc_t;
DI rsrc_t mkrsrc(const void* p) { return __builtin_amdgcn_make_buffer_rsrc((void*)p, 0, 0x7fffffff, 0x00020000); }
DI void bdma16(rsrc_t rs, u32 voff, u32 soff, u32 lds_dst) {
  asm volatile("s_mov_b32 m0, %0\n\ts_nop 0\n\tbuffer_load_dwordx4 %1, %2, %3 offen lds" :: "s"(lds_dst), "v"(voff), "s"(rs), "s"(soff) : "memory");
}
DI void bdma16_dev(rsrc_t rs, u32 voff, u32 soff, u32 lds_dst) {
  asm volatile("s_mov_b32 m0, %0\n\ts_nop 0\n\tbuffer_load_dwordx4 %1, %2, %3 offen sc1 lds" :: "s"(lds_dst), "v"(voff), "s"(rs), "s"(soff) : "memory");
}
DI void bdma4(rsrc_t rs, u32 voff, u32 soff, u32 lds_dst) {
  asm volatile("s_mov_b32 m0, %0\n\ts_nop 0\n\tbuffer_load_dword %1, %2, %3 offen lds" :: "s"(lds_dst), "v"(voff), "s"(rs), "s"(soff) : "memory");
}
DI u32 lds_addr(const void* p) { return (u32)(size_t)(LAS const char*)p; }
DI s16x8 tr_frag(const char* p0, const char* p1) {
  s16x4 a = __builtin_amdgcn_ds_read_tr16_b64_v4i16((LAS s16x4*)p0);
  s16x4 b = __builtin_amdgcn_ds_read_tr16_b64_v4i16((LAS s16x4*)p1);
  return __builtin_shufflevector(a, b, 0, 1, 2, 3, 4, 5, 6, 7);
}

struct IdxTab { u16 v[2064]; };
constexpr int tap_entry(int l) {
  if (l < 0) return 21 * 32;
  int lam = l;
  if (l >= 256) {
    int sh = l >= 1024 ? 3 : (l >= 512 ? 2 : 1);
    int q = l >> sh, rem = l & ((1 << sh) - 1), half = 1 << (sh - 1);
    if (rem > half || (rem == half && (q & 1))) q += 1;
    lam = q << sh;
  }
  int hq = lam >> 8, lo = lam & 255, c = lo >> 3, r = 0;
  if (hq == 0) r = lo & 7;
  else if (hq == 1) r = 8 + ((lo & 7) >> 1);
  else if (hq == 2) r = 12 + ((lo & 7) >> 2);
  else if (hq == 3) r = 14 + ((lo & 7) >> 2);
  else if (hq < 8) r = 16 + hq - 4;
  else { r = 20; c = 0; }
  return r * 32 + c;
}
constexpr IdxTab make_tab() { IdxTab t{}; for (int y = 0; y < 2064; ++y) t.v[y] = (u16)(2 * tap_entry(2047 - y)); return t; }
__device__ const IdxTab RIDX __attribute__((aligned(16))) = make_tab();

DI float pow2_for(float mx, int target) {
  int e = (int)(__float_as_uint(mx) >> 23);
  int be = 127 + target - (e - 126);
  be = be > 254 ? 254 : (be < 1 ? 1 : be);
  return __uint_as_float((u32)be << 23);
}
DI float pow2_inv(float p) { return __uint_as_float((254u - (__float_as_uint(p) >> 23)) << 23); }

struct CM { f32x2 a, b; };
DI CM mk(f32x2 e) { return {e, (f32x2){-e.y, e.x}}; }
DI f32x2 cmul(f32x2 p, CM m) {
  f32x2 t = __builtin_shufflevector(p, p, 0, 0) * m.a;
  return __builtin_elementwise_fma(__builtin_shufflevector(p, p, 1, 1), m.b, t);
}
DI f32x2 cexp_s(float zr, float zi, float s) {
  float m = __builtin_amdgcn_exp2f(zr * (s * 1.44269504088896341f));
  float a = __builtin_amdgcn_fractf(zi * (s * 0.15915494309189535f));
  return (f32x2){m * __builtin_amdgcn_cosf(a), m * __builtin_amdgcn_sinf(a)};
}
template <int OFF>
DI void put4(u32 base, u32 v0, u32 v1, u32 v2, u32 v3) {
  asm volatile("s_mov_b32 m0, %0\n\ts_nop 0\n\tds_write_addtid_b32 %1 offset:%5\n\tds_write_addtid_b32 %2 offset:%6\n\t"
               "ds_write_addtid_b32 %3 offset:%7\n\tds_write_addtid_b32 %4 offset:%8"
               :: "s"(base), "v"(v0), "v"(v1), "v"(v2), "v"(v3), "n"(OFF), "n"(OFF + 272), "n"(OFF + 544), "n"(OFF + 816) : "memory");
}

template <int V> struct IC { static constexpr int value = V; };
template <int N> DI void wait_vm() { asm volatile("s_waitcnt vmcnt(%0)" :: "n"(N) : "memory"); }
DI void glds4(const void* g, u32 lds_dst) {
  u32 keep;
  asm volatile("s_mov_b32 %0, m0\n\ts_mov_b32 m0, %2\n\ts_nop 0\n\tglobal_load_lds_dword %1, off\n\ts_mov_b32 m0, %0"
               : "=&s"(keep) : "v"(g), "s"(lds_dst) : "memory");
}
DI void bar() { __builtin_amdgcn_s_barrier(); }

constexpr int A_T1 = 0, A_T2 = 8192;
constexpr int A_SCR = 25088, A_SCRW = 32 * 272 + 21 * 272;
constexpr int A_CP = 0, A_CPS = 4160, A_CPC = 8 * A_CPS;
constexpr int A_EX = 2 * A_CPC, A_XX = A_EX + 16384, A_HO = A_XX + 10240, A_DUM = A_HO + 10240;
constexpr int A_UU = A_SCR + 8 * A_SCRW, A_UUC = 95 * 80;
constexpr int A_TB = A_UU + 2 * A_UUC, A_TBC = 22 * 32 * 2;
constexpr int A_SW = A_TB + 2 * A_TBC;
constexpr int A_LDS = A_SW + 64;
static_assert(A_LDS <= 163840 && A_DUM + 4096 <= A_UU && (A_SCR % 16) == 0 && (A_SCRW % 16) == 0 && (A_UU % 16) == 0, "lds");

constexpr int B_G = 0, B_U = 8192, B_H = 16384, B_STG = 24576, B_NB = 6, B_NST = 14;
constexpr int B_CS = B_NB * B_STG, B_ST = B_CS + 1024, B_SR = 3 * B_STG  , B_CNT = 163840 - 16, B_LDS = B_ST + 1024;
static_assert(B_LDS <= 163840, "lds");

constexpr int C_STG = 16384, C_NB = 10, C_NST = 28, C_LDS = C_NB * C_STG;

constexpr int K_LDS = 163840;
constexpr int CODE_WARM_KB = 32;
static_assert(A_LDS <= K_LDS - 16 && B_LDS <= K_LDS && C_LDS <= K_LDS, "lds");

__global__ __launch_bounds__(512, 2) void fused_kernel(const u16* __restrict__ x, const u16* __restrict__ Are, const u16* __restrict__ Aim,
    const u16* __restrict__ Bre, const u16* __restrict__ Bim, const u16* __restrict__ Cre, const u16* __restrict__ Cim,
    const u16* __restrict__ logdt, const u16* __restrict__ Dskip, const u16* __restrict__ wg, const u16* __restrict__ wu,
    const u16* __restrict__ wd, char* ws, u16* __restrict__ out) {
  extern __shared__ __attribute__((aligned(16))) char lds[];
  char* dbg = ws + WS_DBG; (void)dbg;
  STAMP_DECL;
  const int tid = threadIdx.x, lane = tid & 63;
  const int wv = __builtin_amdgcn_readfirstlane(tid >> 6);
  const int b = blockIdx.x, xcd = b & 7, j = b >> 3;
  const bool worker = j < 28;
  STAMP(0, 0);
  gu32* epoch_p = (gu32*)(ws + WS_EPOCH);
  gu64* gran = (gu64*)(ws + WS_GRAN);
  const u32 tag = __hip_atomic_load(epoch_p, RLX_AGENT) + 1u;
  if (tid == 0) *(u32*)(lds + B_CNT) = 0u;

  const int w = worker ? xcd * 28 + j : 0, c0 = 2 * w;
  const int trow = 8 * b + wv;
  const u32x4 xr = *(const u32x4*)(x + trow * DM + (lane < 56 ? lane : 0) * 8);
  const u32x4 xt = *(const u32x4*)(x + (128 * (w & 15) + (tid >> 2)) * DM + 32 * (w >> 4) + (tid & 3) * 8);
  const int ch = tid >> 8, n = tid & 255;
  const int idx = (c0 + ch) * NS + n;
  const u32 p_ldt = logdt[c0 + ch];
  const u32 p_are = Are[idx], p_aim = Aim[idx], p_bre = Bre[idx], p_bim = Bim[idx], p_cre = Cre[idx], p_cim = Cim[idx];
  const u32 p_d01 = *(const u32*)(Dskip + c0);
  const u32x4 ix0 = *(const u32x4*)(RIDX.v + 8 * (tid & 255)), ix1 = *(const u32x4*)(RIDX.v + 8 * (tid & 255) + 8);
  {
    const float piv = __uint_as_float((u32)__builtin_amdgcn_readfirstlane(xr[0]) << 16);
    float s = 0.f, q = 0.f, s2 = 0.f, q2 = 0.f;
#pragma unroll
    for (int i = 0; i < 4; ++i) {
      const float lo = bf2f(xr[i] & 0xffffu) - piv, hi = bfhi(xr[i]) - piv;
      s += lo; s2 += hi; q = fmaf(lo, lo, q); q2 = fmaf(hi, hi, q2);
    }
    s = lane < 56 ? s + s2 : 0.f; q = lane < 56 ? q + q2 : 0.f;
    wave_sum2(s, q);
    const float ms = s * (1.f / DM);
    float var = q * (1.f / DM) - ms * ms; var = var < 0.f ? 0.f : var;
    const float sd = rbf(__builtin_sqrtf(rbf(rbf(var) + 1e-5f)));
    if (lane == 0) __hip_atomic_store(gran + trow, ((u64)tag << 32) | pk_bf16(piv + ms, sd), RLX_AGENT);
  }
  STAMP(0, 1);
  if (!worker) {
    {
      u64 pc;
      asm volatile("s_getpc_b64 %0" : "=s"(pc));
      const char* code = (const char*)(pc & ~(u64)63);
      u32x4 cw[CODE_WARM_KB / 8];
#pragma unroll
      for (int i = 0; i < CODE_WARM_KB / 8; ++i) cw[i] = *(const GAS u32x4*)(code + (i * 512 + tid) * 16);
      u32 acc = 0;
#pragma unroll
      for (int i = 0; i < CODE_WARM_KB / 8; ++i) acc |= cw[i][0] & cw[i][1] & cw[i][2] & cw[i][3];
      asm volatile("" :: "v"(acc));
    }
    const int s = xcd * 4 + (j - 28);
    const u16* W = (s >> 4) ? wu : wg;
    const int colb = (s & 15) * 112;
    float a[8] = {0, 0, 0, 0, 0, 0, 0, 0};
    const int cc = tid % 14, rg = tid / 14;
    u32 warm = 0;
    {
      u32x4 wv4[7];
#pragma unroll
      for (int i = 0; i < 7; ++i) { const int c = tid + 512 * i; wv4[i] = *(const u32x4*)(wd + (size_t)s * 25088 + (c < 3136 ? c : 0) * 8); }
#pragma unroll
      for (int i = 0; i < 7; ++i) warm |= wv4[i][0] & wv4[i][1] & wv4[i][2] & wv4[i][3];
      asm volatile("" :: "v"(warm));
    }
    if (tid < 448) {
      u32x4 v[14];
#pragma unroll
      for (int r = 0; r < 14; ++r) v[r] = *(const u32x4*)(W + (rg * 14 + r) * FF + colb + cc * 8);
#pragma unroll
      for (int r = 0; r < 14; ++r)
#pragma unroll
        for (int i = 0; i < 4; ++i) { a[2 * i] += bf2f(v[r][i] & 0xffffu); a[2 * i + 1] += bfhi(v[r][i]); }
      float* red = (float*)lds;
#pragma unroll
      for (int i = 0; i < 8; ++i) red[rg * 112 + cc * 8 + i] = a[i];
    }
    __syncthreads();
    if (tid < 112) {
      float t = 0.f;
      for (int r = 0; r < 32; ++r) t += ((float*)lds)[r * 112 + tid];
      __hip_atomic_store((gu64*)(ws + WS_CS) + (s >> 4) * FF + colb + tid, ((u64)tag << 32) | __float_as_uint(t), RLX_AGENT);
    }
    STAMP_FLUSH(0);
    return;
  }
  *(u32x4*)(lds + A_T1 + tid * 16) = xt;
  const float dt = rbf(__builtin_amdgcn_exp2f(bf2f(p_ldt) * 1.44269504088896341f));
  const float zr = dt * bf2f(p_are), zi = dt * bf2f(p_aim);
  const f32x2 E8 = cexp_s(zr, zi, 8.f);
  const f32x2 E1 = cexp_s(zr, -zi, 1.f), F = cexp_s(zr, -zi, 256.f);
  {
    const int grp = lane >> 4, q = (lane & 15) >> 2, p = lane & 3;
#pragma unroll
    for (int i = 0; i < 2; ++i) {
      const int tb = 16 * wv + 8 * i + 4 * (grp >> 1);
      const s16x4 v = __builtin_amdgcn_ds_read_tr16_b64_v4i16((LAS s16x4*)(lds + A_T1 + (tb + q) * 64 + (grp & 1) * 32 + p * 8));
      const u32x2 vw = __builtin_bit_cast(u32x2, v);
      *(u32x4*)(lds + A_T2 + (16 * (grp & 1) + (lane & 15)) * 528 + tb * 4) = (u32x4){vw[0], tag, vw[1], tag};
    }
  }
  const u32 scb = lds_addr(lds) + A_SCR + wv * A_SCRW;
  const CM M8 = mk(E8);
  f32x2 p0 = {16384.f, 0.f}, p1 = cmul(p0, M8), p2 = cmul(p1, M8), p3 = cmul(p2, M8);
  const CM M32 = mk(cmul(p2, mk(p2 * (1.f / 16384.f))) * (1.f / 16384.f));
  __syncthreads();
  {
    __amdgpu_buffer_rsrc_t rs = __builtin_amdgcn_make_buffer_rsrc((void*)(ws + WS_XG), 0, DM * 8192, 0x00020000);
#pragma unroll
    for (int k = 0; k < 2; ++k) {
      const int id = tid + 512 * k, c = id >> 5, pos = id & 31;
      const u32x4 v = *(const u32x4*)(lds + A_T2 + c * 528 + pos * 16);
      __builtin_amdgcn_raw_buffer_store_b128(v, rs, (32 * (w >> 4) + c) * 8192 + (w & 15) * 512 + pos * 16, 0, 16);
    }
  }
  STAMP(0, 2);
  {
    const float br = dt * bf2f(p_bre), bi = dt * bf2f(p_bim);
    const float cr = bf2f(p_cre), ci = bf2f(p_cim);
    const float CBr = cr * br - ci * bi, CBi = cr * bi + ci * br;
    float mx = fmaxf(fabsf(CBr), fabsf(CBi)), sabs = fabsf(CBr) + fabsf(CBi);
    wave_max_sum(mx, sabs);
    const float scA = pow2_for(mx, 14);
    if (lane == 0) { ((float*)(lds + A_SW))[wv] = pow2_inv(scA) * (1.f / 16384.f); ((float*)(lds + A_SW))[8 + wv] = sabs; }
#define COL4(R) { put4<(R) * 272>(scb, pk2(p0), pk2(p1), pk2(p2), pk2(p3)); \
                  if ((R) < 28) { p0 = cmul(p0, M32); p1 = cmul(p1, M32); p2 = cmul(p2, M32); p3 = cmul(p3, M32); } }
    COL4(0) COL4(4) COL4(8) COL4(12) COL4(16) COL4(20) COL4(24) COL4(28)
#undef COL4
    const CM M1 = mk(E1), MF = mk(F);
    const f32x2 E2 = cmul(E1, M1);
    const CM M2 = mk(E2), M4 = mk(cmul(E2, M2));
    const f32x2 X0 = {CBr * scA, -CBi * scA};
    const f32x2 X1 = cmul(X0, M1), G1 = cmul(X0, MF);
    const f32x2 X2 = cmul(X0, M2), X3 = cmul(X1, M2), G2 = cmul(G1, MF), Y1 = cmul(G1, M2);
    put4<(32 + 0) * 272>(scb, pk2(X0), pk2(X1), pk2(X2), pk2(X3));
    const f32x2 X4 = cmul(X0, M4), X5 = cmul(X1, M4), X6 = cmul(X2, M4), X7 = cmul(X3, M4);
    const f32x2 G3 = cmul(G2, MF), Y2 = cmul(G1, M4), Y3 = cmul(Y1, M4);
    put4<(32 + 4) * 272>(scb, pk2(X4), pk2(X5), pk2(X6), pk2(X7));
    put4<(32 + 8) * 272>(scb, pk2(G1), pk2(Y1), pk2(Y2), pk2(Y3));
    const f32x2 G4 = cmul(G3, MF), Z2 = cmul(G2, M4), Z3 = cmul(G3, M4);
    put4<(32 + 12) * 272>(scb, pk2(G2), pk2(Z2), pk2(G3), pk2(Z3));
    const CM MF2 = mk(cmul(F, MF));
    const f32x2 G5 = cmul(G3, MF2), G6 = cmul(G4, MF2);
    const f32x2 G7 = cmul(G5, MF2), G8 = cmul(G6, MF2);
    put4<(32 + 16) * 272>(scb, pk2(G4), pk2(G5), pk2(G6), pk2(G7));
    {
      const u32 g8 = pk2(G8);
      asm volatile("s_mov_b32 m0, %0\n\ts_nop 0\n\tds_write_addtid_b32 %1 offset:%2" :: "s"(scb), "v"(g8), "n"((32 + 20) * 272) : "memory");
    }
    char* scr = lds + A_SCR + wv * A_SCRW;
    const char* fr = scr + (lane & 31) * 272 + (lane >> 5) * 16;
    f16x8 bf[8], af[8];
#pragma unroll
    for (int ks = 0; ks < 8; ++ks) { bf[ks] = *(const f16x8*)(fr + ks * 32); af[ks] = *(const f16x8*)(fr + 32 * 272 + ks * 32); }
    f32x16 acc0 = {}, acc1 = {};
#pragma unroll
    for (int ks = 0; ks < 8; ks += 2) {
      acc0 = __builtin_amdgcn_mfma_f32_32x32x16_f16(af[ks], bf[ks], acc0, 0, 0, 0);
      acc1 = __builtin_amdgcn_mfma_f32_32x32x16_f16(af[ks + 1], bf[ks + 1], acc1, 0, 0, 0);
    }
#pragma unroll
    for (int r = 0; r < 12; ++r) {
      int row = (r & 3) + 8 * (r >> 2) + 4 * (lane >> 5);
      *(float*)(scr + row * 128 + (lane & 31) * 4) = acc0[r] + acc1[r];
    }
  }
  STAMP(0, 3);
  const __amdgpu_buffer_rsrc_t rs_g = __builtin_amdgcn_make_buffer_rsrc((void*)(ws + WS_GRAN), 0, SL * 8, 0x00020000);
  const __amdgpu_buffer_rsrc_t rs_x = __builtin_amdgcn_make_buffer_rsrc((void*)(ws + WS_XG + (size_t)c0 * 8192), 0, 2 * 8192, 0x00020000);
  u32x4 g[4], xg[4];
  const int t8 = tid & 255;
  auto fetch = [&]() {
#pragma unroll
    for (int i = 0; i < 4; ++i) g[i] = __builtin_amdgcn_raw_buffer_load_b128(rs_g, t8 * 64 + i * 16, 0, 16);
#pragma unroll
    for (int i = 0; i < 4; ++i) xg[i] = __builtin_amdgcn_raw_buffer_load_b128(rs_x, (i >> 1) * 8192 + t8 * 32 + (i & 1) * 16, 0, 16);
  };
  if (wv < 4) fetch();
  asm volatile("s_waitcnt lgkmcnt(0)" ::: "memory");
  bar();
  float kinv0, kinv1;
  {
    const float* sw = (const float*)(lds + A_SW);
    const float k0 = pow2_for(sw[8] + sw[9] + sw[10] + sw[11], 15), k1 = pow2_for(sw[12] + sw[13] + sw[14] + sw[15], 15);
    kinv0 = pow2_inv(k0); kinv1 = pow2_inv(k1);
    const float ks = ch ? k1 : k0;
    const float i0 = sw[4 * ch] * ks, i1 = sw[4 * ch + 1] * ks, i2 = sw[4 * ch + 2] * ks, i3 = sw[4 * ch + 3] * ks;
    const char* p = lds + A_SCR + (4 * ch) * A_SCRW + (tid & 255) * 4;
    float v[3][4];
#pragma unroll
    for (int it = 0; it < 3; ++it)
#pragma unroll
      for (int k = 0; k < 4; ++k) v[it][k] = *(const float*)(p + it * 1024 + k * A_SCRW);
#pragma unroll
    for (int it = 0; it < 3; ++it) {
      const int e = (tid & 255) + 256 * it;
      const float t = v[it][0] * i0 + v[it][1] * i1 + v[it][2] * i2 + v[it][3] * i3;
      if (it < 2 || e < 672) *(_Float16*)(lds + A_TB + ch * A_TBC + e * 2) = (_Float16)t;
    }
    if (tid < 8) { int cc = tid >> 2; *(u32x4*)(lds + A_TB + cc * A_TBC + 21 * 64 + (tid & 3) * 16) = (u32x4){0, 0, 0, 0}; }
    if (tid < 310) { int cc = tid / 155, r = tid % 155; *(u32x4*)(lds + A_UU + cc * A_UUC + r * 16) = (u32x4){0, 0, 0, 0}; }
  }
  asm volatile("s_waitcnt lgkmcnt(0)" ::: "memory");
  bar();
  if (wv >= 4) {
#pragma unroll
   for (int ch = 0; ch < 2; ++ch) {
    const char* tb = lds + A_TB + ch * A_TBC;
    u32 P[8];
#pragma unroll
    for (int k = 0; k < 8; ++k) {
      u32 iw = k < 4 ? ix0[k] : ix1[k - 4];
      u32 lo = *(const u16*)(tb + (iw & 0xffffu));
      u32 hi = (k < 7) ? *(const u16*)(tb + (iw >> 16)) : 0u;
      P[k] = lo | (hi << 16);
    }
    u32 Q[7];
#pragma unroll
    for (int k = 0; k < 7; ++k) Q[k] = __builtin_amdgcn_alignbit(P[k + 1], P[k], 16);
    char* cp = lds + A_CP + ch * A_CPC + (tid & 255) * 16;
#pragma unroll
    for (int s = 0; s < 8; ++s) {
      const int off = 7 - s;
      u32x4 v;
      if ((off & 1) == 0) v = (u32x4){P[off / 2], P[off / 2 + 1], P[off / 2 + 2], P[off / 2 + 3]};
      else v = (u32x4){Q[off / 2], Q[off / 2 + 1], Q[off / 2 + 2], Q[off / 2 + 3]};
      *(u32x4*)(cp + s * A_CPS) = v;
    }
   }
    if (t8 < 48) { int cc = t8 / 24, r = t8 % 24; *(u32x4*)(lds + A_CP + cc * A_CPC + (r / 3) * A_CPS + (256 + r % 3) * 16) = (u32x4){0, 0, 0, 0}; }
    STAMP(0, 4);
  } else {
    for (int spin = 0; spin < (1 << 16); ++spin) {
      u32 bad = 0;
#pragma unroll
      for (int i = 0; i < 4; ++i) bad |= (g[i][1] ^ tag) | (g[i][3] ^ tag) | (xg[i][1] ^ tag) | (xg[i][3] ^ tag);
      if (__all(bad == 0)) break;
      __builtin_amdgcn_s_sleep(2);
      fetch();
    }
    STAMP(0, 4);
    float uu[2][8];
#pragma unroll
    for (int i = 0; i < 8; ++i) {
      const u32 st = g[i >> 1][(i & 1) * 2];
      const float m = bf2f(st & 0xffffu), rs_ = __builtin_amdgcn_rcpf(bfhi(st));
#pragma unroll
      for (int c = 0; c < 2; ++c) {
        const u32 xw = xg[2 * c + (i >> 2)][(i & 2)];
        const float xv = (i & 1) ? bfhi(xw) : bf2f(xw & 0xffffu);
        uu[c][i] = rbf(rbf(xv - m) * rs_);
      }
    }
#pragma unroll
    for (int c = 0; c < 2; ++c) {
      *(u32x4*)(lds + A_UU + c * A_UUC + ((tid >> 2) + 31) * 80 + (tid & 3) * 16) =
          (u32x4){pk_f16(uu[c][0], uu[c][1]), pk_f16(uu[c][2], uu[c][3]), pk_f16(uu[c][4], uu[c][5]), pk_f16(uu[c][6], uu[c][7])};
      *(u32x4*)(lds + A_XX + c * 5120 + (tid >> 2) * 80 + (tid & 3) * 16) = (u32x4){xg[2 * c][0], xg[2 * c][2], xg[2 * c + 1][0], xg[2 * c + 1][2]};
    }
  }
  STAMP(0, 5);
  asm volatile("s_waitcnt lgkmcnt(0)" ::: "memory");
  bar();
  if (b == 0 && tid == 0) __hip_atomic_store(epoch_p, tag, RLX_AGENT);
  const int cw = (wv >> 1) & 1, hf = wv & 1;
  const int q = lane & 31, h = lane >> 5;
  f32x16 keep;
  if (wv < 4) {
    const int a = q >> 3, s = q & 7;
    const char* abase = lds + A_CP + cw * A_CPC + s * A_CPS + 4080 - 16 * a + 16 * h - 32 * (32 * hf + 30) - 2048;
    const char* bbase = lds + A_UU + cw * A_UUC + (q + 31 - (16 * hf + 15)) * 80 + 16 * h;
    f32x16 y0 = {}, y1 = {};
    f16x8 fa[2][8];
    auto ld = [&](int i, f16x8 (&f)[8]) {
#pragma unroll
      for (int kk = 0; kk < 2; ++kk) {
        const int erel = 30 - (2 * i - kk);
        f[4 * kk] = *(const f16x8*)(abase + 2048 + 32 * erel);
        f[4 * kk + 1] = *(const f16x8*)(bbase + (15 - i) * 80 + kk * 32);
        f[4 * kk + 2] = *(const f16x8*)(bbase + (15 - i + 32) * 80 + kk * 32);
        f[4 * kk + 3] = *(const f16x8*)(abase + 32 * erel);
      }
    };
    ld(0, fa[0]);
#pragma unroll
    for (int i = 0; i < 16; ++i) {
      const f16x8 (&f)[8] = fa[i & 1];
      f16x8 (&g)[8] = fa[(i + 1) & 1];
#pragma unroll
      for (int kk = 0; kk < 2; ++kk) {
        const int erel = 30 - (2 * (i + 1) - kk);
        y0 = __builtin_amdgcn_mfma_f32_32x32x16_f16(f[4 * kk], f[4 * kk + 1], y0, 0, 0, 0);
        FENCE();
        y1 = __builtin_amdgcn_mfma_f32_32x32x16_f16(f[4 * kk], f[4 * kk + 2], y1, 0, 0, 0);
        if (i + 1 < 16) { g[4 * kk] = *(const f16x8*)(abase + 2048 + 32 * erel); g[4 * kk + 2] = *(const f16x8*)(bbase + (15 - i - 1 + 32) * 80 + kk * 32); }
        FENCE();
        y1 = __builtin_amdgcn_mfma_f32_32x32x16_f16(f[4 * kk + 3], f[4 * kk + 1], y1, 0, 0, 0);
        if (i + 1 < 16) { g[4 * kk + 1] = *(const f16x8*)(bbase + (15 - i - 1) * 80 + kk * 32); g[4 * kk + 3] = *(const f16x8*)(abase + 32 * erel); }
        FENCE();
      }
    }
    const f32x16 give = hf ? y0 : y1;
    keep = hf ? y1 : y0;
    char* pp = lds + A_EX + wv * 4096 + lane * 16;
#pragma unroll
    for (int g = 0; g < 4; ++g) *(f32x4*)(pp + g * 1024) = (f32x4){give[4 * g], give[4 * g + 1], give[4 * g + 2], give[4 * g + 3]};
  }
  STAMP(0, 6);
  asm volatile("s_waitcnt lgkmcnt(0)" ::: "memory");
  bar();
  const int m = 4 * (xcd >> 1) + j / 7, nb = 7 * (xcd & 1) + j % 7;
  const int lw = wv & 3;
  const int brow = 4 * lw + (lane >> 4), bcp = lane & 15;
  const rsrc_t rg = mkrsrc(wg + 128 * nb), ru = mkrsrc(wu + 128 * nb), rh = mkrsrc((const u16*)(ws + WS_HT) + 128 * m);
  const u32 vg = (brow * FF + (bcp ^ ((brow & 3) << 2)) * 8) * 2, vu = (brow * FF + (bcp ^ (((brow & 3) << 2) ^ 2)) * 8) * 2;
  const u32 vh = (brow * HS + (bcp ^ ((brow & 3) << 2)) * 8) * 2;
  const u32 bdl = lds_addr(lds) + lw * 1024;
  auto issueW = [&](int s, int slot) {
    const u32 d = bdl + slot * B_STG, sw = s * 32 * FF * 2;
    bdma16(rg, vg, sw, d + B_G); bdma16(rg, vg, sw + 16 * FF * 2, d + B_G + 4096);
    bdma16(ru, vu, sw, d + B_U); bdma16(ru, vu, sw + 16 * FF * 2, d + B_U + 4096);
  };
  auto issueH = [&](int s, int slot) {
    const u32 d = bdl + slot * B_STG, sh = s * 32 * HS * 2;
    bdma16_dev(rh, vh, sh, d + B_H); bdma16_dev(rh, vh, sh + 16 * HS * 2, d + B_H + 4096);
  };
  if (wv >= 4) { issueW(0, 0); issueW(1, 1); issueW(2, 2); }
  if (wv < 4) {
    const char* pp = lds + A_EX + (wv ^ 1) * 4096 + lane * 16;
    const float ki = cw ? kinv1 : kinv0, dk = cw ? bfhi(p_d01) : bf2f(p_d01 & 0xffffu);
    const int row = 32 * hf + q;
#pragma unroll
    for (int g = 0; g < 4; ++g) {
      const f32x4 o = *(const f32x4*)(pp + g * 1024);
      const f16x4 u4 = *(const f16x4*)(lds + A_UU + cw * A_UUC + (row + 31) * 80 + g * 16 + h * 8);
      const u32x2 x2 = *(const u32x2*)(lds + A_XX + cw * 5120 + row * 80 + g * 16 + h * 8);
      float hv[4];
#pragma unroll
      for (int i = 0; i < 4; ++i) {
        const u32 xw = x2[i >> 1];
        const float xv = (i & 1) ? bfhi(xw) : bf2f(xw & 0xffffu);
        hv[i] = xv + rbf(rbf((keep[4 * g + i] + o[i]) * ki) + rbf((float)u4[i] * dk));
      }
      *(u32x2*)(lds + A_HO + cw * 5120 + row * 80 + g * 16 + h * 8) = (u32x2){pk_bf16(hv[0], hv[1]), pk_bf16(hv[2], hv[3])};
    }
  }
  asm volatile("s_waitcnt lgkmcnt(0)" ::: "memory");
  bar();
  if (wv < 4) {
#pragma unroll
    for (int c = 0; c < 2; ++c) {
      const u32x4 v = *(const u32x4*)(lds + A_HO + c * 5120 + (t8 >> 2) * 80 + (t8 & 3) * 16);
      __builtin_amdgcn_raw_buffer_store_b128(v, mkrsrc(ws + WS_HT), ((c0 + c) * HS + 8 * t8) * 2, 0, 16);
    }
    asm volatile("s_waitcnt vmcnt(0)" ::: "memory");
  } else issueW(3, 3);
  bar();
  if (tid == 0) __hip_atomic_store((gu32*)(ws + WS_FLA) + w * 32, tag, RLX_AGENT);
  STAMP(0, 7);
  if (wv >= 4) {
    if (lw >= 2) {
      const rsrc_t rc = mkrsrc(ws + WS_CS + ((lw - 2) * FF + 128 * nb) * 8);
      u32x4 cg;
      for (int spin = 0; spin < (1 << 16); ++spin) {
        cg = __builtin_amdgcn_raw_buffer_load_b128(rc, lane * 16, 0, 16);
        if (__all(cg[1] == tag && cg[3] == tag)) break;
        __builtin_amdgcn_s_sleep(2);
      }
      *(u32x2*)(lds + B_CS + (lw - 2) * 512 + lane * 8) = (u32x2){cg[0], cg[2]};
    }
    {
      gu32* fl = (gu32*)(ws + WS_FLA) + (lw * 56 + (lane < 56 ? lane : 0)) * 32;
      for (int spin = 0; spin < (1 << 16); ++spin) {
        if (__all(__hip_atomic_load(fl, RLX_AGENT) == tag)) break;
        __builtin_amdgcn_s_sleep(2);
      }
      LAS u32* cnt = (LAS u32*)(lds + B_CNT);
      if (lane == 0) __hip_atomic_fetch_add(cnt, 1u, __ATOMIC_RELAXED, __HIP_MEMORY_SCOPE_WORKGROUP);
      for (int spin = 0; spin < (1 << 20); ++spin) {
        if (__hip_atomic_load(cnt, __ATOMIC_RELAXED, __HIP_MEMORY_SCOPE_WORKGROUP) >= 4u) break;
        __builtin_amdgcn_s_sleep(1);
      }
    }
    STAMP(0, 8);
    issueH(0, 0); issueH(1, 1); issueH(2, 2); issueH(3, 3);
    int slot = 4, sn = 4;
    auto issue = [&]() { issueH(sn, slot); issueW(sn, slot); ++sn; slot = slot == B_NB - 1 ? 0 : slot + 1; };
    const int soff = B_H + (lw + 4 * (lane >> 4)) * 256 + (((lane & 15) ^ (lw << 2)) * 16);
    float sa[8], sq[8];
#pragma unroll
    for (int i = 0; i < 8; ++i) { sa[i] = 0.f; sq[i] = 0.f; }
    int buf = 0;
    auto consume = [&]() {
      const char* st = lds + buf * B_STG + soff;
      const u32x4 v0 = *(const u32x4*)st, v1 = *(const u32x4*)(st + 16 * 256);
#pragma unroll
      for (int r = 0; r < 2; ++r)
#pragma unroll
        for (int i = 0; i < 4; ++i) {
          const u32 wi = r ? v1[i] : v0[i];
          const float lo = bf2f(wi & 0xffffu), hi = bfhi(wi);
          asm("v_add_f32 %0, %0, %1" : "+v"(sa[2 * i]) : "v"(lo)); asm("v_add_f32 %0, %0, %1" : "+v"(sa[2 * i + 1]) : "v"(hi));
          asm("v_fmac_f32 %0, %1, %1" : "+v"(sq[2 * i]) : "v"(lo)); asm("v_fmac_f32 %0, %1, %1" : "+v"(sq[2 * i + 1]) : "v"(hi));
        }
      buf = buf == B_NB - 1 ? 0 : buf + 1;
    };
    wait_vm<6>(); bar(); issue(); consume();
    wait_vm<10>(); bar(); issue(); consume();
    wait_vm<14>(); bar(); issue(); consume();
#pragma unroll 1
    for (int s = 3; s < 10; ++s) { wait_vm<6 * 3>(); bar(); issue(); consume(); }
    wait_vm<6 * 3>(); bar(); consume();
    wait_vm<6 * 2>(); bar(); consume();
    wait_vm<6 * 1>(); bar(); consume();
    wait_vm<0>(); bar(); consume();
    STAMP(0, 9);
    {
      f32x4* sr = (f32x4*)(lds + B_SR) + ((lw * 4 + (lane >> 4)) * 16 + (lane & 15)) * 4;
#pragma unroll
      for (int i = 0; i < 4; ++i) sr[i] = (f32x4){sa[2 * i], sq[2 * i], sa[2 * i + 1], sq[2 * i + 1]};
    }
    asm volatile("s_waitcnt lgkmcnt(0)" ::: "memory");
    bar();
    if (lw < 2) {
      const int t = lw * 64 + lane;
      const f32x2* sr = (const f32x2*)(lds + B_SR) + t;
      f32x2 tot = {0.f, 0.f};
#pragma unroll
      for (int g = 0; g < 16; ++g) tot += sr[g * 128];
      const float mean = tot[0] * (1.f / DM);
      float var = tot[1] * (1.f / DM) - mean * mean; var = var < 0.f ? 0.f : var;
      const float sd = rbf(__builtin_sqrtf(rbf(rbf(var) + 1e-5f)));
      const float rs = __builtin_amdgcn_rcpf(sd);
      ((f32x2*)(lds + B_ST))[t] = (f32x2){-rbf(mean), rs * rs};
    }
    asm volatile("s_waitcnt lgkmcnt(0)" ::: "memory");
    bar();
  } else {
  const int tp = wv & 1, ag = wv >> 1;
  const int grp = lane >> 4, q = (lane & 15) >> 2, p = lane & 3, h = grp >> 1;
  const int aoff = ((grp & 1) ? B_U : B_G) + (8 * h + q) * 256 + (p & 1) * 8;
  const int akey = (q << 2) ^ ((grp & 1) << 1);
  const int boff = B_H + (8 * h + q) * 256 + (p & 1) * 8;
  int ao[4], bo[2];
#pragma unroll
  for (int i = 0; i < 4; ++i) ao[i] = aoff + (((2 * (4 * ag + i) + (p >> 1)) ^ akey) * 16);
#pragma unroll
  for (int t = 0; t < 2; ++t) bo[t] = boff + (((4 * (2 * tp + t) + 2 * (grp & 1) + (p >> 1)) ^ (q << 2)) * 16);
  f32x16 acc[4][2];
#pragma unroll
  for (int i = 0; i < 4; ++i) { acc[i][0] = (f32x16){}; acc[i][1] = (f32x16){}; }
  struct KF { s16x8 a[4], b[2]; };
  KF X, Y;
  auto kstep = [&](auto havecur, const KF& cur, KF& nxt, const char* st) {
#pragma unroll
    for (int i = 0; i < 4; ++i) {
#pragma unroll
      for (int t = 0; t < 2; ++t) {
        if (decltype(havecur)::value)
          acc[i][t] = __builtin_amdgcn_mfma_f32_32x32x16_bf16(__builtin_bit_cast(bf16x8, cur.a[i]), __builtin_bit_cast(bf16x8, cur.b[t]), acc[i][t], 0, 0, 0);
        const int g = 2 * i + t;
        if (g < 2) nxt.b[g] = tr_frag(st + bo[g], st + bo[g] + 4 * 256);
        else if (g < 6) nxt.a[g - 2] = tr_frag(st + ao[g - 2], st + ao[g - 2] + 4 * 256);
        FENCE();
      }
    }
  };
  int buf = 0;
  auto stage = [&](auto first) {
    bar();
    const char* st = lds + buf * B_STG;
    kstep(IC<!decltype(first)::value>{}, Y, X, st);
    kstep(IC<1>{}, X, Y, st + 16 * 256);
    buf = buf == B_NB - 1 ? 0 : buf + 1;
  };
  stage(IC<1>{});
#pragma unroll 1
  for (int s = 1; s < B_NST; ++s) stage(IC<0>{});
  STAMP(0, 9);
  bar();
#pragma unroll
  for (int i = 0; i < 4; ++i)
#pragma unroll
    for (int t = 0; t < 2; ++t)
      acc[i][t] = __builtin_amdgcn_mfma_f32_32x32x16_bf16(__builtin_bit_cast(bf16x8, Y.a[i]), __builtin_bit_cast(bf16x8, Y.b[t]), acc[i][t], 0, 0, 0);
  bar();
  const float* cs = (const float*)(lds + B_CS);
  const int hh = lane >> 5;
  const rsrc_t rH = mkrsrc(ws + WS_H);
  f32x2 st2[2];
#pragma unroll
  for (int t = 0; t < 2; ++t) st2[t] = ((const f32x2*)(lds + B_ST))[(2 * tp + t) * 32 + (lane & 31)];
#pragma unroll
  for (int i = 0; i < 4; ++i) {
    const int f0 = 16 * (4 * ag + i) + 4 * hh;
    const f32x4 g0 = *(const f32x4*)(cs + f0), g1 = *(const f32x4*)(cs + f0 + 8);
    const f32x4 u0 = *(const f32x4*)(cs + 128 + f0), u1 = *(const f32x4*)(cs + 128 + f0 + 8);
#pragma unroll
    for (int t = 0; t < 2; ++t) {
      const f32x2 nm = {st2[t][0], st2[t][0]}, r2 = {st2[t][1], st2[t][1]};
      u32x4 o;
#pragma unroll
      for (int r = 0; r < 4; ++r) {
        const f32x4 cg = r < 2 ? g0 : g1, cu = r < 2 ? u0 : u1;
        const int e = 2 * (r & 1);
        const f32x2 g = __builtin_elementwise_fma(nm, (f32x2){cg[e], cg[e + 1]}, (f32x2){acc[i][t][2 * r], acc[i][t][2 * r + 1]});
        const f32x2 u = __builtin_elementwise_fma(nm, (f32x2){cu[e], cu[e + 1]}, (f32x2){acc[i][t][8 + 2 * r], acc[i][t][8 + 2 * r + 1]});
        const f32x2 hv = (g * r2) * u;
        o[r] = pk_bf16(hv[0], hv[1]);
      }
      __builtin_amdgcn_raw_buffer_store_b128(o, rH, (((4 * m + 2 * tp + t) * 112 + 8 * nb + 4 * ag + i) * 64 + lane) * 16, 0, 16);
    }
  }
  STAMP(0, 10);
  asm volatile("s_waitcnt vmcnt(0)" ::: "memory");
  }
  const int mm = 4 * xcd + j / 7, nn = j % 7;
  const rsrc_t rHc = mkrsrc(ws + WS_H + ((size_t)((2 * mm + (lw >> 1)) * 112 + 2 * (lw & 1)) * 64) * 16);
  const rsrc_t rw = mkrsrc(wd + 64 * nn);
  const int crow = 16 * lw + (lane >> 3), ccp = lane & 7;
  const u32 cvh = lane * 16, cvw = (crow * DM + (ccp ^ (((crow >> 1) & 1) << 2)) * 8) * 2;
  const u32 cdl = lds_addr(lds) + lw * 2048;
  auto cW = [&](int s, int slot) { const u32 d = cdl + slot * C_STG + 8192, sw = s * 64 * DM * 2; bdma16(rw, cvw, sw, d); bdma16(rw, cvw, sw + 8 * DM * 2, d + 1024); };
  auto cH = [&](int s, int slot) { const u32 d = cdl + slot * C_STG, sh = s * 4096; bdma16_dev(rHc, cvh, sh, d); bdma16_dev(rHc, cvh, sh + 1024, d + 1024); };
  if (wv >= 4) {
#pragma unroll
    for (int s = 0; s < 8; ++s) cW(s, s);
  }
  bar();
  if (tid == 0) __hip_atomic_store((gu32*)(ws + WS_FLB) + (m * 14 + nb) * 32, tag, RLX_AGENT);
  STAMP(0, 11);
  if (wv >= 4) {
    {
      gu32* fl = (gu32*)(ws + WS_FLB) + ((mm >> 1) * 14 + (lane < 14 ? lane : 0)) * 32;
      for (int spin = 0; spin < (1 << 16); ++spin) {
        if (__all(__hip_atomic_load(fl, RLX_AGENT) == tag)) break;
        __builtin_amdgcn_s_sleep(2);
      }
    }
    STAMP(0, 12);
#pragma unroll
    for (int s = 0; s < 8; ++s) cH(s, s);
    int slot = 8, sn = 8;
    auto issue = [&]() { cH(sn, slot); cW(sn, slot); ++sn; slot = slot == C_NB - 1 ? 0 : slot + 1; };
    wait_vm<14>(); bar(); issue();
    wait_vm<16>(); bar(); issue();
    wait_vm<18>(); bar(); issue();
    wait_vm<20>(); bar(); issue();
    wait_vm<22>(); bar(); issue();
    wait_vm<24>(); bar(); issue();
    wait_vm<26>(); bar(); issue();
#pragma unroll 1
    for (int s = 7; s < 20; ++s) { wait_vm<4 * 7>(); bar(); issue(); }
    wait_vm<4 * 7>(); bar(); wait_vm<4 * 6>(); bar(); wait_vm<4 * 5>(); bar(); wait_vm<4 * 4>(); bar();
    wait_vm<4 * 3>(); bar(); wait_vm<4 * 2>(); bar(); wait_vm<4 * 1>(); bar(); wait_vm<0>(); bar();
    STAMP(0, 13);
    STAMP_FLUSH(0);
    return;
  }
  const int dtl = wv & 1, tt = wv >> 1;
  const int tok = 64 * mm + 32 * tt + (lane & 31);
  const rsrc_t rres = mkrsrc(ws + WS_HT);
  u32 res[16];
#pragma unroll
  for (int i = 0; i < 16; ++i) res[i] = __builtin_amdgcn_raw_buffer_load_b16(rres, ((64 * nn + 32 * dtl + 8 * (i >> 2) + 4 * (lane >> 5) + (i & 3)) * HS + tok) * 2, 0, 16);
  const int cgrp = lane >> 4, cq = (lane & 15) >> 2, cp_ = lane & 3, chh = cgrp >> 1;
  const int achunk = 4 * dtl + 2 * (cgrp & 1) + (cp_ >> 1);
  const int caoff = 8192 + (4 * chh + cq) * 128 + ((achunk ^ (((cq >> 1) & 1) << 2)) * 16) + (cp_ & 1) * 8;
  const int cboff = tt * 4096 + lane * 16;
  f32x16 cacc = {};
  struct F4 { s16x8 a[4]; bf16x8 b[4]; };
  F4 Xc, Yc;
  int cbuf = 0;
  auto cstage = [&](auto first, F4& cur, const F4& prev) {
    bar();
    const char* st = lds + cbuf * C_STG;
#pragma unroll
    for (int k = 0; k < 4; ++k) {
      if (!decltype(first)::value) cacc = __builtin_amdgcn_mfma_f32_32x32x16_bf16(__builtin_bit_cast(bf16x8, prev.a[k]), prev.b[k], cacc, 0, 0, 0);
      cur.a[k] = tr_frag(st + caoff + k * 16 * 128, st + caoff + (k * 16 + 8) * 128);
      cur.b[k] = *(const bf16x8*)(st + cboff + k * 1024);
      FENCE();
    }
    cbuf = cbuf == C_NB - 1 ? 0 : cbuf + 1;
  };
  cstage(IC<1>{}, Xc, Yc);
  cstage(IC<0>{}, Yc, Xc);
#pragma unroll 1
  for (int s = 2; s < C_NST; s += 2) { cstage(IC<0>{}, Xc, Yc); cstage(IC<0>{}, Yc, Xc); }
#pragma unroll
  for (int k = 0; k < 4; ++k) cacc = __builtin_amdgcn_mfma_f32_32x32x16_bf16(__builtin_bit_cast(bf16x8, Yc.a[k]), Yc.b[k], cacc, 0, 0, 0);
  STAMP(0, 13);
  const int hh = lane >> 5;
  u16* op = out + tok * DM + 64 * nn + 32 * dtl + 4 * hh;
#pragma unroll
  for (int g = 0; g < 4; ++g)
    *(u32x2*)(op + 8 * g) = (u32x2){pk_bf16(bf2f(res[4 * g]) + rbf(cacc[4 * g]), bf2f(res[4 * g + 1]) + rbf(cacc[4 * g + 1])),
                                    pk_bf16(bf2f(res[4 * g + 2]) + rbf(cacc[4 * g + 2]), bf2f(res[4 * g + 3]) + rbf(cacc[4 * g + 3]))};
  STAMP(0, 14);
  STAMP_FLUSH(0);
}

extern "C" void kernel_launch(void* const* d_in, const int* in_sizes, int n_in, void* d_out, int out_size, void* d_ws, size_t ws_size,
                              hipStream_t stream) {
  (void)in_sizes; (void)n_in; (void)out_size; (void)ws_size;
  (void)hipFuncSetAttribute((const void*)fused_kernel, hipFuncAttributeMaxDynamicSharedMemorySize, K_LDS);
  fused_kernel<<<256, 512, K_LDS, stream>>>((const u16*)d_in[0], (const u16*)d_in[1], (const u16*)d_in[2], (const u16*)d_in[3], (const u16*)d_in[4],
                                            (const u16*)d_in[5], (const u16*)d_in[6], (const u16*)d_in[7], (const u16*)d_in[8],
                                            (const u16*)d_in[10], (const u16*)d_in[11], (const u16*)d_in[12], (char*)d_ws, (u16*)d_out);
}
```

```cpp
#include <hip/hip_runtime.h>
#include <stdint.h>

typedef unsigned short u16;
typedef unsigned int u32;
typedef unsigned long long u64;
typedef float f32x2 __attribute__((ext_vector_type(2)));
typedef float f32x4 __attribute__((ext_vector_type(4)));
typedef float f32x16 __attribute__((ext_vector_type(16)));
typedef unsigned u32x2 __attribute__((ext_vector_type(2)));
typedef unsigned u32x4 __attribute__((ext_vector_type(4)));
typedef short s16x4 __attribute__((ext_vector_type(4)));
typedef short s16x8 __attribute__((ext_vector_type(8)));
typedef __bf16 bf16x8 __attribute__((ext_vector_type(8)));
typedef __bf16 bf16x2 __attribute__((ext_vector_type(2)));
typedef _Float16 f16x8 __attribute__((ext_vector_type(8)));
typedef _Float16 f16x4 __attribute__((ext_vector_type(4)));
typedef _Float16 f16x2 __attribute__((ext_vector_type(2)));
#define DI __device__ __forceinline__
#define LAS __attribute__((address_space(3)))
#define GAS __attribute__((address_space(1)))
typedef GAS unsigned gu32;
typedef GAS unsigned long long gu64;
#define RLX_AGENT __ATOMIC_RELAXED, __HIP_MEMORY_SCOPE_AGENT
#define FENCE() __builtin_amdgcn_sched_barrier(0)

constexpr int SL = 2048, DM = 448, NS = 256, FF = 1792;
constexpr int HS = SL + 64;

constexpr size_t WS_EPOCH = 0;
constexpr size_t WS_GRAN  = 4096;
constexpr size_t WS_CS    = 32768;
constexpr size_t WS_FLA   = 65536;
constexpr size_t WS_FLB   = WS_FLA + 32768;
constexpr size_t WS_XG    = 131072;
constexpr size_t WS_HT    = WS_XG + (size_t)DM * 8192;
constexpr size_t WS_H     = WS_HT + (size_t)DM * HS * 2;
constexpr size_t WS_DBG   = WS_H + (size_t)SL * FF * 2;

#define STAMP_DECL do {} while (0)
#define STAMP(k, i) do {} while (0)
#define STAMP_FLUSH(k) do {} while (0)

DI float bf2f(u32 v) { return __uint_as_float(v << 16); }
DI float bfhi(u32 v) { return __uint_as_float(v & 0xffff0000u); }
DI u32 pk_bf16(float a, float b) { f32x2 v = {a, b}; return __builtin_bit_cast(u32, __builtin_convertvector(v, bf16x2)); }
DI u32 pk_f16(float a, float b) { f32x2 v = {a, b}; return __builtin_bit_cast(u32, __builtin_convertvector(v, f16x2)); }
DI u32 pk2(f32x2 v) { return __builtin_bit_cast(u32, __builtin_convertvector(v, f16x2)); }
DI float rbf(float a) { return __uint_as_float(pk_bf16(a, a) << 16); }

template <int CTRL, int RM> DI float dpp0(float v) { return __int_as_float(__builtin_amdgcn_update_dpp(0, __float_as_int(v), CTRL, RM, 0xf, true)); }
DI float lane63(float v) { return __int_as_float(__builtin_amdgcn_readlane(__float_as_int(v), 63)); }
DI void wave_sum2(float& a, float& b) {
  a += dpp0<0x111, 0xf>(a); b += dpp0<0x111, 0xf>(b); a += dpp0<0x112, 0xf>(a); b += dpp0<0x112, 0xf>(b);
  a += dpp0<0x114, 0xf>(a); b += dpp0<0x114, 0xf>(b); a += dpp0<0x118, 0xf>(a); b += dpp0<0x118, 0xf>(b);
  a += dpp0<0x142, 0xa>(a); b += dpp0<0x142, 0xa>(b); a += dpp0<0x143, 0xc>(a); b += dpp0<0x143, 0xc>(b);
  a = lane63(a); b = lane63(b);
}
DI void wave_max_sum(float& m, float& s) {
  m = fmaxf(m, dpp0<0x111, 0xf>(m)); s += dpp0<0x111, 0xf>(s); m = fmaxf(m, dpp0<0x112, 0xf>(m)); s += dpp0<0x112, 0xf>(s);
  m = fmaxf(m, dpp0<0x114, 0xf>(m)); s += dpp0<0x114, 0xf>(s); m = fmaxf(m, dpp0<0x118, 0xf>(m)); s += dpp0<0x118, 0xf>(s);
  m = fmaxf(m, dpp0<0x142, 0xa>(m)); s += dpp0<0x142, 0xa>(s); m = fmaxf(m, dpp0<0x143, 0xc>(m)); s += dpp0<0x143, 0xc>(s);
  m = lane63(m); s = lane63(s);
}

DI void glds16(const void* g, u32 lds_dst) {
  u32 keep;
  asm volatile("s_mov_b32 %0, m0\n\ts_mov_b32 m0, %2\n\ts_nop 0\n\tglobal_load_lds_dwordx4 %1, off\n\ts_mov_b32 m0, %0"
               : "=&s"(keep) : "v"(g), "s"(lds_dst) : "memory");
}
typedef __amdgpu_buffer_rsrc_t rsrc_t;
DI rsrc_t mkrsrc(const void* p) { return __builtin_amdgcn_make_buffer_rsrc((void*)p, 0, 0x7fffffff, 0x00020000); }
DI void bdma16(rsrc_t rs, u32 voff, u32 soff, u32 lds_dst) {
  asm volatile("s_mov_b32 m0, %0\n\ts_nop 0\n\tbuffer_load_dwordx4 %1, %2, %3 offen lds" :: "s"(lds_dst), "v"(voff), "s"(rs), "s"(soff) : "memory");
}
DI void bdma16_dev(rsrc_t rs, u32 voff, u32 soff, u32 lds_dst) {
  asm volatile("s_mov_b32 m0, %0\n\ts_nop 0\n\tbuffer_load_dwordx4 %1, %2, %3 offen sc1 lds" :: "s"(lds_dst), "v"(voff), "s"(rs), "s"(soff) : "memory");
}
DI void bdma4(rsrc_t rs, u32 voff, u32 soff, u32 lds_dst) {
  asm volatile("s_mov_b32 m0, %0\n\ts_nop 0\n\tbuffer_load_dword %1, %2, %3 offen lds" :: "s"(lds_dst), "v"(voff), "s"(rs), "s"(soff) : "memory");
}
DI u32 lds_addr(const void* p) { return (u32)(size_t)(LAS const char*)p; }
DI s16x8 tr_frag(const char* p0, const char* p1) {
  s16x4 a = __builtin_amdgcn_ds_read_tr16_b64_v4i16((LAS s16x4*)p0);
  s16x4 b = __builtin_amdgcn_ds_read_tr16_b64_v4i16((LAS s16x4*)p1);
  return __builtin_shufflevector(a, b, 0, 1, 2, 3, 4, 5, 6, 7);
}

struct IdxTab { u16 v[2064]; };
constexpr int tap_entry(int l) {
  if (l < 0) return 21 * 32;
  int lam = l;
  if (l >= 256) {
    int sh = l >= 1024 ? 3 : (l >= 512 ? 2 : 1);
    int q = l >> sh, rem = l & ((1 << sh) - 1), half = 1 << (sh - 1);
    if (rem > half || (rem == half && (q & 1))) q += 1;
    lam = q << sh;
  }
  int hq = lam >> 8, lo = lam & 255, c = lo >> 3, r = 0;
  if (hq == 0) r = lo & 7;
  else if (hq == 1) r = 8 + ((lo & 7) >> 1);
  else if (hq == 2) r = 12 + ((lo & 7) >> 2);
  else if (hq == 3) r = 14 + ((lo & 7) >> 2);
  else if (hq < 8) r = 16 + hq - 4;
  else { r = 20; c = 0; }
  return r * 32 + c;
}
constexpr IdxTab make_tab() { IdxTab t{}; for (int y = 0; y < 2064; ++y) t.v[y] = (u16)(2 * tap_entry(2047 - y)); return t; }
__device__ const IdxTab RIDX __attribute__((aligned(16))) = make_tab();

DI float pow2_for(float mx, int target) {
  int e = (int)(__float_as_uint(mx) >> 23);
  int be = 127 + target - (e - 126);
  be = be > 254 ? 254 : (be < 1 ? 1 : be);
  return __uint_as_float((u32)be << 23);
}
DI float pow2_inv(float p) { return __uint_as_float((254u - (__float_as_uint(p) >> 23)) << 23); }

struct CM { f32x2 a, b; };
DI CM mk(f32x2 e) { return {e, (f32x2){-e.y, e.x}}; }
DI f32x2 cmul(f32x2 p, CM m) {
  f32x2 t = __builtin_shufflevector(p, p, 0, 0) * m.a;
  return __builtin_elementwise_fma(__builtin_shufflevector(p, p, 1, 1), m.b, t);
}
DI f32x2 cexp_s(float zr, float zi, float s) {
  float m = __builtin_amdgcn_exp2f(zr * (s * 1.44269504088896341f));
  float a = __builtin_amdgcn_fractf(zi * (s * 0.15915494309189535f));
  return (f32x2){m * __builtin_amdgcn_cosf(a), m * __builtin_amdgcn_sinf(a)};
}
template <int OFF>
DI void put4(u32 base, u32 v0, u32 v1, u32 v2, u32 v3) {
  asm volatile("s_mov_b32 m0, %0\n\ts_nop 0\n\tds_write_addtid_b32 %1 offset:%5\n\tds_write_addtid_b32 %2 offset:%6\n\t"
               "ds_write_addtid_b32 %3 offset:%7\n\tds_write_addtid_b32 %4 offset:%8"
               :: "s"(base), "v"(v0), "v"(v1), "v"(v2), "v"(v3), "n"(OFF), "n"(OFF + 272), "n"(OFF + 544), "n"(OFF + 816) : "memory");
}

template <int V> struct IC { static constexpr int value = V; };
template <int N> DI void wait_vm() { asm volatile("s_waitcnt vmcnt(%0)" :: "n"(N) : "memory"); }
DI void glds4(const void* g, u32 lds_dst) {
  u32 keep;
  asm volatile("s_mov_b32 %0, m0\n\ts_mov_b32 m0, %2\n\ts_nop 0\n\tglobal_load_lds_dword %1, off\n\ts_mov_b32 m0, %0"
               : "=&s"(keep) : "v"(g), "s"(lds_dst) : "memory");
}
DI void bar() { __builtin_amdgcn_s_barrier(); }

constexpr int A_T1 = 0, A_T2 = 8192;
constexpr int A_SCR = 25088, A_SCRW = 32 * 272 + 21 * 272;
constexpr int A_CP = 0, A_CPS = 4160, A_CPC = 8 * A_CPS;
constexpr int A_EX = 2 * A_CPC, A_EXW = 64 * 144, A_XX = A_EX + 4 * A_EXW;
constexpr int A_UU = A_SCR + 8 * A_SCRW, A_UUC = 95 * 80;
constexpr int A_TB = A_UU + 2 * A_UUC, A_TBC = 22 * 32 * 2;
constexpr int A_SW = A_TB + 2 * A_TBC;
constexpr int A_LDS = A_SW + 64;
static_assert(A_LDS <= 163840 && A_XX + 10240 <= A_UU && (A_SCR % 16) == 0 && (A_SCRW % 16) == 0 && (A_UU % 16) == 0, "lds");

constexpr int B_G = 0, B_U = 8192, B_H = 16384, B_STG = 24576, B_NB = 6, B_NST = 14;
constexpr int B_CS = B_NB * B_STG, B_ST = B_CS + 1024, B_CNT = 163840 - 16, B_LDS = B_ST + 1024;
static_assert(B_LDS <= 163840, "lds");

constexpr int C_HN = 12, C_WN = 8, C_W0 = C_HN * 8192, C_NST = 28, C_LDS = C_W0 + C_WN * 8192;

constexpr int K_LDS = 163840;
constexpr int CODE_WARM_KB = 24;
static_assert(A_LDS <= K_LDS - 16 && B_LDS <= K_LDS && C_LDS <= K_LDS, "lds");

__global__ __launch_bounds__(512, 2) void fused_kernel(const u16* __restrict__ x, const u16* __restrict__ Are, const u16* __restrict__ Aim,
    const u16* __restrict__ Bre, const u16* __restrict__ Bim, const u16* __restrict__ Cre, const u16* __restrict__ Cim,
    const u16* __restrict__ logdt, const u16* __restrict__ Dskip, const u16* __restrict__ wg, const u16* __restrict__ wu,
    const u16* __restrict__ wd, char* ws, u16* __restrict__ out) {
  extern __shared__ __attribute__((aligned(16))) char lds[];
  char* dbg = ws + WS_DBG; (void)dbg;
  STAMP_DECL;
  const int tid = threadIdx.x, lane = tid & 63;
  const int wv = __builtin_amdgcn_readfirstlane(tid >> 6);
  const int b = blockIdx.x, xcd = b & 7, j = b >> 3;
  const bool worker = j < 28;
  STAMP(0, 0);
  asm volatile("" :: "s"(x), "s"(Are), "s"(Aim), "s"(Bre), "s"(Bim), "s"(Cre), "s"(Cim), "s"(logdt), "s"(Dskip), "s"(wg), "s"(wu), "s"(wd), "s"(ws), "s"(out));
  gu32* epoch_p = (gu32*)(ws + WS_EPOCH);
  gu64* gran = (gu64*)(ws + WS_GRAN);

  const int w = worker ? xcd * 28 + j : 0, c0 = 2 * w;
  const int trow = 8 * b + wv;
  const u32x4 xr = *(const u32x4*)(x + trow * DM + (lane < 56 ? lane : 0) * 8);
  const u32 tag = __hip_atomic_load(epoch_p, RLX_AGENT) + 1u;
  const int ch = tid >> 8, n = tid & 255;
  const int idx = (c0 + ch) * NS + n;
  const u32 p_ldt = logdt[c0 + ch];
  const u32 p_are = Are[idx], p_aim = Aim[idx];
  const u32x4 xt = *(const u32x4*)(x + (128 * (w & 15) + (tid >> 2)) * DM + 32 * (w >> 4) + (tid & 3) * 8);
  const u32 p_bre = Bre[idx], p_bim = Bim[idx], p_cre = Cre[idx], p_cim = Cim[idx];
  const u32 p_d01 = *(const u32*)(Dskip + c0);
  const u32x4 ix0 = *(const u32x4*)(RIDX.v + 8 * (tid & 255)), ix1 = *(const u32x4*)(RIDX.v + 8 * (tid & 255) + 8);
  if (tid == 0) *(u32*)(lds + B_CNT) = 0u;
  {
    const float piv = __uint_as_float((u32)__builtin_amdgcn_readfirstlane(xr[0]) << 16);
    float s = 0.f, q = 0.f, s2 = 0.f, q2 = 0.f;
#pragma unroll
    for (int i = 0; i < 4; ++i) {
      const float lo = bf2f(xr[i] & 0xffffu) - piv, hi = bfhi(xr[i]) - piv;
      s += lo; s2 += hi; q = fmaf(lo, lo, q); q2 = fmaf(hi, hi, q2);
    }
    s = lane < 56 ? s + s2 : 0.f; q = lane < 56 ? q + q2 : 0.f;
    wave_sum2(s, q);
    const float ms = s * (1.f / DM);
    float var = q * (1.f / DM) - ms * ms; var = var < 0.f ? 0.f : var;
    const float sd = rbf(__builtin_sqrtf(rbf(rbf(var) + 1e-5f)));
    if (lane == 0) __hip_atomic_store(gran + trow, ((u64)tag << 32) | pk_bf16(piv + ms, sd), RLX_AGENT);
  }
  STAMP(0, 1);
  if (!worker) {
    {
      u64 pc;
      asm volatile("s_getpc_b64 %0" : "=s"(pc));
      const char* code = (const char*)(pc & ~(u64)63);
      u32x4 cw[CODE_WARM_KB / 8];
#pragma unroll
      for (int i = 0; i < CODE_WARM_KB / 8; ++i) cw[i] = *(const GAS u32x4*)(code + (i * 512 + tid) * 16);
      u32 acc = 0;
#pragma unroll
      for (int i = 0; i < CODE_WARM_KB / 8; ++i) acc |= cw[i][0] & cw[i][1] & cw[i][2] & cw[i][3];
      asm volatile("" :: "v"(acc));
    }
    {
      u32 t0 = 0;
      if (wv == 1) {
#pragma unroll
        for (int i = 0; i < 4; ++i) t0 |= *(const GAS u32*)(ws + (size_t)(i * 64 + lane) * 65536);
      } else if (wv == 2 && lane < 28) t0 = *(const GAS u32*)((const char*)out + lane * 65536);
      asm volatile("" :: "v"(t0));
    }
    const int s = xcd * 4 + (j - 28);
    const u16* W = (s >> 4) ? wu : wg;
    const int colb = (s & 15) * 112;
    float a[8] = {0, 0, 0, 0, 0, 0, 0, 0};
    const int cc = tid % 14, rg = tid / 14;
    u32 warm = 0;
    {
      u32x4 wv4[7];
#pragma unroll
      for (int i = 0; i < 7; ++i) { const int c = tid + 512 * i; wv4[i] = *(const u32x4*)(wd + (size_t)s * 25088 + (c < 3136 ? c : 0) * 8); }
#pragma unroll
      for (int i = 0; i < 7; ++i) warm |= wv4[i][0] & wv4[i][1] & wv4[i][2] & wv4[i][3];
      asm volatile("" :: "v"(warm));
    }
    if (tid < 448) {
      u32x4 v[14];
#pragma unroll
      for (int r = 0; r < 14; ++r) v[r] = *(const u32x4*)(W + (rg * 14 + r) * FF + colb + cc * 8);
#pragma unroll
      for (int r = 0; r < 14; ++r)
#pragma unroll
        for (int i = 0; i < 4; ++i) { a[2 * i] += bf2f(v[r][i] & 0xffffu); a[2 * i + 1] += bfhi(v[r][i]); }
      float* red = (float*)lds;
#pragma unroll
      for (int i = 0; i < 8; ++i) red[rg * 112 + cc * 8 + i] = a[i];
    }
    __syncthreads();
    if (tid < 112) {
      float t = 0.f;
      for (int r = 0; r < 32; ++r) t += ((float*)lds)[r * 112 + tid];
      __hip_atomic_store((gu64*)(ws + WS_CS) + (s >> 4) * FF + colb + tid, ((u64)tag << 32) | __float_as_uint(t), RLX_AGENT);
    }
    STAMP_FLUSH(0);
    return;
  }
  *(u32x4*)(lds + A_T1 + tid * 16) = xt;
  const float dt = rbf(__builtin_amdgcn_exp2f(bf2f(p_ldt) * 1.44269504088896341f));
  const float zr = dt * bf2f(p_are), zi = dt * bf2f(p_aim);
  const f32x2 E8 = cexp_s(zr, zi, 8.f);
  const f32x2 E1 = cexp_s(zr, -zi, 1.f), F = cexp_s(zr, -zi, 256.f);
  {
    const int grp = lane >> 4, q = (lane & 15) >> 2, p = lane & 3;
#pragma unroll
    for (int i = 0; i < 2; ++i) {
      const int tb = 16 * wv + 8 * i + 4 * (grp >> 1);
      const s16x4 v = __builtin_amdgcn_ds_read_tr16_b64_v4i16((LAS s16x4*)(lds + A_T1 + (tb + q) * 64 + (grp & 1) * 32 + p * 8));
      const u32x2 vw = __builtin_bit_cast(u32x2, v);
      *(u32x4*)(lds + A_T2 + (16 * (grp & 1) + (lane & 15)) * 528 + tb * 4) = (u32x4){vw[0], tag, vw[1], tag};
    }
  }
  const u32 scb = lds_addr(lds) + A_SCR + wv * A_SCRW;
  const CM M8 = mk(E8);
  f32x2 p0 = {16384.f, 0.f}, p1 = cmul(p0, M8), p2 = cmul(p1, M8), p3 = cmul(p2, M8);
  const CM M32 = mk(cmul(p2, mk(p2 * (1.f / 16384.f))) * (1.f / 16384.f));
  __syncthreads();
  {
    __amdgpu_buffer_rsrc_t rs = __builtin_amdgcn_make_buffer_rsrc((void*)(ws + WS_XG), 0, DM * 8192, 0x00020000);
#pragma unroll
    for (int k = 0; k < 2; ++k) {
      const int id = tid + 512 * k, c = id >> 5, pos = id & 31;
      const u32x4 v = *(const u32x4*)(lds + A_T2 + c * 528 + pos * 16);
      __builtin_amdgcn_raw_buffer_store_b128(v, rs, (32 * (w >> 4) + c) * 8192 + (w & 15) * 512 + pos * 16, 0, 16);
    }
  }
  STAMP(0, 2);
  {
    const float br = dt * bf2f(p_bre), bi = dt * bf2f(p_bim);
    const float cr = bf2f(p_cre), ci = bf2f(p_cim);
    const float CBr = cr * br - ci * bi, CBi = cr * bi + ci * br;
    float mx = fmaxf(fabsf(CBr), fabsf(CBi)), sabs = fabsf(CBr) + fabsf(CBi);
    wave_max_sum(mx, sabs);
    const float scA = pow2_for(mx, 14);
    if (lane == 0) { ((float*)(lds + A_SW))[wv] = pow2_inv(scA) * (1.f / 16384.f); ((float*)(lds + A_SW))[8 + wv] = sabs; }
#define COL4(R) { put4<(R) * 272>(scb, pk2(p0), pk2(p1), pk2(p2), pk2(p3)); \
                  if ((R) < 28) { p0 = cmul(p0, M32); p1 = cmul(p1, M32); p2 = cmul(p2, M32); p3 = cmul(p3, M32); } }
    COL4(0) COL4(4) COL4(8) COL4(12) COL4(16) COL4(20) COL4(24) COL4(28)
#undef COL4
    const CM M1 = mk(E1), MF = mk(F);
    const f32x2 E2 = cmul(E1, M1);
    const CM M2 = mk(E2), M4 = mk(cmul(E2, M2));
    const f32x2 X0 = {CBr * scA, -CBi * scA};
    const f32x2 X1 = cmul(X0, M1), G1 = cmul(X0, MF);
    const f32x2 X2 = cmul(X0, M2), X3 = cmul(X1, M2), G2 = cmul(G1, MF), Y1 = cmul(G1, M2);
    put4<(32 + 0) * 272>(scb, pk2(X0), pk2(X1), pk2(X2), pk2(X3));
    const f32x2 X4 = cmul(X0, M4), X5 = cmul(X1, M4), X6 = cmul(X2, M4), X7 = cmul(X3, M4);
    const f32x2 G3 = cmul(G2, MF), Y2 = cmul(G1, M4), Y3 = cmul(Y1, M4);
    put4<(32 + 4) * 272>(scb, pk2(X4), pk2(X5), pk2(X6), pk2(X7));
    put4<(32 + 8) * 272>(scb, pk2(G1), pk2(Y1), pk2(Y2), pk2(Y3));
    const f32x2 G4 = cmul(G3, MF), Z2 = cmul(G2, M4), Z3 = cmul(G3, M4);
    put4<(32 + 12) * 272>(scb, pk2(G2), pk2(Z2), pk2(G3), pk2(Z3));
    const CM MF2 = mk(cmul(F, MF));
    const f32x2 G5 = cmul(G3, MF2), G6 = cmul(G4, MF2);
    const f32x2 G7 = cmul(G5, MF2), G8 = cmul(G6, MF2);
    put4<(32 + 16) * 272>(scb, pk2(G4), pk2(G5), pk2(G6), pk2(G7));
    {
      const u32 g8 = pk2(G8);
      asm volatile("s_mov_b32 m0, %0\n\ts_nop 0\n\tds_write_addtid_b32 %1 offset:%2" :: "s"(scb), "v"(g8), "n"((32 + 20) * 272) : "memory");
    }
    char* scr = lds + A_SCR + wv * A_SCRW;
    const char* fr = scr + (lane & 31) * 272 + (lane >> 5) * 16;
    f16x8 bf[8], af[8];
#pragma unroll
    for (int ks = 0; ks < 8; ++ks) { bf[ks] = *(const f16x8*)(fr + ks * 32); af[ks] = *(const f16x8*)(fr + 32 * 272 + ks * 32); }
    f32x16 acc0 = {}, acc1 = {};
#pragma unroll
    for (int ks = 0; ks < 8; ks += 2) {
      acc0 = __builtin_amdgcn_mfma_f32_32x32x16_f16(af[ks], bf[ks], acc0, 0, 0, 0);
      acc1 = __builtin_amdgcn_mfma_f32_32x32x16_f16(af[ks + 1], bf[ks + 1], acc1, 0, 0, 0);
    }
#pragma unroll
    for (int r = 0; r < 12; ++r) {
      int row = (r & 3) + 8 * (r >> 2) + 4 * (lane >> 5);
      *(float*)(scr + row * 128 + (lane & 31) * 4) = acc0[r] + acc1[r];
    }
  }
  STAMP(0, 3);
  const __amdgpu_buffer_rsrc_t rs_g = __builtin_amdgcn_make_buffer_rsrc((void*)(ws + WS_GRAN), 0, SL * 8, 0x00020000);
  const __amdgpu_buffer_rsrc_t rs_x = __builtin_amdgcn_make_buffer_rsrc((void*)(ws + WS_XG + (size_t)c0 * 8192), 0, 2 * 8192, 0x00020000);
  u32x4 g[4], xg[4];
  const int t8 = tid & 255;
  auto fetch = [&]() {
#pragma unroll
    for (int i = 0; i < 4; ++i) g[i] = __builtin_amdgcn_raw_buffer_load_b128(rs_g, t8 * 64 + i * 16, 0, 16);
#pragma unroll
    for (int i = 0; i < 4; ++i) xg[i] = __builtin_amdgcn_raw_buffer_load_b128(rs_x, (i >> 1) * 8192 + t8 * 32 + (i & 1) * 16, 0, 16);
  };
  if (wv < 4) fetch();
  asm volatile("s_waitcnt lgkmcnt(0)" ::: "memory");
  bar();
  float kinv0, kinv1;
  {
    const float* sw = (const float*)(lds + A_SW);
    const float k0 = pow2_for(sw[8] + sw[9] + sw[10] + sw[11], 15), k1 = pow2_for(sw[12] + sw[13] + sw[14] + sw[15], 15);
    kinv0 = pow2_inv(k0); kinv1 = pow2_inv(k1);
    const float ks = ch ? k1 : k0;
    const float i0 = sw[4 * ch] * ks, i1 = sw[4 * ch + 1] * ks, i2 = sw[4 * ch + 2] * ks, i3 = sw[4 * ch + 3] * ks;
    const char* p = lds + A_SCR + (4 * ch) * A_SCRW + (tid & 255) * 4;
    float v[3][4];
#pragma unroll
    for (int it = 0; it < 3; ++it)
#pragma unroll
      for (int k = 0; k < 4; ++k) v[it][k] = *(const float*)(p + it * 1024 + k * A_SCRW);
#pragma unroll
    for (int it = 0; it < 3; ++it) {
      const int e = (tid & 255) + 256 * it;
      const float t = v[it][0] * i0 + v[it][1] * i1 + v[it][2] * i2 + v[it][3] * i3;
      if (it < 2 || e < 672) *(_Float16*)(lds + A_TB + ch * A_TBC + e * 2) = (_Float16)t;
    }
    if (tid < 8) { int cc = tid >> 2; *(u32x4*)(lds + A_TB + cc * A_TBC + 21 * 64 + (tid & 3) * 16) = (u32x4){0, 0, 0, 0}; }
    if (tid < 310) { int cc = tid / 155, r = tid % 155; *(u32x4*)(lds + A_UU + cc * A_UUC + r * 16) = (u32x4){0, 0, 0, 0}; }
  }
  asm volatile("s_waitcnt lgkmcnt(0)" ::: "memory");
  bar();
  if (wv >= 4) {
#pragma unroll
   for (int ch = 0; ch < 2; ++ch) {
    const char* tb = lds + A_TB + ch * A_TBC;
    u32 P[8];
#pragma unroll
    for (int k = 0; k < 8; ++k) {
      u32 iw = k < 4 ? ix0[k] : ix1[k - 4];
      u32 lo = *(const u16*)(tb + (iw & 0xffffu));
      u32 hi = (k < 7) ? *(const u16*)(tb + (iw >> 16)) : 0u;
      P[k] = lo | (hi << 16);
    }
    u32 Q[7];
#pragma unroll
    for (int k = 0; k < 7; ++k) Q[k] = __builtin_amdgcn_alignbit(P[k + 1], P[k], 16);
    char* cp = lds + A_CP + ch * A_CPC + (tid & 255) * 16;
#pragma unroll
    for (int s = 0; s < 8; ++s) {
      const int off = 7 - s;
      u32x4 v;
      if ((off & 1) == 0) v = (u32x4){P[off / 2], P[off / 2 + 1], P[off / 2 + 2], P[off / 2 + 3]};
      else v = (u32x4){Q[off / 2], Q[off / 2 + 1], Q[off / 2 + 2], Q[off / 2 + 3]};
      *(u32x4*)(cp + s * A_CPS) = v;
    }
   }
    if (t8 < 48) { int cc = t8 / 24, r = t8 % 24; *(u32x4*)(lds + A_CP + cc * A_CPC + (r / 3) * A_CPS + (256 + r % 3) * 16) = (u32x4){0, 0, 0, 0}; }
    STAMP(0, 4);
  } else {
    for (int spin = 0; spin < (1 << 16); ++spin) {
      u32 bad = 0;
#pragma unroll
      for (int i = 0; i < 4; ++i) bad |= (g[i][1] ^ tag) | (g[i][3] ^ tag) | (xg[i][1] ^ tag) | (xg[i][3] ^ tag);
      if (__all(bad == 0)) break;
      __builtin_amdgcn_s_sleep(2);
      fetch();
    }
    STAMP(0, 4);
    float uu[2][8];
#pragma unroll
    for (int i = 0; i < 8; ++i) {
      const u32 st = g[i >> 1][(i & 1) * 2];
      const float m = bf2f(st & 0xffffu), rs_ = __builtin_amdgcn_rcpf(bfhi(st));
#pragma unroll
      for (int c = 0; c < 2; ++c) {
        const u32 xw = xg[2 * c + (i >> 2)][(i & 2)];
        const float xv = (i & 1) ? bfhi(xw) : bf2f(xw & 0xffffu);
        uu[c][i] = rbf(rbf(xv - m) * rs_);
      }
    }
#pragma unroll
    for (int c = 0; c < 2; ++c) {
      *(u32x4*)(lds + A_UU + c * A_UUC + ((tid >> 2) + 31) * 80 + (tid & 3) * 16) =
          (u32x4){pk_f16(uu[c][0], uu[c][1]), pk_f16(uu[c][2], uu[c][3]), pk_f16(uu[c][4], uu[c][5]), pk_f16(uu[c][6], uu[c][7])};
      *(u32x4*)(lds + A_XX + c * 5120 + (tid >> 2) * 80 + (tid & 3) * 16) = (u32x4){xg[2 * c][0], xg[2 * c][2], xg[2 * c + 1][0], xg[2 * c + 1][2]};
    }
  }
  STAMP(0, 5);
  asm volatile("s_waitcnt lgkmcnt(0)" ::: "memory");
  bar();
  if (b == 0 && tid == 0) __hip_atomic_store(epoch_p, tag, RLX_AGENT);
  const int cw = (wv >> 1) & 1, hf = wv & 1;
  const int q = lane & 31, h = lane >> 5;
  if (wv < 4) {
    const int a = q >> 3, s = q & 7;
    const char* abase = lds + A_CP + cw * A_CPC + s * A_CPS + 4080 - 16 * a + 16 * h - 32 * (32 * hf + 30) - 2048;
    const char* bbase = lds + A_UU + cw * A_UUC + (q + 31 - (16 * hf + 15)) * 80 + 16 * h;
    f32x16 y0 = {}, y1 = {};
    f16x8 fa[2][8];
    auto ld = [&](int i, f16x8 (&f)[8]) {
#pragma unroll
      for (int kk = 0; kk < 2; ++kk) {
        const int erel = 30 - (2 * i - kk);
        f[4 * kk] = *(const f16x8*)(abase + 2048 + 32 * erel);
        f[4 * kk + 1] = *(const f16x8*)(bbase + (15 - i) * 80 + kk * 32);
        f[4 * kk + 2] = *(const f16x8*)(bbase + (15 - i + 32) * 80 + kk * 32);
        f[4 * kk + 3] = *(const f16x8*)(abase + 32 * erel);
      }
    };
    ld(0, fa[0]);
#pragma unroll
    for (int i = 0; i < 16; ++i) {
      const f16x8 (&f)[8] = fa[i & 1];
      f16x8 (&g)[8] = fa[(i + 1) & 1];
#pragma unroll
      for (int kk = 0; kk < 2; ++kk) {
        const int erel = 30 - (2 * (i + 1) - kk);
        y0 = __builtin_amdgcn_mfma_f32_32x32x16_f16(f[4 * kk], f[4 * kk + 1], y0, 0, 0, 0);
        FENCE();
        y1 = __builtin_amdgcn_mfma_f32_32x32x16_f16(f[4 * kk], f[4 * kk + 2], y1, 0, 0, 0);
        if (i + 1 < 16) { g[4 * kk] = *(const f16x8*)(abase + 2048 + 32 * erel); g[4 * kk + 2] = *(const f16x8*)(bbase + (15 - i - 1 + 32) * 80 + kk * 32); }
        FENCE();
        y1 = __builtin_amdgcn_mfma_f32_32x32x16_f16(f[4 * kk + 3], f[4 * kk + 1], y1, 0, 0, 0);
        if (i + 1 < 16) { g[4 * kk + 1] = *(const f16x8*)(bbase + (15 - i - 1) * 80 + kk * 32); g[4 * kk + 3] = *(const f16x8*)(abase + 32 * erel); }
        FENCE();
      }
    }
    char* pp = lds + A_EX + wv * A_EXW + q * 144 + h * 16;
#pragma unroll
    for (int g = 0; g < 4; ++g) {
      *(f32x4*)(pp + g * 32) = (f32x4){y0[4 * g], y0[4 * g + 1], y0[4 * g + 2], y0[4 * g + 3]};
      *(f32x4*)(pp + 32 * 144 + g * 32) = (f32x4){y1[4 * g], y1[4 * g + 1], y1[4 * g + 2], y1[4 * g + 3]};
    }
  }
  STAMP(0, 6);
  asm volatile("s_waitcnt lgkmcnt(0)" ::: "memory");
  bar();
  const int m = 4 * (xcd >> 1) + j / 7, nb = 7 * (xcd & 1) + j % 7;
  const int lw = wv & 3;
  const int brow = 4 * lw + (lane >> 4), bcp = lane & 15;
  const rsrc_t rg = mkrsrc(wg + 128 * nb), ru = mkrsrc(wu + 128 * nb), rh = mkrsrc((const u16*)(ws + WS_HT) + 128 * m);
  const u32 vg = (brow * FF + (bcp ^ ((brow & 3) << 2)) * 8) * 2, vu = (brow * FF + (bcp ^ (((brow & 3) << 2) ^ 2)) * 8) * 2;
  const u32 vh = (brow * HS + (bcp ^ ((brow & 3) << 2)) * 8) * 2;
  const u32 bdl = lds_addr(lds) + lw * 1024;
  auto issueW = [&](int s, int slot) {
    const u32 d = bdl + slot * B_STG, sw = s * 32 * FF * 2;
    bdma16(rg, vg, sw, d + B_G); bdma16(rg, vg, sw + 16 * FF * 2, d + B_G + 4096);
    bdma16(ru, vu, sw, d + B_U); bdma16(ru, vu, sw + 16 * FF * 2, d + B_U + 4096);
  };
  auto issueH = [&](int s, int slot) {
    const u32 d = bdl + slot * B_STG, sh = s * 32 * HS * 2;
    bdma16_dev(rh, vh, sh, d + B_H); bdma16_dev(rh, vh, sh + 16 * HS * 2, d + B_H + 4096);
  };
  {
    const int c = tid >> 8;
    const char* pp = lds + A_EX + (2 * c) * A_EXW + (t8 >> 2) * 144 + (t8 & 3) * 32;
    const f32x4 a0 = *(const f32x4*)pp, a1 = *(const f32x4*)(pp + 16), b0 = *(const f32x4*)(pp + A_EXW), b1 = *(const f32x4*)(pp + A_EXW + 16);
    const f16x8 u8 = *(const f16x8*)(lds + A_UU + c * A_UUC + ((t8 >> 2) + 31) * 80 + (t8 & 3) * 16);
    const u32x4 x8 = *(const u32x4*)(lds + A_XX + c * 5120 + (t8 >> 2) * 80 + (t8 & 3) * 16);
    const float ki = c ? kinv1 : kinv0, dk = c ? bfhi(p_d01) : bf2f(p_d01 & 0xffffu);
    u32x4 o;
#pragma unroll
    for (int i = 0; i < 4; ++i) {
      const float y0 = (i < 2 ? a0[2 * i] + b0[2 * i] : a1[2 * i - 4] + b1[2 * i - 4]) * ki;
      const float y1 = (i < 2 ? a0[2 * i + 1] + b0[2 * i + 1] : a1[2 * i - 3] + b1[2 * i - 3]) * ki;
      const u32 yb = pk_bf16(y0, y1), ub = pk_bf16((float)u8[2 * i] * dk, (float)u8[2 * i + 1] * dk);
      const u32 sb = pk_bf16(bf2f(yb & 0xffffu) + bf2f(ub & 0xffffu), bfhi(yb) + bfhi(ub));
      const u32 xw = x8[i];
      o[i] = pk_bf16(bf2f(xw & 0xffffu) + bf2f(sb & 0xffffu), bfhi(xw) + bfhi(sb));
    }
    __builtin_amdgcn_raw_buffer_store_b128(o, mkrsrc(ws + WS_HT), ((c0 + c) * HS + 8 * t8) * 2, 0, 16);
  }
  if (wv >= 4) { issueW(0, 0); issueW(1, 1); issueW(2, 2); wait_vm<12>(); }
  else wait_vm<0>();
  bar();
  if (tid == 0) __hip_atomic_store((gu32*)(ws + WS_FLA) + w * 32, tag, RLX_AGENT);
  if (wv >= 4) issueW(3, 3);
  STAMP(0, 7);
  if (wv >= 4) {
    if (lw >= 2) {
      const rsrc_t rc = mkrsrc(ws + WS_CS + ((lw - 2) * FF + 128 * nb) * 8);
      u32x4 cg;
      for (int spin = 0; spin < (1 << 16); ++spin) {
        cg = __builtin_amdgcn_raw_buffer_load_b128(rc, lane * 16, 0, 16);
        if (__all(cg[1] == tag && cg[3] == tag)) break;
        __builtin_amdgcn_s_sleep(2);
      }
      *(u32x2*)(lds + B_CS + (lw - 2) * 512 + lane * 8) = (u32x2){cg[0], cg[2]};
    }
    {
      gu32* fl = (gu32*)(ws + WS_FLA) + (lw * 56 + (lane < 56 ? lane : 0)) * 32;
      for (int spin = 0; spin < (1 << 16); ++spin) {
        if (__all(__hip_atomic_load(fl, RLX_AGENT) == tag)) break;
        __builtin_amdgcn_s_sleep(2);
      }
      LAS u32* cnt = (LAS u32*)(lds + B_CNT);
      if (lane == 0) __hip_atomic_fetch_add(cnt, 1u, __ATOMIC_RELAXED, __HIP_MEMORY_SCOPE_WORKGROUP);
      for (int spin = 0; spin < (1 << 20); ++spin) {
        if (__hip_atomic_load(cnt, __ATOMIC_RELAXED, __HIP_MEMORY_SCOPE_WORKGROUP) >= 4u) break;
        __builtin_amdgcn_s_sleep(1);
      }
    }
    STAMP(0, 8);
    issueH(0, 0); issueH(1, 1); issueH(2, 2); issueH(3, 3);
    int slot = 4, sn = 4;
    auto issue = [&]() { issueH(sn, slot); issueW(sn, slot); ++sn; slot = slot == B_NB - 1 ? 0 : slot + 1; };
    wait_vm<6>(); bar(); issue();
    wait_vm<10>(); bar(); issue();
    wait_vm<14>(); bar(); issue();
#pragma unroll 1
    for (int s = 3; s < 10; ++s) { wait_vm<6 * 3>(); bar(); issue(); }
    wait_vm<6 * 3>(); bar();
    wait_vm<6 * 2>(); bar();
    wait_vm<6 * 1>(); bar();
    wait_vm<0>(); bar();
    STAMP(0, 9);
    bar();
  } else {
  const int tp = wv & 1, ag = wv >> 1;
  const int grp = lane >> 4, q = (lane & 15) >> 2, p = lane & 3, h = grp >> 1;
  const int aoff = ((grp & 1) ? B_U : B_G) + (8 * h + q) * 256 + (p & 1) * 8;
  const int akey = (q << 2) ^ ((grp & 1) << 1);
  const int boff = B_H + (8 * h + q) * 256 + (p & 1) * 8;
  int ao[4], bo[2];
#pragma unroll
  for (int i = 0; i < 4; ++i) ao[i] = aoff + (((2 * (4 * ag + i) + (p >> 1)) ^ akey) * 16);
#pragma unroll
  for (int t = 0; t < 2; ++t) bo[t] = boff + (((4 * (2 * tp + (t ^ ag)) + 2 * (grp & 1) + (p >> 1)) ^ (q << 2)) * 16);
  f32x16 acc[4][2];
#pragma unroll
  for (int i = 0; i < 4; ++i) { acc[i][0] = (f32x16){}; acc[i][1] = (f32x16){}; }
  struct KF { s16x8 a[4], b[2]; };
  KF X, Y;
  float s0 = 0.f, s1 = 0.f, q0 = 0.f, q1 = 0.f;
  auto stat1 = [&](u32 wi) {
    const float lo = bf2f(wi & 0xffffu), hi = bfhi(wi);
    asm("v_add_f32 %0, %0, %1" : "+v"(s0) : "v"(lo)); asm("v_add_f32 %0, %0, %1" : "+v"(s1) : "v"(hi));
    asm("v_fmac_f32 %0, %1, %1" : "+v"(q0) : "v"(lo)); asm("v_fmac_f32 %0, %1, %1" : "+v"(q1) : "v"(hi));
  };
  auto stat_half = [&](u32 wi, int hi_) {
    if (hi_) { const float hi = bfhi(wi); asm("v_add_f32 %0, %0, %1" : "+v"(s1) : "v"(hi)); asm("v_fmac_f32 %0, %1, %1" : "+v"(q1) : "v"(hi)); }
    else { const float lo = bf2f(wi & 0xffffu); asm("v_add_f32 %0, %0, %1" : "+v"(s0) : "v"(lo)); asm("v_fmac_f32 %0, %1, %1" : "+v"(q0) : "v"(lo)); }
  };
  auto kstep = [&](auto havecur, const KF& cur, KF& nxt, const char* st) {
    const u32x4 sw = __builtin_bit_cast(u32x4, cur.b[0]);
#pragma unroll
    for (int i = 0; i < 4; ++i) {
#pragma unroll
      for (int t = 0; t < 2; ++t) {
        if (decltype(havecur)::value)
          acc[i][t] = __builtin_amdgcn_mfma_f32_32x32x16_bf16(__builtin_bit_cast(bf16x8, cur.a[i]), __builtin_bit_cast(bf16x8, cur.b[t]), acc[i][t], 0, 0, 0);
        const int g = 2 * i + t;
        if (g < 2) nxt.b[g] = tr_frag(st + bo[g], st + bo[g] + 4 * 256);
        else if (g < 6) nxt.a[g - 2] = tr_frag(st + ao[g - 2], st + ao[g - 2] + 4 * 256);
        if (decltype(havecur)::value) stat_half(sw[g >> 1], g & 1);
        FENCE();
      }
    }
  };
  int buf = 0;
  auto stage = [&](auto first) {
    bar();
    const char* st = lds + buf * B_STG;
    kstep(IC<!decltype(first)::value>{}, Y, X, st);
    kstep(IC<1>{}, X, Y, st + 16 * 256);
    buf = buf == B_NB - 1 ? 0 : buf + 1;
  };
  stage(IC<1>{});
  STAMP(0, 8);
#pragma unroll 1
  for (int s = 1; s < B_NST; ++s) stage(IC<0>{});
  STAMP(0, 9);
  {
    const u32x4 sw = __builtin_bit_cast(u32x4, Y.b[0]);
#pragma unroll
    for (int i = 0; i < 4; ++i) {
#pragma unroll
      for (int t = 0; t < 2; ++t)
        acc[i][t] = __builtin_amdgcn_mfma_f32_32x32x16_bf16(__builtin_bit_cast(bf16x8, Y.a[i]), __builtin_bit_cast(bf16x8, Y.b[t]), acc[i][t], 0, 0, 0);
      if (i == 0) { stat1(sw[0]); stat1(sw[1]); stat1(sw[2]); stat1(sw[3]); }
      if (i == 1) {
        const float a = s0 + s1, c = q0 + q1;
        const u32x2 ra = __builtin_amdgcn_permlane32_swap(__float_as_uint(a), __float_as_uint(a), false, false);
        const u32x2 rc = __builtin_amdgcn_permlane32_swap(__float_as_uint(c), __float_as_uint(c), false, false);
        const float mean = (__uint_as_float(ra[0]) + __uint_as_float(ra[1])) * (1.f / DM);
        float var = (__uint_as_float(rc[0]) + __uint_as_float(rc[1])) * (1.f / DM) - mean * mean; var = var < 0.f ? 0.f : var;
        const float sd = rbf(__builtin_sqrtf(rbf(rbf(var) + 1e-5f)));
        const float rs = __builtin_amdgcn_rcpf(sd);
        if (lane < 32) ((f32x2*)(lds + B_ST))[(2 * tp + ag) * 32 + lane] = (f32x2){-rbf(mean), rs * rs};
      }
      FENCE();
    }
  }
  asm volatile("s_waitcnt lgkmcnt(0)" ::: "memory");
  bar();
  const float* cs = (const float*)(lds + B_CS);
  const int hh = lane >> 5;
  const rsrc_t rH = mkrsrc(ws + WS_H);
  f32x2 st2[2];
#pragma unroll
  for (int t = 0; t < 2; ++t) st2[t] = ((const f32x2*)(lds + B_ST))[(2 * tp + (t ^ ag)) * 32 + (lane & 31)];
#pragma unroll
  for (int i = 0; i < 4; ++i) {
    const int f0 = 16 * (4 * ag + i) + 4 * hh;
    const f32x4 g0 = *(const f32x4*)(cs + f0), g1 = *(const f32x4*)(cs + f0 + 8);
    const f32x4 u0 = *(const f32x4*)(cs + 128 + f0), u1 = *(const f32x4*)(cs + 128 + f0 + 8);
#pragma unroll
    for (int t = 0; t < 2; ++t) {
      const f32x2 nm = {st2[t][0], st2[t][0]}, r2 = {st2[t][1], st2[t][1]};
      u32x4 o;
#pragma unroll
      for (int r = 0; r < 4; ++r) {
        const f32x4 cg = r < 2 ? g0 : g1, cu = r < 2 ? u0 : u1;
        const int e = 2 * (r & 1);
        const f32x2 g = __builtin_elementwise_fma(nm, (f32x2){cg[e], cg[e + 1]}, (f32x2){acc[i][t][2 * r], acc[i][t][2 * r + 1]});
        const f32x2 u = __builtin_elementwise_fma(nm, (f32x2){cu[e], cu[e + 1]}, (f32x2){acc[i][t][8 + 2 * r], acc[i][t][8 + 2 * r + 1]});
        const f32x2 hv = (g * r2) * u;
        o[r] = pk_bf16(hv[0], hv[1]);
      }
      __builtin_amdgcn_raw_buffer_store_b128(o, rH, (((4 * m + 2 * tp + (t ^ ag)) * 112 + 8 * nb + 4 * ag + i) * 64 + lane) * 16, 0, 16);
    }
  }
  STAMP(0, 10);
  asm volatile("s_waitcnt vmcnt(0)" ::: "memory");
  }
  const int mm = 4 * xcd + j / 7, nn = j % 7;
  const rsrc_t rHc = mkrsrc(ws + WS_H + ((size_t)((2 * mm + (lw >> 1)) * 112 + 2 * (lw & 1)) * 64) * 16);
  const rsrc_t rw = mkrsrc(wd + 64 * nn);
  const int crow = 16 * lw + (lane >> 3), ccp = lane & 7;
  const u32 cvh = lane * 16, cvw = (crow * DM + (ccp ^ (((crow >> 1) & 1) << 2)) * 8) * 2;
  const u32 cdl = lds_addr(lds) + lw * 2048;
  auto cW = [&](int s, int slot) { const u32 d = cdl + C_W0 + slot * 8192, sw = s * 64 * DM * 2; bdma16(rw, cvw, sw, d); bdma16(rw, cvw, sw + 8 * DM * 2, d + 1024); };
  auto cH = [&](int s, int slot) { const u32 d = cdl + slot * 8192, sh = s * 4096; bdma16_dev(rHc, cvh, sh, d); bdma16_dev(rHc, cvh, sh + 1024, d + 1024); };
  if (wv >= 4) {
#pragma unroll
    for (int s = 0; s < 6; ++s) cW(s, s);
  }
  bar();
  if (tid == 0) __hip_atomic_store((gu32*)(ws + WS_FLB) + (m * 14 + nb) * 32, tag, RLX_AGENT);
  STAMP(0, 11);
  if (wv >= 4) {
    {
      gu32* fl = (gu32*)(ws + WS_FLB) + ((mm >> 1) * 14 + (lane < 14 ? lane : 0)) * 32;
      for (int spin = 0; spin < (1 << 16); ++spin) {
        if (__all(__hip_atomic_load(fl, RLX_AGENT) == tag)) break;
        __builtin_amdgcn_s_sleep(2);
      }
    }
    STAMP(0, 12);
#pragma unroll
    for (int s = 0; s < 10; ++s) cH(s, s);
    int hs_ = 10, hslot = 10, wsn = 6, wslot = 6;
    auto issue2 = [&](bool h_, bool w_) {
      if (w_) { cW(wsn, wslot); ++wsn; wslot = (wslot + 1) & 7; }
      if (h_) { cH(hs_, hslot); ++hs_; hslot = hslot == C_HN - 1 ? 0 : hslot + 1; }
    };
    wait_vm<18>(); bar(); issue2(true, true);
    wait_vm<20>(); bar(); issue2(true, true);
    wait_vm<22>(); bar(); issue2(true, true);
    wait_vm<24>(); bar(); issue2(true, true);
    wait_vm<26>(); bar(); issue2(true, true);
    wait_vm<28>(); bar(); issue2(true, true);
#pragma unroll 1
    for (int s = 6; s < 18; ++s) { wait_vm<22>(); bar(); issue2(true, true); }
    wait_vm<22>(); bar(); issue2(false, true);
    wait_vm<20>(); bar(); issue2(false, true);
    wait_vm<18>(); bar(); issue2(false, true);
    wait_vm<16>(); bar(); issue2(false, true);
    wait_vm<14>(); bar();
    wait_vm<10>(); bar();
    wait_vm<6>(); bar();
    wait_vm<4>(); bar();
    wait_vm<2>(); bar();
    wait_vm<0>(); bar();
    STAMP(0, 13);
    STAMP_FLUSH(0);
    return;
  }
  const int dtl = wv & 1, tt = wv >> 1;
  const int tok = 64 * mm + 32 * tt + (lane & 31);
  const rsrc_t rres = mkrsrc(ws + WS_HT);
  u32 res[16];
#pragma unroll
  for (int i = 0; i < 16; ++i) res[i] = __builtin_amdgcn_raw_buffer_load_b16(rres, ((64 * nn + 32 * dtl + 8 * (i >> 2) + 4 * (lane >> 5) + (i & 3)) * HS + tok) * 2, 0, 16);
  const int cgrp = lane >> 4, cq = (lane & 15) >> 2, cp_ = lane & 3, chh = cgrp >> 1;
  const int achunk = 4 * dtl + 2 * (cgrp & 1) + (cp_ >> 1);
  const int caoff = C_W0 + (4 * chh + cq) * 128 + ((achunk ^ (((cq >> 1) & 1) << 2)) * 16) + (cp_ & 1) * 8;
  const int cboff = tt * 4096 + lane * 16;
  f32x16 cacc = {};
  struct F4 { s16x8 a[4]; bf16x8 b[4]; };
  F4 Xc, Yc;
  int chb = 0, cwb = 0;
  auto cstage = [&](auto first, F4& cur, const F4& prev) {
    bar();
    const char* sth = lds + chb * 8192;
    const char* stw = lds + cwb * 8192;
#pragma unroll
    for (int k = 0; k < 4; ++k) {
      if (!decltype(first)::value) cacc = __builtin_amdgcn_mfma_f32_32x32x16_bf16(__builtin_bit_cast(bf16x8, prev.a[k]), prev.b[k], cacc, 0, 0, 0);
      cur.a[k] = tr_frag(stw + caoff + k * 16 * 128, stw + caoff + (k * 16 + 8) * 128);
      cur.b[k] = *(const bf16x8*)(sth + cboff + k * 1024);
      FENCE();
    }
    chb = chb == C_HN - 1 ? 0 : chb + 1; cwb = (cwb + 1) & 7;
  };
  cstage(IC<1>{}, Xc, Yc);
  STAMP(0, 12);
  cstage(IC<0>{}, Yc, Xc);
#pragma unroll 1
  for (int s = 2; s < C_NST; s += 2) { cstage(IC<0>{}, Xc, Yc); cstage(IC<0>{}, Yc, Xc); }
#pragma unroll
  for (int k = 0; k < 4; ++k) cacc = __builtin_amdgcn_mfma_f32_32x32x16_bf16(__builtin_bit_cast(bf16x8, Yc.a[k]), Yc.b[k], cacc, 0, 0, 0);
  STAMP(0, 13);
  const int hh = lane >> 5;
  u16* op = out + tok * DM + 64 * nn + 32 * dtl + 4 * hh;
#pragma unroll
  for (int g = 0; g < 4; ++g)
    *(u32x2*)(op + 8 * g) = (u32x2){pk_bf16(bf2f(res[4 * g]) + rbf(cacc[4 * g]), bf2f(res[4 * g + 1]) + rbf(cacc[4 * g + 1])),
                                    pk_bf16(bf2f(res[4 * g + 2]) + rbf(cacc[4 * g + 2]), bf2f(res[4 * g + 3]) + rbf(cacc[4 * g + 3]))};
  STAMP(0, 14);
  STAMP_FLUSH(0);
}

extern "C" void kernel_launch(void* const* d_in, const int* in_sizes, int n_in, void* d_out, int out_size, void* d_ws, size_t ws_size,
                              hipStream_t stream) {
  (void)in_sizes; (void)n_in; (void)out_size; (void)ws_size;
  (void)hipFuncSetAttribute((const void*)fused_kernel, hipFuncAttributeMaxDynamicSharedMemorySize, K_LDS);
  fused_kernel<<<256, 512, K_LDS, stream>>>((const u16*)d_in[0], (const u16*)d_in[1], (const u16*)d_in[2], (const u16*)d_in[3], (const u16*)d_in[4],
                                            (const u16*)d_in[5], (const u16*)d_in[6], (const u16*)d_in[7], (const u16*)d_in[8],
                                            (const u16*)d_in[10], (const u16*)d_in[11], (const u16*)d_in[12], (char*)d_ws, (u16*)d_out);
}
```

```cpp
#include <hip/hip_runtime.h>
#include <stdint.h>

typedef unsigned short u16;
typedef unsigned int u32;
typedef unsigned long long u64;
typedef float f32x2 __attribute__((ext_vector_type(2)));
typedef float f32x4 __attribute__((ext_vector_type(4)));
typedef float f32x16 __attribute__((ext_vector_type(16)));
typedef unsigned u32x2 __attribute__((ext_vector_type(2)));
typedef unsigned u32x4 __attribute__((ext_vector_type(4)));
typedef short s16x4 __attribute__((ext_vector_type(4)));
typedef short s16x8 __attribute__((ext_vector_type(8)));
typedef __bf16 bf16x8 __attribute__((ext_vector_type(8)));
typedef __bf16 bf16x2 __attribute__((ext_vector_type(2)));
typedef _Float16 f16x8 __attribute__((ext_vector_type(8)));
typedef _Float16 f16x4 __attribute__((ext_vector_type(4)));
typedef _Float16 f16x2 __attribute__((ext_vector_type(2)));
#define DI __device__ __forceinline__
#define LAS __attribute__((address_space(3)))
#define GAS __attribute__((address_space(1)))
typedef GAS unsigned gu32;
typedef GAS unsigned long long gu64;
#define RLX_AGENT __ATOMIC_RELAXED, __HIP_MEMORY_SCOPE_AGENT
#define FENCE() __builtin_amdgcn_sched_barrier(0)

constexpr int SL = 2048, DM = 448, NS = 256, FF = 1792;
constexpr int HS = SL + 64;

constexpr size_t WS_EPOCH = 0;
constexpr size_t WS_GRAN  = 4096;
constexpr size_t WS_CS    = 32768;
constexpr size_t WS_FLA   = 65536;
constexpr size_t WS_FLB   = WS_FLA + 32768;
constexpr size_t WS_GRAN2 = 163840;
constexpr size_t WS_XG    = 196608;
constexpr size_t WS_HT    = WS_XG + (size_t)DM * 8192;
constexpr size_t WS_H     = WS_HT + (size_t)DM * HS * 2;
constexpr size_t WS_DBG   = WS_H + (size_t)SL * FF * 2;

#define STAMP_DECL do {} while (0)
#define STAMP(k, i) do {} while (0)
#define STAMP_FLUSH(k) do {} while (0)

DI float bf2f(u32 v) { return __uint_as_float(v << 16); }
DI float bfhi(u32 v) { return __uint_as_float(v & 0xffff0000u); }
DI u32 pk_bf16(float a, float b) { f32x2 v = {a, b}; return __builtin_bit_cast(u32, __builtin_convertvector(v, bf16x2)); }
DI u32 pk_f16(float a, float b) { f32x2 v = {a, b}; return __builtin_bit_cast(u32, __builtin_convertvector(v, f16x2)); }
DI u32 pk2(f32x2 v) { return __builtin_bit_cast(u32, __builtin_convertvector(v, f16x2)); }
DI float rbf(float a) { return __uint_as_float(pk_bf16(a, a) << 16); }

template <int CTRL, int RM> DI float dpp0(float v) { return __int_as_float(__builtin_amdgcn_update_dpp(0, __float_as_int(v), CTRL, RM, 0xf, true)); }
DI float lane63(float v) { return __int_as_float(__builtin_amdgcn_readlane(__float_as_int(v), 63)); }
DI void wave_sum2(float& a, float& b) {
  a += dpp0<0x111, 0xf>(a); b += dpp0<0x111, 0xf>(b); a += dpp0<0x112, 0xf>(a); b += dpp0<0x112, 0xf>(b);
  a += dpp0<0x114, 0xf>(a); b += dpp0<0x114, 0xf>(b); a += dpp0<0x118, 0xf>(a); b += dpp0<0x118, 0xf>(b);
  a += dpp0<0x142, 0xa>(a); b += dpp0<0x142, 0xa>(b); a += dpp0<0x143, 0xc>(a); b += dpp0<0x143, 0xc>(b);
  a = lane63(a); b = lane63(b);
}
DI void wave_max_sum(float& m, float& s) {
  m = fmaxf(m, dpp0<0x111, 0xf>(m)); s += dpp0<0x111, 0xf>(s); m = fmaxf(m, dpp0<0x112, 0xf>(m)); s += dpp0<0x112, 0xf>(s);
  m = fmaxf(m, dpp0<0x114, 0xf>(m)); s += dpp0<0x114, 0xf>(s); m = fmaxf(m, dpp0<0x118, 0xf>(m)); s += dpp0<0x118, 0xf>(s);
  m = fmaxf(m, dpp0<0x142, 0xa>(m)); s += dpp0<0x142, 0xa>(s); m = fmaxf(m, dpp0<0x143, 0xc>(m)); s += dpp0<0x143, 0xc>(s);
  m = lane63(m); s = lane63(s);
}

DI void glds16(const void* g, u32 lds_dst) {
  u32 keep;
  asm volatile("s_mov_b32 %0, m0\n\ts_mov_b32 m0, %2\n\ts_nop 0\n\tglobal_load_lds_dwordx4 %1, off\n\ts_mov_b32 m0, %0"
               : "=&s"(keep) : "v"(g), "s"(lds_dst) : "memory");
}
typedef __amdgpu_buffer_rsrc_t rsrc_t;
DI rsrc_t mkrsrc(const void* p) { return __builtin_amdgcn_make_buffer_rsrc((void*)p, 0, 0x7fffffff, 0x00020000); }
DI void bdma16(rsrc_t rs, u32 voff, u32 soff, u32 lds_dst) {
  asm volatile("s_mov_b32 m0, %0\n\ts_nop 0\n\tbuffer_load_dwordx4 %1, %2, %3 offen lds" :: "s"(lds_dst), "v"(voff), "s"(rs), "s"(soff) : "memory");
}
DI void bdma16_dev(rsrc_t rs, u32 voff, u32 soff, u32 lds_dst) {
  asm volatile("s_mov_b32 m0, %0\n\ts_nop 0\n\tbuffer_load_dwordx4 %1, %2, %3 offen sc1 lds" :: "s"(lds_dst), "v"(voff), "s"(rs), "s"(soff) : "memory");
}
DI void warm16(u32x4& sink, rsrc_t rs, u32 voff, u32 soff) {
  asm volatile("buffer_load_dwordx4 %0, %1, %2, %3 offen" : "+v"(sink) : "v"(voff), "s"(rs), "s"(soff) : "memory");
}
DI void bdma4(rsrc_t rs, u32 voff, u32 soff, u32 lds_dst) {
  asm volatile("s_mov_b32 m0, %0\n\ts_nop 0\n\tbuffer_load_dword %1, %2, %3 offen lds" :: "s"(lds_dst), "v"(voff), "s"(rs), "s"(soff) : "memory");
}
DI u32 lds_addr(const void* p) { return (u32)(size_t)(LAS const char*)p; }
DI s16x8 tr_frag(const char* p0, const char* p1) {
  s16x4 a = __builtin_amdgcn_ds_read_tr16_b64_v4i16((LAS s16x4*)p0);
  s16x4 b = __builtin_amdgcn_ds_read_tr16_b64_v4i16((LAS s16x4*)p1);
  return __builtin_shufflevector(a, b, 0, 1, 2, 3, 4, 5, 6, 7);
}

struct IdxTab { u16 v[2064]; };
constexpr int tap_entry(int l) {
  if (l < 0) return 21 * 32;
  int lam = l;
  if (l >= 256) {
    int sh = l >= 1024 ? 3 : (l >= 512 ? 2 : 1);
    int q = l >> sh, rem = l & ((1 << sh) - 1), half = 1 << (sh - 1);
    if (rem > half || (rem == half && (q & 1))) q += 1;
    lam = q << sh;
  }
  int hq = lam >> 8, lo = lam & 255, c = lo >> 3, r = 0;
  if (hq == 0) r = lo & 7;
  else if (hq == 1) r = 8 + ((lo & 7) >> 1);
  else if (hq == 2) r = 12 + ((lo & 7) >> 2);
  else if (hq == 3) r = 14 + ((lo & 7) >> 2);
  else if (hq < 8) r = 16 + hq - 4;
  else { r = 20; c = 0; }
  return r * 32 + c;
}
constexpr IdxTab make_tab() { IdxTab t{}; for (int y = 0; y < 2064; ++y) t.v[y] = (u16)(2 * tap_entry(2047 - y)); return t; }
__device__ const IdxTab RIDX __attribute__((aligned(16))) = make_tab();

DI float pow2_for(float mx, int target) {
  int e = (int)(__float_as_uint(mx) >> 23);
  int be = 127 + target - (e - 126);
  be = be > 254 ? 254 : (be < 1 ? 1 : be);
  return __uint_as_float((u32)be << 23);
}
DI float pow2_inv(float p) { return __uint_as_float((254u - (__float_as_uint(p) >> 23)) << 23); }

struct CM { f32x2 a, b; };
DI CM mk(f32x2 e) { return {e, (f32x2){-e.y, e.x}}; }
DI f32x2 cmul(f32x2 p, CM m) {
  f32x2 t = __builtin_shufflevector(p, p, 0, 0) * m.a;
  return __builtin_elementwise_fma(__builtin_shufflevector(p, p, 1, 1), m.b, t);
}
DI f32x2 cexp_s(float zr, float zi, float s) {
  float m = __builtin_amdgcn_exp2f(zr * (s * 1.44269504088896341f));
  float a = __builtin_amdgcn_fractf(zi * (s * 0.15915494309189535f));
  return (f32x2){m * __builtin_amdgcn_cosf(a), m * __builtin_amdgcn_sinf(a)};
}
template <int OFF>
DI void put4(u32 base, u32 v0, u32 v1, u32 v2, u32 v3) {
  asm volatile("s_mov_b32 m0, %0\n\ts_nop 0\n\tds_write_addtid_b32 %1 offset:%5\n\tds_write_addtid_b32 %2 offset:%6\n\t"
               "ds_write_addtid_b32 %3 offset:%7\n\tds_write_addtid_b32 %4 offset:%8"
               :: "s"(base), "v"(v0), "v"(v1), "v"(v2), "v"(v3), "n"(OFF), "n"(OFF + 272), "n"(OFF + 544), "n"(OFF + 816) : "memory");
}

template <int V> struct IC { static constexpr int value = V; };
template <int N> DI void wait_vm() { asm volatile("s_waitcnt vmcnt(%0)" :: "n"(N) : "memory"); }
DI void glds4(const void* g, u32 lds_dst) {
  u32 keep;
  asm volatile("s_mov_b32 %0, m0\n\ts_mov_b32 m0, %2\n\ts_nop 0\n\tglobal_load_lds_dword %1, off\n\ts_mov_b32 m0, %0"
               : "=&s"(keep) : "v"(g), "s"(lds_dst) : "memory");
}
DI void bar() { __builtin_amdgcn_s_barrier(); }

constexpr int A_T1 = 0, A_T2 = 8192;
constexpr int A_SCR = 25088, A_SCRW = 32 * 272 + 21 * 272;
constexpr int A_CP = 0, A_CPS = 4160, A_CPC = 8 * A_CPS;
constexpr int A_EX = 2 * A_CPC, A_EXW = 64 * 144, A_XX = A_EX + 4 * A_EXW;
constexpr int A_UU = A_SCR + 8 * A_SCRW, A_UUC = 95 * 80;
constexpr int A_TB = A_UU + 2 * A_UUC, A_TBC = 22 * 32 * 2;
constexpr int A_SW = A_TB + 2 * A_TBC;
constexpr int A_LDS = A_SW + 64;
static_assert(A_LDS <= 163840 && A_XX + 10240 <= A_UU && (A_SCR % 16) == 0 && (A_SCRW % 16) == 0 && (A_UU % 16) == 0, "lds");

constexpr int SLOT = 8192, P_W = 12 * SLOT;
constexpr int B_CS = 19 * SLOT, B_CNT = 163840 - 16, CNT0 = 163840 - 12, CNT1 = 163840 - 8;

constexpr int K_LDS = 163840;
constexpr int CODE_WARM_KB = 64;
static_assert(A_LDS <= K_LDS - 16, "lds");

__global__ __launch_bounds__(512, 2) void fused_kernel(const u16* __restrict__ x, const u16* __restrict__ Are, const u16* __restrict__ Aim,
    const u16* __restrict__ Bre, const u16* __restrict__ Bim, const u16* __restrict__ Cre, const u16* __restrict__ Cim,
    const u16* __restrict__ logdt, const u16* __restrict__ Dskip, const u16* __restrict__ wg, const u16* __restrict__ wu,
    const u16* __restrict__ wd, char* ws, u16* __restrict__ out) {
  extern __shared__ __attribute__((aligned(16))) char lds[];
  char* dbg = ws + WS_DBG; (void)dbg;
  STAMP_DECL;
  const int tid = threadIdx.x, lane = tid & 63;
  const int wv = __builtin_amdgcn_readfirstlane(tid >> 6);
  const int b = blockIdx.x, xcd = b & 7, j = b >> 3;
  const bool worker = j < 28;
  STAMP(0, 0);
  asm volatile("" :: "s"(x), "s"(Are), "s"(Aim), "s"(Bre), "s"(Bim), "s"(Cre), "s"(Cim), "s"(logdt), "s"(Dskip), "s"(wg), "s"(wu), "s"(wd), "s"(ws), "s"(out));
  gu32* epoch_p = (gu32*)(ws + WS_EPOCH);
  gu64* gran = (gu64*)(ws + WS_GRAN);

  const int w = worker ? xcd * 28 + j : 0, c0 = 2 * w;
  const int trow = 8 * b + wv;
  const u32x4 xr = *(const u32x4*)(x + trow * DM + (lane < 56 ? lane : 0) * 8);
  const u32 tag = __hip_atomic_load(epoch_p, RLX_AGENT) + 1u;
  const int ch = tid >> 8, n = tid & 255;
  const int idx = (c0 + ch) * NS + n;
  const u32 p_ldt = logdt[c0 + ch];
  const u32 p_are = Are[idx], p_aim = Aim[idx];
  const u32x4 xt = *(const u32x4*)(x + (128 * (w & 15) + (tid >> 2)) * DM + 32 * (w >> 4) + (tid & 3) * 8);
  const u32 p_bre = Bre[idx], p_bim = Bim[idx], p_cre = Cre[idx], p_cim = Cim[idx];
  const u32 p_d01 = *(const u32*)(Dskip + c0);
  const u32x4 ix0 = *(const u32x4*)(RIDX.v + 8 * (tid & 255)), ix1 = *(const u32x4*)(RIDX.v + 8 * (tid & 255) + 8);
  if (tid == 0) { *(u32*)(lds + B_CNT) = 0u; *(u32*)(lds + CNT0) = 0u; *(u32*)(lds + CNT1) = 0u; }
  {
    const float piv = __uint_as_float((u32)__builtin_amdgcn_readfirstlane(xr[0]) << 16);
    float s = 0.f, q = 0.f, s2 = 0.f, q2 = 0.f;
#pragma unroll
    for (int i = 0; i < 4; ++i) {
      const float lo = bf2f(xr[i] & 0xffffu) - piv, hi = bfhi(xr[i]) - piv;
      s += lo; s2 += hi; q = fmaf(lo, lo, q); q2 = fmaf(hi, hi, q2);
    }
    s = lane < 56 ? s + s2 : 0.f; q = lane < 56 ? q + q2 : 0.f;
    wave_sum2(s, q);
    const float ms = s * (1.f / DM);
    float var = q * (1.f / DM) - ms * ms; var = var < 0.f ? 0.f : var;
    const float sd = rbf(__builtin_sqrtf(rbf(rbf(var) + 1e-5f)));
    if (lane == 0) __hip_atomic_store(gran + trow, ((u64)tag << 32) | pk_bf16(piv + ms, sd), RLX_AGENT);
  }
  STAMP(0, 1);
  if (!worker) {
    {
      u64 pc; u32 len, len2;
      asm volatile("s_getpc_b64 %0\n.Lfused_pc:\n\ts_mov_b32 %1, .Lfused_end-.Lfused_pc\n\ts_mov_b32 %2, .Lfused_end2-.Lfused_pc" : "=s"(pc), "=s"(len), "=s"(len2));
      len = (int)len2 > (int)len ? len2 : len;
      const char* code = (const char*)pc;
      u32x4 cw[CODE_WARM_KB / 8];
#pragma unroll
      for (int i = 0; i < CODE_WARM_KB / 8; ++i) {
        const u32 o = (u32)(i * 512 + tid) * 16u;
        cw[i] = *(const GAS u32x4*)(code + (((int)len > 16 && o + 16u <= len) ? o : 0u));
      }
      u32 acc = 0;
#pragma unroll
      for (int i = 0; i < CODE_WARM_KB / 8; ++i) acc |= cw[i][0] & cw[i][1] & cw[i][2] & cw[i][3];
      asm volatile("" :: "v"(acc));
    }
    {
      u32 t0 = 0;
      if (wv == 1) {
#pragma unroll
        for (int i = 0; i < 4; ++i) t0 |= *(const GAS u32*)(ws + (size_t)(i * 64 + lane) * 65536);
      } else if (wv == 2 && lane < 28) t0 = *(const GAS u32*)((const char*)out + lane * 65536);
      asm volatile("" :: "v"(t0));
    }
    const int s = xcd * 4 + (j - 28);
    const u16* W = (s >> 4) ? wu : wg;
    const int colb = (s & 15) * 112;
    float a[8] = {0, 0, 0, 0, 0, 0, 0, 0};
    const int cc = tid % 14, rg = tid / 14;
    u32 warm = 0;
    {
      u32x4 wv4[7];
#pragma unroll
      for (int i = 0; i < 7; ++i) { const int c = tid + 512 * i; wv4[i] = *(const u32x4*)(wd + (size_t)s * 25088 + (c < 3136 ? c : 0) * 8); }
#pragma unroll
      for (int i = 0; i < 7; ++i) warm |= wv4[i][0] & wv4[i][1] & wv4[i][2] & wv4[i][3];
      asm volatile("" :: "v"(warm));
    }
    if (tid < 448) {
      u32x4 v[14];
#pragma unroll
      for (int r = 0; r < 14; ++r) v[r] = *(const u32x4*)(W + (rg * 14 + r) * FF + colb + cc * 8);
#pragma unroll
      for (int r = 0; r < 14; ++r)
#pragma unroll
        for (int i = 0; i < 4; ++i) { a[2 * i] += bf2f(v[r][i] & 0xffffu); a[2 * i + 1] += bfhi(v[r][i]); }
      float* red = (float*)lds;
#pragma unroll
      for (int i = 0; i < 8; ++i) red[rg * 112 + cc * 8 + i] = a[i];
    }
    __syncthreads();
    if (tid < 112) {
      float t = 0.f;
      for (int r = 0; r < 32; ++r) t += ((float*)lds)[r * 112 + tid];
      __hip_atomic_store((gu64*)(ws + WS_CS) + (s >> 4) * FF + colb + tid, ((u64)tag << 32) | __float_as_uint(t), RLX_AGENT);
    }
    if (wv < 4) {
      gu32* fl = (gu32*)(ws + WS_FLA) + (wv * 56 + (lane < 56 ? lane : 0)) * 32;
      for (int spin = 0; spin < (1 << 16); ++spin) {
        if (__all(__hip_atomic_load(fl, RLX_AGENT) == tag)) break;
        __builtin_amdgcn_s_sleep(2);
      }
    }
    __syncthreads();
    {
      const rsrc_t rh2 = mkrsrc((const u16*)(ws + WS_HT) + 64 * s);
      u32x4 hv[7];
#pragma unroll
      for (int i = 0; i < 7; ++i) hv[i] = __builtin_amdgcn_raw_buffer_load_b128(rh2, (((tid >> 3) + 64 * i) * HS + (tid & 7) * 8) * 2, 0, 16);
      f32x2 sa[4], sq[4];
#pragma unroll
      for (int k = 0; k < 4; ++k) { sa[k] = (f32x2){0.f, 0.f}; sq[k] = (f32x2){0.f, 0.f}; }
#pragma unroll
      for (int i = 0; i < 7; ++i)
#pragma unroll
        for (int k = 0; k < 4; ++k) { const u32 wi = hv[i][k]; const f32x2 v = {bf2f(wi & 0xffffu), bfhi(wi)}; sa[k] += v; sq[k] = __builtin_elementwise_fma(v, v, sq[k]); }
      f32x4* r1 = (f32x4*)(lds + (tid >> 3) * 528 + (tid & 7) * 64);
#pragma unroll
      for (int k = 0; k < 4; ++k) r1[k] = (f32x4){sa[k][0], sq[k][0], sa[k][1], sq[k][1]};
      __syncthreads();
      f32x2 t2 = {0.f, 0.f};
#pragma unroll
      for (int i = 0; i < 8; ++i) t2 += *(const f32x2*)(lds + ((tid >> 6) * 8 + i) * 528 + (tid & 63) * 8);
      *(f32x2*)(lds + 34816 + (tid >> 6) * 512 + (tid & 63) * 8) = t2;
      __syncthreads();
      if (tid < 64) {
        f32x2 tot = {0.f, 0.f};
#pragma unroll
        for (int i = 0; i < 8; ++i) tot += *(const f32x2*)(lds + 34816 + i * 512 + tid * 8);
        const float mean = tot[0] * (1.f / DM);
        float var = tot[1] * (1.f / DM) - mean * mean; var = var < 0.f ? 0.f : var;
        const float sd = rbf(__builtin_sqrtf(rbf(rbf(var) + 1e-5f)));
        __hip_atomic_store((gu64*)(ws + WS_GRAN2) + 64 * s + tid, ((u64)tag << 32) | pk_bf16(mean, sd), RLX_AGENT);
      }
    }
    STAMP_FLUSH(0);
    return;
  }
  *(u32x4*)(lds + A_T1 + tid * 16) = xt;
  const float dt = rbf(__builtin_amdgcn_exp2f(bf2f(p_ldt) * 1.44269504088896341f));
  const float zr = dt * bf2f(p_are), zi = dt * bf2f(p_aim);
  const f32x2 E8 = cexp_s(zr, zi, 8.f);
  const f32x2 E1 = cexp_s(zr, -zi, 1.f), F = cexp_s(zr, -zi, 256.f);
  {
    const int grp = lane >> 4, q = (lane & 15) >> 2, p = lane & 3;
#pragma unroll
    for (int i = 0; i < 2; ++i) {
      const int tb = 16 * wv + 8 * i + 4 * (grp >> 1);
      const s16x4 v = __builtin_amdgcn_ds_read_tr16_b64_v4i16((LAS s16x4*)(lds + A_T1 + (tb + q) * 64 + (grp & 1) * 32 + p * 8));
      const u32x2 vw = __builtin_bit_cast(u32x2, v);
      *(u32x4*)(lds + A_T2 + (16 * (grp & 1) + (lane & 15)) * 528 + tb * 4) = (u32x4){vw[0], tag, vw[1], tag};
    }
  }
  const u32 scb = lds_addr(lds) + A_SCR + wv * A_SCRW;
  const CM M8 = mk(E8);
  f32x2 p0 = {16384.f, 0.f}, p1 = cmul(p0, M8), p2 = cmul(p1, M8), p3 = cmul(p2, M8);
  const CM M32 = mk(cmul(p2, mk(p2 * (1.f / 16384.f))) * (1.f / 16384.f));
  __syncthreads();
  {
    __amdgpu_buffer_rsrc_t rs = __builtin_amdgcn_make_buffer_rsrc((void*)(ws + WS_XG), 0, DM * 8192, 0x00020000);
#pragma unroll
    for (int k = 0; k < 2; ++k) {
      const int id = tid + 512 * k, c = id >> 5, pos = id & 31;
      const u32x4 v = *(const u32x4*)(lds + A_T2 + c * 528 + pos * 16);
      __builtin_amdgcn_raw_buffer_store_b128(v, rs, (32 * (w >> 4) + c) * 8192 + (w & 15) * 512 + pos * 16, 0, 16);
    }
  }
  STAMP(0, 2);
  {
    const float br = dt * bf2f(p_bre), bi = dt * bf2f(p_bim);
    const float cr = bf2f(p_cre), ci = bf2f(p_cim);
    const float CBr = cr * br - ci * bi, CBi = cr * bi + ci * br;
    float mx = fmaxf(fabsf(CBr), fabsf(CBi)), sabs = fabsf(CBr) + fabsf(CBi);
    wave_max_sum(mx, sabs);
    const float scA = pow2_for(mx, 14);
    if (lane == 0) { ((float*)(lds + A_SW))[wv] = pow2_inv(scA) * (1.f / 16384.f); ((float*)(lds + A_SW))[8 + wv] = sabs; }
#define COL4(R) { put4<(R) * 272>(scb, pk2(p0), pk2(p1), pk2(p2), pk2(p3)); \
                  if ((R) < 28) { p0 = cmul(p0, M32); p1 = cmul(p1, M32); p2 = cmul(p2, M32); p3 = cmul(p3, M32); } }
    COL4(0) COL4(4) COL4(8) COL4(12) COL4(16) COL4(20) COL4(24) COL4(28)
#undef COL4
    const CM M1 = mk(E1), MF = mk(F);
    const f32x2 E2 = cmul(E1, M1);
    const CM M2 = mk(E2), M4 = mk(cmul(E2, M2));
    const f32x2 X0 = {CBr * scA, -CBi * scA};
    const f32x2 X1 = cmul(X0, M1), G1 = cmul(X0, MF);
    const f32x2 X2 = cmul(X0, M2), X3 = cmul(X1, M2), G2 = cmul(G1, MF), Y1 = cmul(G1, M2);
    put4<(32 + 0) * 272>(scb, pk2(X0), pk2(X1), pk2(X2), pk2(X3));
    const f32x2 X4 = cmul(X0, M4), X5 = cmul(X1, M4), X6 = cmul(X2, M4), X7 = cmul(X3, M4);
    const f32x2 G3 = cmul(G2, MF), Y2 = cmul(G1, M4), Y3 = cmul(Y1, M4);
    put4<(32 + 4) * 272>(scb, pk2(X4), pk2(X5), pk2(X6), pk2(X7));
    put4<(32 + 8) * 272>(scb, pk2(G1), pk2(Y1), pk2(Y2), pk2(Y3));
    const f32x2 G4 = cmul(G3, MF), Z2 = cmul(G2, M4), Z3 = cmul(G3, M4);
    put4<(32 + 12) * 272>(scb, pk2(G2), pk2(Z2), pk2(G3), pk2(Z3));
    const CM MF2 = mk(cmul(F, MF));
    const f32x2 G5 = cmul(G3, MF2), G6 = cmul(G4, MF2);
    const f32x2 G7 = cmul(G5, MF2), G8 = cmul(G6, MF2);
    put4<(32 + 16) * 272>(scb, pk2(G4), pk2(G5), pk2(G6), pk2(G7));
    {
      const u32 g8 = pk2(G8);
      asm volatile("s_mov_b32 m0, %0\n\ts_nop 0\n\tds_write_addtid_b32 %1 offset:%2" :: "s"(scb), "v"(g8), "n"((32 + 20) * 272) : "memory");
    }
    char* scr = lds + A_SCR + wv * A_SCRW;
    const char* fr = scr + (lane & 31) * 272 + (lane >> 5) * 16;
    f16x8 bf[8], af[8];
#pragma unroll
    for (int ks = 0; ks < 8; ++ks) { bf[ks] = *(const f16x8*)(fr + ks * 32); af[ks] = *(const f16x8*)(fr + 32 * 272 + ks * 32); }
    f32x16 acc0 = {}, acc1 = {};
#pragma unroll
    for (int ks = 0; ks < 8; ks += 2) {
      acc0 = __builtin_amdgcn_mfma_f32_32x32x16_f16(af[ks], bf[ks], acc0, 0, 0, 0);
      acc1 = __builtin_amdgcn_mfma_f32_32x32x16_f16(af[ks + 1], bf[ks + 1], acc1, 0, 0, 0);
    }
#pragma unroll
    for (int r = 0; r < 12; ++r) {
      int row = (r & 3) + 8 * (r >> 2) + 4 * (lane >> 5);
      *(float*)(scr + row * 128 + (lane & 31) * 4) = acc0[r] + acc1[r];
    }
  }
  STAMP(0, 3);
  const __amdgpu_buffer_rsrc_t rs_g = __builtin_amdgcn_make_buffer_rsrc((void*)(ws + WS_GRAN), 0, SL * 8, 0x00020000);
  const __amdgpu_buffer_rsrc_t rs_x = __builtin_amdgcn_make_buffer_rsrc((void*)(ws + WS_XG + (size_t)c0 * 8192), 0, 2 * 8192, 0x00020000);
  u32x4 g[4], xg[4];
  const int t8 = tid & 255;
  auto fetch = [&]() {
#pragma unroll
    for (int i = 0; i < 4; ++i) g[i] = __builtin_amdgcn_raw_buffer_load_b128(rs_g, t8 * 64 + i * 16, 0, 16);
#pragma unroll
    for (int i = 0; i < 4; ++i) xg[i] = __builtin_amdgcn_raw_buffer_load_b128(rs_x, (i >> 1) * 8192 + t8 * 32 + (i & 1) * 16, 0, 16);
  };
  if (wv < 4) fetch();
  asm volatile("s_waitcnt lgkmcnt(0)" ::: "memory");
  bar();
  float kinv0, kinv1;
  {
    const float* sw = (const float*)(lds + A_SW);
    const float k0 = pow2_for(sw[8] + sw[9] + sw[10] + sw[11], 15), k1 = pow2_for(sw[12] + sw[13] + sw[14] + sw[15], 15);
    kinv0 = pow2_inv(k0); kinv1 = pow2_inv(k1);
    const float ks = ch ? k1 : k0;
    const float i0 = sw[4 * ch] * ks, i1 = sw[4 * ch + 1] * ks, i2 = sw[4 * ch + 2] * ks, i3 = sw[4 * ch + 3] * ks;
    const char* p = lds + A_SCR + (4 * ch) * A_SCRW + (tid & 255) * 4;
    float v[3][4];
#pragma unroll
    for (int it = 0; it < 3; ++it)
#pragma unroll
      for (int k = 0; k < 4; ++k) v[it][k] = *(const float*)(p + it * 1024 + k * A_SCRW);
#pragma unroll
    for (int it = 0; it < 3; ++it) {
      const int e = (tid & 255) + 256 * it;
      const float t = v[it][0] * i0 + v[it][1] * i1 + v[it][2] * i2 + v[it][3] * i3;
      if (it < 2 || e < 672) *(_Float16*)(lds + A_TB + ch * A_TBC + e * 2) = (_Float16)t;
    }
    if (tid < 8) { int cc = tid >> 2; *(u32x4*)(lds + A_TB + cc * A_TBC + 21 * 64 + (tid & 3) * 16) = (u32x4){0, 0, 0, 0}; }
    if (tid < 310) { int cc = tid / 155, r = tid % 155; *(u32x4*)(lds + A_UU + cc * A_UUC + r * 16) = (u32x4){0, 0, 0, 0}; }
  }
  asm volatile("s_waitcnt lgkmcnt(0)" ::: "memory");
  bar();
  if (wv >= 4) {
#pragma unroll
   for (int ch = 0; ch < 2; ++ch) {
    const char* tb = lds + A_TB + ch * A_TBC;
    u32 P[8];
#pragma unroll
    for (int k = 0; k < 8; ++k) {
      u32 iw = k < 4 ? ix0[k] : ix1[k - 4];
      u32 lo = *(const u16*)(tb + (iw & 0xffffu));
      u32 hi = (k < 7) ? *(const u16*)(tb + (iw >> 16)) : 0u;
      P[k] = lo | (hi << 16);
    }
    u32 Q[7];
#pragma unroll
    for (int k = 0; k < 7; ++k) Q[k] = __builtin_amdgcn_alignbit(P[k + 1], P[k], 16);
    char* cp = lds + A_CP + ch * A_CPC + (tid & 255) * 16;
#pragma unroll
    for (int s = 0; s < 8; ++s) {
      const int off = 7 - s;
      u32x4 v;
      if ((off & 1) == 0) v = (u32x4){P[off / 2], P[off / 2 + 1], P[off / 2 + 2], P[off / 2 + 3]};
      else v = (u32x4){Q[off / 2], Q[off / 2 + 1], Q[off / 2 + 2], Q[off / 2 + 3]};
      *(u32x4*)(cp + s * A_CPS) = v;
    }
   }
    if (t8 < 48) { int cc = t8 / 24, r = t8 % 24; *(u32x4*)(lds + A_CP + cc * A_CPC + (r / 3) * A_CPS + (256 + r % 3) * 16) = (u32x4){0, 0, 0, 0}; }
    STAMP(0, 4);
  } else {
    for (int spin = 0; spin < (1 << 16); ++spin) {
      u32 bad = 0;
#pragma unroll
      for (int i = 0; i < 4; ++i) bad |= (g[i][1] ^ tag) | (g[i][3] ^ tag) | (xg[i][1] ^ tag) | (xg[i][3] ^ tag);
      if (__all(bad == 0)) break;
      __builtin_amdgcn_s_sleep(2);
      fetch();
    }
    STAMP(0, 4);
    float uu[2][8];
#pragma unroll
    for (int i = 0; i < 8; ++i) {
      const u32 st = g[i >> 1][(i & 1) * 2];
      const float m = bf2f(st & 0xffffu), rs_ = __builtin_amdgcn_rcpf(bfhi(st));
#pragma unroll
      for (int c = 0; c < 2; ++c) {
        const u32 xw = xg[2 * c + (i >> 2)][(i & 2)];
        const float xv = (i & 1) ? bfhi(xw) : bf2f(xw & 0xffffu);
        uu[c][i] = rbf(rbf(xv - m) * rs_);
      }
    }
#pragma unroll
    for (int c = 0; c < 2; ++c) {
      *(u32x4*)(lds + A_UU + c * A_UUC + ((tid >> 2) + 31) * 80 + (tid & 3) * 16) =
          (u32x4){pk_f16(uu[c][0], uu[c][1]), pk_f16(uu[c][2], uu[c][3]), pk_f16(uu[c][4], uu[c][5]), pk_f16(uu[c][6], uu[c][7])};
      *(u32x4*)(lds + A_XX + c * 5120 + (tid >> 2) * 80 + (tid & 3) * 16) = (u32x4){xg[2 * c][0], xg[2 * c][2], xg[2 * c + 1][0], xg[2 * c + 1][2]};
    }
  }
  STAMP(0, 5);
  asm volatile("s_waitcnt lgkmcnt(0)" ::: "memory");
  bar();
  if (b == 0 && tid == 0) __hip_atomic_store(epoch_p, tag, RLX_AGENT);
  const int m = 4 * (xcd >> 1) + j / 7, nb = 7 * (xcd & 1) + j % 7;
  const int mm = 4 * xcd + j / 7, nn = j % 7;
  const int lw = wv & 3;
  const u32 ldsb = lds_addr(lds);
  const rsrc_t rg = mkrsrc(wg + 128 * nb), ru = mkrsrc(wu + 128 * nb), rh = mkrsrc((const u16*)(ws + WS_HT) + 128 * m);
  const int lk = lw & 1;
  const int wrow = 16 * lk + (lane >> 3), wcp = lane & 7, wkey = ((wrow >> 1) & 1) << 2;
  const u32 vg = (wrow * FF + (wcp ^ wkey) * 8) * 2, vu = (wrow * FF + (wcp ^ wkey ^ 2) * 8) * 2;
  auto issueW = [&](int t) {
    const int p_ = t >= 14, s_ = p_ ? 27 - t : t;
    const u32 d = ldsb + P_W + ((t + 2) % 7) * SLOT + lk * 2048, so = s_ * 32 * FF * 2 + (p_ ^ (m & 1)) * 128;
    bdma16(rg, vg, so, d); bdma16(rg, vg, so + 8 * FF * 2, d + 1024);
    bdma16(ru, vu, so, d + 4096); bdma16(ru, vu, so + 8 * FF * 2, d + 4096 + 1024);
  };
  const int hrow = 16 * lk + (lane >> 4), hcp = lane & 15;
  const u32 vh = (hrow * HS + (hcp ^ ((hrow & 3) << 2)) * 8) * 2;
  auto issueH = [&](int s_) {
    const u32 d = ldsb + (s_ % 12) * SLOT + lk * 4096, so = s_ * 32 * HS * 2;
#pragma unroll
    for (int k = 0; k < 4; ++k) bdma16_dev(rh, vh, so + 4 * k * HS * 2, d + 1024 * k);
  };
  u32x4 sink = {0, 0, 0, 0};
  if (wv >= 4) {
    if (wv < 6) { issueW(0); issueW(1); issueW(2); }
    const u32 vq = ((112 * (j / 7) + 56 * lk + (lane >> 4)) * FF + (lane & 15) * 8) * 2;
#pragma unroll
    for (int i = 0; i < 14; ++i) warm16(sink, lw >= 2 ? ru : rg, vq, i * 4 * FF * 2);
  }
  const int cw = (wv >> 1) & 1, hf = wv & 1;
  const int q = lane & 31, h = lane >> 5;
  if (wv < 4) {
    const int a = q >> 3, s = q & 7;
    const char* abase = lds + A_CP + cw * A_CPC + s * A_CPS + 4080 - 16 * a + 16 * h - 32 * (32 * hf + 30) - 2048;
    const char* bbase = lds + A_UU + cw * A_UUC + (q + 31 - (16 * hf + 15)) * 80 + 16 * h;
    f32x16 y0 = {}, y1 = {};
    f16x8 fa[2][8];
    auto ld = [&](int i, f16x8 (&f)[8]) {
#pragma unroll
      for (int kk = 0; kk < 2; ++kk) {
        const int erel = 30 - (2 * i - kk);
        f[4 * kk] = *(const f16x8*)(abase + 2048 + 32 * erel);
        f[4 * kk + 1] = *(const f16x8*)(bbase + (15 - i) * 80 + kk * 32);
        f[4 * kk + 2] = *(const f16x8*)(bbase + (15 - i + 32) * 80 + kk * 32);
        f[4 * kk + 3] = *(const f16x8*)(abase + 32 * erel);
      }
    };
    ld(0, fa[0]);
#pragma unroll
    for (int i = 0; i < 16; ++i) {
      const f16x8 (&f)[8] = fa[i & 1];
      f16x8 (&g)[8] = fa[(i + 1) & 1];
#pragma unroll
      for (int kk = 0; kk < 2; ++kk) {
        const int erel = 30 - (2 * (i + 1) - kk);
        y0 = __builtin_amdgcn_mfma_f32_32x32x16_f16(f[4 * kk], f[4 * kk + 1], y0, 0, 0, 0);
        FENCE();
        y1 = __builtin_amdgcn_mfma_f32_32x32x16_f16(f[4 * kk], f[4 * kk + 2], y1, 0, 0, 0);
        if (i + 1 < 16) { g[4 * kk] = *(const f16x8*)(abase + 2048 + 32 * erel); g[4 * kk + 2] = *(const f16x8*)(bbase + (15 - i - 1 + 32) * 80 + kk * 32); }
        FENCE();
        y1 = __builtin_amdgcn_mfma_f32_32x32x16_f16(f[4 * kk + 3], f[4 * kk + 1], y1, 0, 0, 0);
        if (i + 1 < 16) { g[4 * kk + 1] = *(const f16x8*)(bbase + (15 - i - 1) * 80 + kk * 32); g[4 * kk + 3] = *(const f16x8*)(abase + 32 * erel); }
        FENCE();
      }
    }
    char* pp = lds + A_EX + wv * A_EXW + q * 144 + h * 16;
#pragma unroll
    for (int g = 0; g < 4; ++g) {
      *(f32x4*)(pp + g * 32) = (f32x4){y0[4 * g], y0[4 * g + 1], y0[4 * g + 2], y0[4 * g + 3]};
      *(f32x4*)(pp + 32 * 144 + g * 32) = (f32x4){y1[4 * g], y1[4 * g + 1], y1[4 * g + 2], y1[4 * g + 3]};
    }
  }
  STAMP(0, 6);
  asm volatile("s_waitcnt lgkmcnt(0)" ::: "memory");
  bar();
  {
    const int c = tid >> 8;
    const char* pp = lds + A_EX + (2 * c) * A_EXW + (t8 >> 2) * 144 + (t8 & 3) * 32;
    const f32x4 a0 = *(const f32x4*)pp, a1 = *(const f32x4*)(pp + 16), b0 = *(const f32x4*)(pp + A_EXW), b1 = *(const f32x4*)(pp + A_EXW + 16);
    const f16x8 u8 = *(const f16x8*)(lds + A_UU + c * A_UUC + ((t8 >> 2) + 31) * 80 + (t8 & 3) * 16);
    const u32x4 x8 = *(const u32x4*)(lds + A_XX + c * 5120 + (t8 >> 2) * 80 + (t8 & 3) * 16);
    const float ki = c ? kinv1 : kinv0, dk = c ? bfhi(p_d01) : bf2f(p_d01 & 0xffffu);
    u32x4 o;
#pragma unroll
    for (int i = 0; i < 4; ++i) {
      const float y0 = (i < 2 ? a0[2 * i] + b0[2 * i] : a1[2 * i - 4] + b1[2 * i - 4]) * ki;
      const float y1 = (i < 2 ? a0[2 * i + 1] + b0[2 * i + 1] : a1[2 * i - 3] + b1[2 * i - 3]) * ki;
      const u32 yb = pk_bf16(y0, y1), ub = pk_bf16((float)u8[2 * i] * dk, (float)u8[2 * i + 1] * dk);
      const u32 sb = pk_bf16(bf2f(yb & 0xffffu) + bf2f(ub & 0xffffu), bfhi(yb) + bfhi(ub));
      const u32 xw = x8[i];
      o[i] = pk_bf16(bf2f(xw & 0xffffu) + bf2f(sb & 0xffffu), bfhi(xw) + bfhi(sb));
    }
    __builtin_amdgcn_raw_buffer_store_b128(o, mkrsrc(ws + WS_HT), ((c0 + c) * HS + 8 * t8) * 2, 0, 16);
  }
  wait_vm<0>();
  asm volatile("" : "+v"(sink));
  bar();
  if (tid == 0) __hip_atomic_store((gu32*)(ws + WS_FLA) + w * 32, tag, RLX_AGENT);
  STAMP(0, 7);

  const rsrc_t rHc = mkrsrc(ws + WS_H + (size_t)(2 * mm + lk) * 112 * 1024);
  const rsrc_t rw = mkrsrc(wd + 64 * nn);
  const int crow = 32 * lk + (lane >> 3), ccp = lane & 7;
  const u32 cvh = lane * 16, cvw = (crow * DM + (ccp ^ (((crow >> 1) & 1) << 2)) * 8) * 2;
  auto cW = [&](int u) {
    const int p_ = u >= 14, n_ = p_ ? u - 14 : u;
    const u32 d = ldsb + (11 - u % 7) * SLOT + lk * 4096, so = (128 * n_ + 64 * (p_ ^ ((mm >> 1) & 1))) * DM * 2;
#pragma unroll
    for (int k = 0; k < 4; ++k) bdma16(rw, cvw, so + 8 * k * DM * 2, d + 1024 * k);
  };
  auto cH = [&](int u) {
    const int p_ = u >= 14, n_ = p_ ? u - 14 : u, v = u % 12;
    const u32 d = ldsb + (v < 7 ? 12 + v : v - 7) * SLOT + lk * 4096, so = (8 * n_ + 4 * (p_ ^ ((mm >> 1) & 1))) * 1024;
#pragma unroll
    for (int k = 0; k < 4; ++k) bdma16_dev(rHc, cvh, so + 1024 * k, d + 1024 * k);
  };

  if (wv >= 4) {
    if (lw < 2) {
      issueW(3); issueW(4);
      {
        const u32 vq = ((448 * (j / 7) + 224 * lk + (lane >> 3)) * DM + (lane & 7) * 8) * 2;
#pragma unroll
        for (int i = 0; i < 28; ++i) warm16(sink, rw, vq, i * 8 * DM * 2);
      }
#define W_IT(T, N) { wait_vm<N>(); bar(); if ((T) + 5 <= 27) issueW((T) + 5); }
      W_IT(0, 16) W_IT(1, 16) W_IT(2, 16) W_IT(3, 16) W_IT(4, 16) W_IT(5, 16) W_IT(6, 16) W_IT(7, 16) W_IT(8, 16) W_IT(9, 16) W_IT(10, 16)
      W_IT(11, 16) W_IT(12, 16) W_IT(13, 16)
      asm volatile("" : "+v"(sink));
      W_IT(14, 16) W_IT(15, 16) W_IT(16, 16) W_IT(17, 16) W_IT(18, 16) W_IT(19, 16) W_IT(20, 16) W_IT(21, 16) W_IT(22, 16) W_IT(23, 16)
      W_IT(24, 12) W_IT(25, 8) W_IT(26, 4) W_IT(27, 0)
#undef W_IT
      bar();
#define D_IT(U, N) { wait_vm<N>(); bar(); if ((U) >= 2 && (U) <= 22) cW((U) + 5); }
      D_IT(0, 0) D_IT(1, 0) D_IT(2, 0) D_IT(3, 63) D_IT(4, 63) D_IT(5, 63) D_IT(6, 63) D_IT(7, 16) D_IT(8, 16) D_IT(9, 16) D_IT(10, 16) D_IT(11, 16)
      D_IT(12, 16) D_IT(13, 16) D_IT(14, 16) D_IT(15, 16) D_IT(16, 16) D_IT(17, 16) D_IT(18, 16) D_IT(19, 16) D_IT(20, 16) D_IT(21, 16) D_IT(22, 16)
      D_IT(23, 16) D_IT(24, 12) D_IT(25, 8) D_IT(26, 4) D_IT(27, 0)
#undef D_IT
    } else {
      {
        const rsrc_t rc = mkrsrc(ws + WS_CS + (lk * FF + 128 * nb) * 8);
        u32x4 cg;
        for (int spin = 0; spin < (1 << 16); ++spin) {
          cg = __builtin_amdgcn_raw_buffer_load_b128(rc, lane * 16, 0, 16);
          if (__all(cg[1] == tag && cg[3] == tag)) break;
          __builtin_amdgcn_s_sleep(2);
        }
        *(u32x2*)(lds + B_CS + lk * 512 + lane * 8) = (u32x2){cg[0], cg[2]};
      }
      {
        gu32* f0 = (gu32*)(ws + WS_FLA) + (lk * 112 + (lane < 56 ? lane : 0)) * 32;
        for (int spin = 0; spin < (1 << 16); ++spin) {
          const u32 a_ = __hip_atomic_load(f0, RLX_AGENT), b_ = __hip_atomic_load(f0 + 56 * 32, RLX_AGENT);
          if (__all(a_ == tag && b_ == tag)) break;
          __builtin_amdgcn_s_sleep(2);
        }
        LAS u32* cnt = (LAS u32*)(lds + B_CNT);
        if (lane == 0) __hip_atomic_fetch_add(cnt, 1u, __ATOMIC_RELAXED, __HIP_MEMORY_SCOPE_WORKGROUP);
        for (int spin = 0; spin < (1 << 20); ++spin) {
          if (__hip_atomic_load(cnt, __ATOMIC_RELAXED, __HIP_MEMORY_SCOPE_WORKGROUP) >= 2u) break;
          __builtin_amdgcn_s_sleep(1);
        }
      }
      STAMP(0, 8);
      issueH(0); issueH(1); issueH(2);
      wait_vm<8>(); bar(); issueH(3); issueH(4); issueH(5);
      wait_vm<16>(); bar(); issueH(6); issueH(7); issueH(8);
      wait_vm<24>(); bar(); issueH(9); issueH(10); issueH(11);
      wait_vm<32>(); bar(); issueH(12);
      wait_vm<32>(); bar(); issueH(13);
      wait_vm<32>(); bar();
      wait_vm<28>(); bar(); wait_vm<24>(); bar(); wait_vm<20>(); bar(); wait_vm<16>(); bar();
      wait_vm<12>(); bar(); wait_vm<8>(); bar(); wait_vm<4>(); bar(); wait_vm<0>(); bar();
      bar(); bar();
      bar(); issueH(1);
      bar(); issueH(0);
      bar(); cW(0); bar(); cW(1); bar(); cW(2); bar(); cW(3); bar(); cW(4); bar(); cW(5); bar(); cW(6);
      bar();
      wait_vm<32>(); bar();
      wait_vm<28>(); bar();
      STAMP(0, 9);
      bar();
      {
        gu32* fl = (gu32*)(ws + WS_FLB) + (((mm >> 1) * 14 + (lane < 14 ? lane : 0)) * 2) * 32;
        for (int spin = 0; spin < (1 << 16); ++spin) {
          if (__all(__hip_atomic_load(fl, RLX_AGENT) == tag)) break;
          __builtin_amdgcn_s_sleep(2);
        }
      }
      STAMP(0, 11);
      cH(0); cH(1); cH(2);
      wait_vm<8>(); bar(); cH(3); cH(4); cH(5);
      wait_vm<16>(); bar(); cH(6); cH(7); cH(8);
      wait_vm<24>(); bar(); cH(9); cH(10); cH(11);
      wait_vm<32>(); bar(); cH(12); cH(13);
#define E_IT(U, N) { wait_vm<N>(); bar(); if ((U) <= 17) cH((U) + 10); }
      E_IT(4, 36) E_IT(5, 36) E_IT(6, 36) E_IT(7, 36) E_IT(8, 36) E_IT(9, 36) E_IT(10, 36)
      E_IT(11, 36) E_IT(12, 36) E_IT(13, 36) E_IT(14, 36) E_IT(15, 36) E_IT(16, 36) E_IT(17, 36) E_IT(18, 36) E_IT(19, 32) E_IT(20, 28)
      E_IT(21, 24) E_IT(22, 20) E_IT(23, 16) E_IT(24, 12) E_IT(25, 8) E_IT(26, 4) E_IT(27, 0)
#undef E_IT
    }
    STAMP(0, 13);
    STAMP_FLUSH(0);
    asm volatile(".Lfused_end2:");
    return;
  }

  const int tp = wv & 1, ag = wv >> 1;
  const int grp = lane >> 4, fq = (lane & 15) >> 2, fp = lane & 3, fh = grp >> 1;
  int ao[2], bo[2];
  {
    const int akey = (((fq >> 1) & 1) << 2) ^ ((grp & 1) << 1);
#pragma unroll
    for (int i = 0; i < 2; ++i) ao[i] = P_W + (grp & 1) * 4096 + (8 * fh + fq) * 128 + (((2 * (2 * ag + i) + (fp >> 1)) ^ akey) * 16) + (fp & 1) * 8;
#pragma unroll
    for (int t = 0; t < 2; ++t)
      bo[t] = (8 * fh + fq) * 256 + (((4 * (2 * tp + t) + 2 * (grp & 1) + (fp >> 1)) ^ (fq << 2)) * 16) + (fp & 1) * 8;
  }
  f32x16 accs[2][2][2];
  struct KF { s16x8 a[2][2], b[2][2]; };
  KF X, Y;
  const rsrc_t rH = mkrsrc(ws + WS_H);
  const int hh = lane >> 5;
  f32x2 st2[2];
  auto epilogue = [&](auto pass_) {
    constexpr int p_ = decltype(pass_)::value;
    auto& acc = accs[p_];
    const float* cs = (const float*)(lds + B_CS);
#pragma unroll
    for (int i = 0; i < 2; ++i) {
      const int tile = 4 * (p_ ^ (m & 1)) + 2 * ag + i, f0 = 16 * tile + 4 * hh;
      const f32x4 g0 = *(const f32x4*)(cs + f0), g1 = *(const f32x4*)(cs + f0 + 8);
      const f32x4 u0 = *(const f32x4*)(cs + 128 + f0), u1 = *(const f32x4*)(cs + 128 + f0 + 8);
#pragma unroll
      for (int t = 0; t < 2; ++t) {
        const f32x2 nm = {st2[t][0], st2[t][0]}, r2 = {st2[t][1], st2[t][1]};
        u32x4 o;
#pragma unroll
        for (int r = 0; r < 4; ++r) {
          const f32x4 cg = r < 2 ? g0 : g1, cu = r < 2 ? u0 : u1;
          const int e = 2 * (r & 1);
          const f32x2 g = __builtin_elementwise_fma(nm, (f32x2){cg[e], cg[e + 1]}, (f32x2){acc[i][t][2 * r], acc[i][t][2 * r + 1]});
          const f32x2 u = __builtin_elementwise_fma(nm, (f32x2){cu[e], cu[e + 1]}, (f32x2){acc[i][t][8 + 2 * r], acc[i][t][8 + 2 * r + 1]});
          const f32x2 hv = (g * r2) * u;
          o[r] = pk_bf16(hv[0], hv[1]);
        }
        __builtin_amdgcn_raw_buffer_store_b128(o, rH, (((4 * m + 2 * tp + t) * 112 + 8 * nb + tile) * 64 + lane) * 16, 0, 16);
      }
    }
  };
  auto publish = [&](int p_) {
    asm volatile("s_waitcnt vmcnt(0)" ::: "memory");
    u32 old = 0;
    if (lane == 0) old = __hip_atomic_fetch_add((LAS u32*)(lds + CNT0) + p_, 1u, __ATOMIC_RELAXED, __HIP_MEMORY_SCOPE_WORKGROUP);
    if (lane == 0 && old == 3u) __hip_atomic_store((gu32*)(ws + WS_FLB) + ((m * 14 + nb) * 2 + p_) * 32, tag, RLX_AGENT);
  };
  auto mm8 = [&](auto pass_, const KF& f) {
    auto& acc = accs[decltype(pass_)::value];
#pragma unroll
    for (int g = 0; g < 8; ++g) {
      const int ks = g >> 2, i = (g >> 1) & 1, t = g & 1;
      acc[i][t] = __builtin_amdgcn_mfma_f32_32x32x16_bf16(__builtin_bit_cast(bf16x8, f.a[ks][i]), __builtin_bit_cast(bf16x8, f.b[ks][t]), acc[i][t], 0, 0, 0);
    }
  };
  int hsl = 0, wsl = 2;
  typedef LAS const char* lptr;
  auto trf = [&](u32 a0, int off) {
    s16x4 x0 = __builtin_amdgcn_ds_read_tr16_b64_v4i16((LAS s16x4*)(a0 + off));
    return x0;
  };
  auto stage = [&](auto pass_, auto first, KF& cur, const KF& prev) {
    auto& acc = accs[decltype(pass_)::value];
    u32 hb0 = ldsb + hsl * SLOT + bo[0], hb1 = ldsb + hsl * SLOT + bo[1], wa0 = ldsb + wsl * SLOT + ao[0], wa1 = ldsb + wsl * SLOT + ao[1];
    asm volatile("" : "+v"(hb0), "+v"(hb1), "+v"(wa0), "+v"(wa1));
    auto rdb = [&](int rk, int rt_) {
      const u32 a0 = rt_ ? hb1 : hb0;
      cur.b[rk][rt_] = __builtin_shufflevector(trf(a0, rk * 16 * 256), trf(a0, (rk * 16 + 4) * 256), 0, 1, 2, 3, 4, 5, 6, 7);
    };
    auto rda = [&](int rk, int rt_) {
      const u32 a0 = rt_ ? wa1 : wa0;
      cur.a[rk][rt_] = __builtin_shufflevector(trf(a0, rk * 16 * 128), trf(a0, (rk * 16 + 4) * 128), 0, 1, 2, 3, 4, 5, 6, 7);
    };
#pragma unroll
    for (int g = 0; g < 8; ++g) {
      const int ks = g >> 2, i = (g >> 1) & 1, t = g & 1;
      if (!decltype(first)::value)
        acc[i][t] = __builtin_amdgcn_mfma_f32_32x32x16_bf16(__builtin_bit_cast(bf16x8, prev.a[ks][i]), __builtin_bit_cast(bf16x8, prev.b[ks][t]), acc[i][t], 0, 0, 0);
      if (g == 0) { FENCE(); bar(); }
      if (g == 1) { rdb(0, 0); rda(0, 0); }
      if (g == 2) { rdb(0, 1); }
      if (g == 3) { rda(0, 1); }
      if (g == 4) { rdb(1, 0); }
      if (g == 5) { rda(1, 0); }
      if (g == 6) { rdb(1, 1); }
      if (g == 7) { rda(1, 1); }
      FENCE();
    }
    wsl = wsl == 6 ? 0 : wsl + 1;
  };
#pragma unroll
  for (int g = 0; g < 8; ++g) accs[g >> 2][(g >> 1) & 1][g & 1] = (f32x16){};
  stage(IC<0>{}, IC<1>{}, X, Y); hsl = 1;
  STAMP(0, 8);
  stage(IC<0>{}, IC<0>{}, Y, X); hsl = 2;
#pragma unroll 1
  for (int t = 2; t < 14; t += 2) {
    stage(IC<0>{}, IC<0>{}, X, Y); hsl = hsl == 11 ? 0 : hsl + 1;
    stage(IC<0>{}, IC<0>{}, Y, X); hsl = hsl == 11 ? 0 : hsl + 1;
  }
  STAMP(0, 15);
  const rsrc_t rg2 = mkrsrc(ws + WS_GRAN2 + (size_t)(128 * m + 64 * tp) * 8);
  u32x2 sg[2];
#pragma unroll
  for (int t = 0; t < 2; ++t) sg[t] = __builtin_amdgcn_raw_buffer_load_b64(rg2, (32 * t + (lane & 31)) * 8, 0, 16);
  mm8(IC<0>{}, Y);
  hsl = 1;
  stage(IC<1>{}, IC<1>{}, X, Y); hsl = 0;
  stage(IC<1>{}, IC<0>{}, Y, X); hsl = 11;
  stage(IC<1>{}, IC<0>{}, X, Y); hsl = 10;
  for (int spin = 0; spin < (1 << 16); ++spin) {
    if (__all(sg[0][1] == tag && sg[1][1] == tag)) break;
    __builtin_amdgcn_s_sleep(2);
#pragma unroll
    for (int t = 0; t < 2; ++t) sg[t] = __builtin_amdgcn_raw_buffer_load_b64(rg2, (32 * t + (lane & 31)) * 8, 0, 16);
  }
#pragma unroll
  for (int t = 0; t < 2; ++t) { const float rs = __builtin_amdgcn_rcpf(bfhi(sg[t][0])); st2[t] = (f32x2){-bf2f(sg[t][0] & 0xffffu), rs * rs}; }
  STAMP(0, 11);
  epilogue(IC<0>{});
  STAMP(0, 10);
  stage(IC<1>{}, IC<0>{}, Y, X); hsl = 9;
  stage(IC<1>{}, IC<0>{}, X, Y); hsl = 8;
  stage(IC<1>{}, IC<0>{}, Y, X); hsl = 7;
  publish(0);
#pragma unroll 1
  for (int t = 20; t < 28; t += 2) {
    stage(IC<1>{}, IC<0>{}, X, Y); hsl = hsl - 1;
    stage(IC<1>{}, IC<0>{}, Y, X); hsl = hsl - 1;
  }
  mm8(IC<1>{}, Y);
  STAMP(0, 9);
  bar();
  epilogue(IC<1>{});
  publish(1);
  const int dtl = wv & 1, tt = wv >> 1;
  const int tok = 64 * mm + 32 * tt + (lane & 31);
  const rsrc_t rres = mkrsrc(ws + WS_HT);
  u32 res[16];
#pragma unroll
  for (int i = 0; i < 16; ++i) res[i] = __builtin_amdgcn_raw_buffer_load_b16(rres, ((64 * nn + 32 * dtl + 8 * (i >> 2) + 4 * hh + (i & 3)) * HS + tok) * 2, 0, 16);
  const int achunk = 4 * dtl + 2 * (grp & 1) + (fp >> 1);
  const int caoff = (4 * fh + fq) * 128 + ((achunk ^ (((fq >> 1) & 1) << 2)) * 16) + (fp & 1) * 8;
  const int cboff = tt * 4096 + lane * 16;
  f32x16 cacc = {};
  struct F4 { s16x8 a[4]; bf16x8 b[4]; };
  F4 Xc, Yc;
  int cwb = 11, chv = 0;
  auto cstage = [&](auto first, F4& cur, const F4& prev) {
    bar();
    const char* sth = lds + (chv < 7 ? 12 + chv : chv - 7) * SLOT;
    const char* stw = lds + cwb * SLOT;
#pragma unroll
    for (int k = 0; k < 4; ++k) {
      if (!decltype(first)::value) cacc = __builtin_amdgcn_mfma_f32_32x32x16_bf16(__builtin_bit_cast(bf16x8, prev.a[k]), prev.b[k], cacc, 0, 0, 0);
      cur.a[k] = tr_frag(stw + caoff + k * 16 * 128, stw + caoff + (k * 16 + 8) * 128);
      cur.b[k] = *(const bf16x8*)(sth + cboff + k * 1024);
      FENCE();
    }
    chv = chv == 11 ? 0 : chv + 1; cwb = cwb == 5 ? 11 : cwb - 1;
  };
  cstage(IC<1>{}, Xc, Yc);
  STAMP(0, 12);
  cstage(IC<0>{}, Yc, Xc);
  cstage(IC<0>{}, Xc, Yc);
  cstage(IC<0>{}, Yc, Xc);
  if (wv == 0) {
    gu32* fl = (gu32*)(ws + WS_FLB) + (((mm >> 1) * 14 + (lane < 14 ? lane : 0)) * 2 + 1) * 32;
    for (int spin = 0; spin < (1 << 16); ++spin) {
      if (__all(__hip_atomic_load(fl, RLX_AGENT) == tag)) break;
      __builtin_amdgcn_s_sleep(2);
    }
  }
#pragma unroll 1
  for (int u = 4; u < 28; u += 2) { cstage(IC<0>{}, Xc, Yc); cstage(IC<0>{}, Yc, Xc); }
#pragma unroll
  for (int k = 0; k < 4; ++k) cacc = __builtin_amdgcn_mfma_f32_32x32x16_bf16(__builtin_bit_cast(bf16x8, Yc.a[k]), Yc.b[k], cacc, 0, 0, 0);
  STAMP(0, 13);
  u16* op = out + tok * DM + 64 * nn + 32 * dtl + 4 * hh;
#pragma unroll
  for (int g = 0; g < 4; ++g)
    *(u32x2*)(op + 8 * g) = (u32x2){pk_bf16(bf2f(res[4 * g]) + rbf(cacc[4 * g]), bf2f(res[4 * g + 1]) + rbf(cacc[4 * g + 1])),
                                    pk_bf16(bf2f(res[4 * g + 2]) + rbf(cacc[4 * g + 2]), bf2f(res[4 * g + 3]) + rbf(cacc[4 * g + 3]))};
  STAMP(0, 14);
  STAMP_FLUSH(0);
  asm volatile(".Lfused_end:");
}

extern "C" void kernel_launch(void* const* d_in, const int* in_sizes, int n_in, void* d_out, int out_size, void* d_ws, size_t ws_size,
                              hipStream_t stream) {
  (void)in_sizes; (void)n_in; (void)out_size; (void)ws_size;
  (void)hipFuncSetAttribute((const void*)fused_kernel, hipFuncAttributeMaxDynamicSharedMemorySize, K_LDS);
  fused_kernel<<<256, 512, K_LDS, stream>>>((const u16*)d_in[0], (const u16*)d_in[1], (const u16*)d_in[2], (const u16*)d_in[3], (const u16*)d_in[4],
                                            (const u16*)d_in[5], (const u16*)d_in[6], (const u16*)d_in[7], (const u16*)d_in[8],
                                            (const u16*)d_in[10], (const u16*)d_in[11], (const u16*)d_in[12], (char*)d_ws, (u16*)d_out);
}
```

```cpp
#include <hip/hip_runtime.h>
#include <stdint.h>

typedef unsigned short u16;
typedef unsigned int u32;
typedef unsigned long long u64;
typedef float f32x2 __attribute__((ext_vector_type(2)));
typedef float f32x4 __attribute__((ext_vector_type(4)));
typedef float f32x16 __attribute__((ext_vector_type(16)));
typedef unsigned u32x2 __attribute__((ext_vector_type(2)));
typedef unsigned u32x4 __attribute__((ext_vector_type(4)));
typedef short s16x4 __attribute__((ext_vector_type(4)));
typedef short s16x8 __attribute__((ext_vector_type(8)));
typedef __bf16 bf16x8 __attribute__((ext_vector_type(8)));
typedef __bf16 bf16x2 __attribute__((ext_vector_type(2)));
typedef _Float16 f16x8 __attribute__((ext_vector_type(8)));
typedef _Float16 f16x4 __attribute__((ext_vector_type(4)));
typedef _Float16 f16x2 __attribute__((ext_vector_type(2)));
#define DI __device__ __forceinline__
#define LAS __attribute__((address_space(3)))
#define GAS __attribute__((address_space(1)))
typedef GAS unsigned gu32;
typedef GAS unsigned long long gu64;
#define RLX_AGENT __ATOMIC_RELAXED, __HIP_MEMORY_SCOPE_AGENT
#define FENCE() __builtin_amdgcn_sched_barrier(0)

constexpr int SL = 2048, DM = 448, NS = 256, FF = 1792;
constexpr int HS = SL + 64;

constexpr size_t WS_EPOCH = 0;
constexpr size_t WS_GRAN  = 4096;
constexpr size_t WS_CS    = 32768;
constexpr size_t WS_FLA   = 65536;
constexpr size_t WS_FLB   = WS_FLA + 32768;
constexpr size_t WS_GRAN2 = 163840;
constexpr size_t WS_XG    = 196608;
constexpr size_t WS_HT    = WS_XG + (size_t)DM * 8192;
constexpr size_t WS_H     = WS_HT + (size_t)DM * HS * 2;
constexpr size_t WS_DBG   = WS_H + (size_t)SL * FF * 2;

#define STAMP_DECL do {} while (0)
#define STAMP(k, i) do {} while (0)
#define STAMP_FLUSH(k) do {} while (0)

DI float bf2f(u32 v) { return __uint_as_float(v << 16); }
DI float bfhi(u32 v) { return __uint_as_float(v & 0xffff0000u); }
DI u32 pk_bf16(float a, float b) { f32x2 v = {a, b}; return __builtin_bit_cast(u32, __builtin_convertvector(v, bf16x2)); }
DI u32 pk_f16(float a, float b) { f32x2 v = {a, b}; return __builtin_bit_cast(u32, __builtin_convertvector(v, f16x2)); }
DI u32 pk2(f32x2 v) { return __builtin_bit_cast(u32, __builtin_convertvector(v, f16x2)); }
DI float rbf(float a) { return __uint_as_float(pk_bf16(a, a) << 16); }

template <int CTRL, int RM> DI float dpp0(float v) { return __int_as_float(__builtin_amdgcn_update_dpp(0, __float_as_int(v), CTRL, RM, 0xf, true)); }
DI float lane63(float v) { return __int_as_float(__builtin_amdgcn_readlane(__float_as_int(v), 63)); }
DI void wave_sum2(float& a, float& b) {
  a += dpp0<0x111, 0xf>(a); b += dpp0<0x111, 0xf>(b); a += dpp0<0x112, 0xf>(a); b += dpp0<0x112, 0xf>(b);
  a += dpp0<0x114, 0xf>(a); b += dpp0<0x114, 0xf>(b); a += dpp0<0x118, 0xf>(a); b += dpp0<0x118, 0xf>(b);
  a += dpp0<0x142, 0xa>(a); b += dpp0<0x142, 0xa>(b); a += dpp0<0x143, 0xc>(a); b += dpp0<0x143, 0xc>(b);
  a = lane63(a); b = lane63(b);
}
DI void wave_max_sum(float& m, float& s) {
  m = fmaxf(m, dpp0<0x111, 0xf>(m)); s += dpp0<0x111, 0xf>(s); m = fmaxf(m, dpp0<0x112, 0xf>(m)); s += dpp0<0x112, 0xf>(s);
  m = fmaxf(m, dpp0<0x114, 0xf>(m)); s += dpp0<0x114, 0xf>(s); m = fmaxf(m, dpp0<0x118, 0xf>(m)); s += dpp0<0x118, 0xf>(s);
  m = fmaxf(m, dpp0<0x142, 0xa>(m)); s += dpp0<0x142, 0xa>(s); m = fmaxf(m, dpp0<0x143, 0xc>(m)); s += dpp0<0x143, 0xc>(s);
  m = lane63(m); s = lane63(s);
}

DI void glds16(const void* g, u32 lds_dst) {
  u32 keep;
  asm volatile("s_mov_b32 %0, m0\n\ts_mov_b32 m0, %2\n\ts_nop 0\n\tglobal_load_lds_dwordx4 %1, off\n\ts_mov_b32 m0, %0"
               : "=&s"(keep) : "v"(g), "s"(lds_dst) : "memory");
}
typedef __amdgpu_buffer_rsrc_t rsrc_t;
DI rsrc_t mkrsrc(const void* p) { return __builtin_amdgcn_make_buffer_rsrc((void*)p, 0, 0x7fffffff, 0x00020000); }
DI void bdma16(rsrc_t rs, u32 voff, u32 soff, u32 lds_dst) {
  asm volatile("s_mov_b32 m0, %0\n\ts_nop 0\n\tbuffer_load_dwordx4 %1, %2, %3 offen lds" :: "s"(lds_dst), "v"(voff), "s"(rs), "s"(soff) : "memory");
}
DI void bdma16_dev(rsrc_t rs, u32 voff, u32 soff, u32 lds_dst) {
  asm volatile("s_mov_b32 m0, %0\n\ts_nop 0\n\tbuffer_load_dwordx4 %1, %2, %3 offen sc1 lds" :: "s"(lds_dst), "v"(voff), "s"(rs), "s"(soff) : "memory");
}
DI void bdma4(rsrc_t rs, u32 voff, u32 soff, u32 lds_dst) {
  asm volatile("s_mov_b32 m0, %0\n\ts_nop 0\n\tbuffer_load_dword %1, %2, %3 offen lds" :: "s"(lds_dst), "v"(voff), "s"(rs), "s"(soff) : "memory");
}
DI u32 lds_addr(const void* p) { return (u32)(size_t)(LAS const char*)p; }
DI s16x8 tr_frag(const char* p0, const char* p1) {
  s16x4 a = __builtin_amdgcn_ds_read_tr16_b64_v4i16((LAS s16x4*)p0);
  s16x4 b = __builtin_amdgcn_ds_read_tr16_b64_v4i16((LAS s16x4*)p1);
  return __builtin_shufflevector(a, b, 0, 1, 2, 3, 4, 5, 6, 7);
}

struct IdxTab { u16 v[2064]; };
constexpr int tap_entry(int l) {
  if (l < 0) return 21 * 32;
  int lam = l;
  if (l >= 256) {
    int sh = l >= 1024 ? 3 : (l >= 512 ? 2 : 1);
    int q = l >> sh, rem = l & ((1 << sh) - 1), half = 1 << (sh - 1);
    if (rem > half || (rem == half && (q & 1))) q += 1;
    lam = q << sh;
  }
  int hq = lam >> 8, lo = lam & 255, c = lo >> 3, r = 0;
  if (hq == 0) r = lo & 7;
  else if (hq == 1) r = 8 + ((lo & 7) >> 1);
  else if (hq == 2) r = 12 + ((lo & 7) >> 2);
  else if (hq == 3) r = 14 + ((lo & 7) >> 2);
  else if (hq < 8) r = 16 + hq - 4;
  else { r = 20; c = 0; }
  return r * 32 + c;
}
constexpr IdxTab make_tab() { IdxTab t{}; for (int y = 0; y < 2064; ++y) t.v[y] = (u16)(2 * tap_entry(2047 - y)); return t; }
__device__ const IdxTab RIDX __attribute__((aligned(16))) = make_tab();

DI float pow2_for(float mx, int target) {
  int e = (int)(__float_as_uint(mx) >> 23);
  int be = 127 + target - (e - 126);
  be = be > 254 ? 254 : (be < 1 ? 1 : be);
  return __uint_as_float((u32)be << 23);
}
DI float pow2_inv(float p) { return __uint_as_float((254u - (__float_as_uint(p) >> 23)) << 23); }

struct CM { f32x2 a, b; };
DI CM mk(f32x2 e) { return {e, (f32x2){-e.y, e.x}}; }
DI f32x2 cmul(f32x2 p, CM m) {
  f32x2 t = __builtin_shufflevector(p, p, 0, 0) * m.a;
  return __builtin_elementwise_fma(__builtin_shufflevector(p, p, 1, 1), m.b, t);
}
DI f32x2 cexp_s(float zr, float zi, float s) {
  float m = __builtin_amdgcn_exp2f(zr * (s * 1.44269504088896341f));
  float a = __builtin_amdgcn_fractf(zi * (s * 0.15915494309189535f));
  return (f32x2){m * __builtin_amdgcn_cosf(a), m * __builtin_amdgcn_sinf(a)};
}
template <int OFF>
DI void put4(u32 base, u32 v0, u32 v1, u32 v2, u32 v3) {
  asm volatile("s_mov_b32 m0, %0\n\ts_nop 0\n\tds_write_addtid_b32 %1 offset:%5\n\tds_write_addtid_b32 %2 offset:%6\n\t"
               "ds_write_addtid_b32 %3 offset:%7\n\tds_write_addtid_b32 %4 offset:%8"
               :: "s"(base), "v"(v0), "v"(v1), "v"(v2), "v"(v3), "n"(OFF), "n"(OFF + 272), "n"(OFF + 544), "n"(OFF + 816) : "memory");
}

template <int V> struct IC { static constexpr int value = V; };
template <int N> DI void wait_vm() { asm volatile("s_waitcnt vmcnt(%0)" :: "n"(N) : "memory"); }
DI void glds4(const void* g, u32 lds_dst) {
  u32 keep;
  asm volatile("s_mov_b32 %0, m0\n\ts_mov_b32 m0, %2\n\ts_nop 0\n\tglobal_load_lds_dword %1, off\n\ts_mov_b32 m0, %0"
               : "=&s"(keep) : "v"(g), "s"(lds_dst) : "memory");
}
DI void bar() { __builtin_amdgcn_s_barrier(); }

constexpr int A_T1 = 0, A_T2 = 8192;
constexpr int A_SCR = 25088, A_SCRW = 32 * 272 + 21 * 272;
constexpr int A_CP = 0, A_CPS = 4160, A_CPC = 4 * A_CPS;
constexpr int A_EX = 66560, A_EXW = 64 * 144, A_XX = A_EX + 4 * A_EXW;
constexpr int A_UU = A_SCR + 8 * A_SCRW, A_UUC = 95 * 80;
constexpr int A_TB = A_UU + 2 * A_UUC, A_TBC = 22 * 32 * 2;
constexpr int A_SW = A_TB + 2 * A_TBC;
constexpr int A_LDS = A_SW + 64;
static_assert(A_LDS <= 163840 && A_XX + 10240 <= A_UU && (A_SCR % 16) == 0 && (A_SCRW % 16) == 0 && (A_UU % 16) == 0, "lds");

constexpr int SLOT = 8192, P_W = 12 * SLOT;
constexpr int B_CS = 19 * SLOT, B_CNT = 163840 - 16, CNT0 = 163840 - 12, CNT1 = 163840 - 8;

constexpr int K_LDS = 163840;
constexpr int CODE_WARM_KB = 64;
static_assert(A_LDS <= K_LDS - 16, "lds");

__global__ __launch_bounds__(512, 2) void fused_kernel(const u16* __restrict__ x, const u16* __restrict__ Are, const u16* __restrict__ Aim,
    const u16* __restrict__ Bre, const u16* __restrict__ Bim, const u16* __restrict__ Cre, const u16* __restrict__ Cim,
    const u16* __restrict__ logdt, const u16* __restrict__ Dskip, const u16* __restrict__ wg, const u16* __restrict__ wu,
    const u16* __restrict__ wd, char* ws, u16* __restrict__ out) {
  extern __shared__ __attribute__((aligned(16))) char lds[];
  char* dbg = ws + WS_DBG; (void)dbg;
  STAMP_DECL;
  const int tid = threadIdx.x, lane = tid & 63;
  const int wv = __builtin_amdgcn_readfirstlane(tid >> 6);
  const int b = blockIdx.x, xcd = b & 7, j = b >> 3;
  const bool worker = j < 28;
  STAMP(0, 0);
  u32x4 cwarm[CODE_WARM_KB / 8];
  if (!worker) {
      u64 pc; u32 len, len2;
      asm volatile("s_getpc_b64 %0\n.Lfused_pc:\n\ts_mov_b32 %1, .Lfused_end-.Lfused_pc\n\ts_mov_b32 %2, .Lfused_end2-.Lfused_pc" : "=s"(pc), "=s"(len), "=s"(len2));
      len = (int)len2 > (int)len ? len2 : len;
      const char* code = (const char*)pc;
#pragma unroll
      for (int i = 0; i < CODE_WARM_KB / 8; ++i) {
        const u32 o = (u32)(i * 512 + tid) * 16u;
        cwarm[i] = *(const GAS u32x4*)(code + (((int)len > 16 && o + 16u <= len) ? o : 0u));
      }
  }
  asm volatile("" :: "s"(x), "s"(Are), "s"(Aim), "s"(Bre), "s"(Bim), "s"(Cre), "s"(Cim), "s"(logdt), "s"(Dskip), "s"(wg), "s"(wu), "s"(wd), "s"(ws), "s"(out));
  gu32* epoch_p = (gu32*)(ws + WS_EPOCH);
  gu64* gran = (gu64*)(ws + WS_GRAN);

  const int w = worker ? xcd * 28 + j : 0, c0 = 2 * w;
  const int trow = 8 * b + wv;
  const u32x4 xr = *(const u32x4*)(x + trow * DM + (lane < 56 ? lane : 0) * 8);
  const u32 tag = __hip_atomic_load(epoch_p, RLX_AGENT) + 1u;
  const int ch = tid >> 8, n = tid & 255;
  const int idx = (c0 + ch) * NS + n;
  const u32 p_ldt = logdt[c0 + ch];
  const u32 p_are = Are[idx], p_aim = Aim[idx];
  const u32x4 xt = *(const u32x4*)(x + (128 * (w & 15) + (tid >> 2)) * DM + 32 * (w >> 4) + (tid & 3) * 8);
  const u32 p_bre = Bre[idx], p_bim = Bim[idx], p_cre = Cre[idx], p_cim = Cim[idx];
  const u32 p_d01 = *(const u32*)(Dskip + c0);
  const u32x4 ix0 = *(const u32x4*)(RIDX.v + 8 * (tid & 255)), ix1 = *(const u32x4*)(RIDX.v + 8 * (tid & 255) + 8);
  if (tid == 0) { *(u32*)(lds + B_CNT) = 0u; *(u32*)(lds + CNT0) = 0u; *(u32*)(lds + CNT1) = 0u; }
  if (tid < 310) *(u32x4*)(lds + A_UU + (tid >= 155 ? A_UUC - 155 * 16 : 0) + tid * 16) = (u32x4){0, 0, 0, 0};
  if (tid >= 504) *(u32x4*)(lds + A_TB + ((tid >> 2) & 1) * A_TBC + 21 * 64 + (tid & 3) * 16) = (u32x4){0, 0, 0, 0};
  {
    const float piv = __uint_as_float((u32)__builtin_amdgcn_readfirstlane(xr[0]) << 16);
    float s = 0.f, q = 0.f, s2 = 0.f, q2 = 0.f;
#pragma unroll
    for (int i = 0; i < 4; ++i) {
      const float lo = bf2f(xr[i] & 0xffffu) - piv, hi = bfhi(xr[i]) - piv;
      s += lo; s2 += hi; q = fmaf(lo, lo, q); q2 = fmaf(hi, hi, q2);
    }
    s = lane < 56 ? s + s2 : 0.f; q = lane < 56 ? q + q2 : 0.f;
    wave_sum2(s, q);
    const float ms = s * (1.f / DM);
    float var = q * (1.f / DM) - ms * ms; var = var < 0.f ? 0.f : var;
    const float sd = rbf(__builtin_sqrtf(rbf(rbf(var) + 1e-5f)));
    if (lane == 0) __hip_atomic_store(gran + trow, ((u64)tag << 32) | pk_bf16(piv + ms, sd), RLX_AGENT);
  }
  STAMP(0, 1);
  if (!worker) {
    {
      u32 acc = 0;
#pragma unroll
      for (int i = 0; i < CODE_WARM_KB / 8; ++i) acc |= cwarm[i][0] & cwarm[i][1] & cwarm[i][2] & cwarm[i][3];
      asm volatile("" :: "v"(acc));
    }
    const int s = xcd * 4 + (j - 28);
    const u16* W = (s >> 4) ? wu : wg;
    const int colb = (s & 15) * 112;
    float a[8] = {0, 0, 0, 0, 0, 0, 0, 0};
    const int cc = tid % 14, rg = tid / 14;
    u32 warm = 0;
    {
      u32x4 wv4[7];
#pragma unroll
      for (int i = 0; i < 7; ++i) { const int c = tid + 512 * i; wv4[i] = *(const u32x4*)(wd + (size_t)s * 25088 + (c < 3136 ? c : 0) * 8); }
#pragma unroll
      for (int i = 0; i < 7; ++i) warm |= wv4[i][0] & wv4[i][1] & wv4[i][2] & wv4[i][3];
      asm volatile("" :: "v"(warm));
    }
    if (tid < 448) {
      u32x4 v[14];
#pragma unroll
      for (int r = 0; r < 14; ++r) v[r] = *(const u32x4*)(W + (rg * 14 + r) * FF + colb + cc * 8);
#pragma unroll
      for (int r = 0; r < 14; ++r)
#pragma unroll
        for (int i = 0; i < 4; ++i) { a[2 * i] += bf2f(v[r][i] & 0xffffu); a[2 * i + 1] += bfhi(v[r][i]); }
      float* red = (float*)lds;
#pragma unroll
      for (int i = 0; i < 8; ++i) red[rg * 112 + cc * 8 + i] = a[i];
    }
    __syncthreads();
    if (tid < 112) {
      float t = 0.f;
      for (int r = 0; r < 32; ++r) t += ((float*)lds)[r * 112 + tid];
      __hip_atomic_store((gu64*)(ws + WS_CS) + (s >> 4) * FF + colb + tid, ((u64)tag << 32) | __float_as_uint(t), RLX_AGENT);
    }
    if (wv < 4) {
      gu32* fl = (gu32*)(ws + WS_FLA) + (wv * 56 + (lane < 56 ? lane : 0)) * 32;
      for (int spin = 0; spin < (1 << 16); ++spin) {
        if (__all(__hip_atomic_load(fl, RLX_AGENT) == tag)) break;
        __builtin_amdgcn_s_sleep(2);
      }
    }
    __syncthreads();
    {
      const rsrc_t rh2 = mkrsrc((const u16*)(ws + WS_HT) + 64 * s);
      u32x4 hv[7];
#pragma unroll
      for (int i = 0; i < 7; ++i) hv[i] = __builtin_amdgcn_raw_buffer_load_b128(rh2, (((tid >> 3) + 64 * i) * HS + (tid & 7) * 8) * 2, 0, 16);
      f32x2 sa[4], sq[4];
#pragma unroll
      for (int k = 0; k < 4; ++k) { sa[k] = (f32x2){0.f, 0.f}; sq[k] = (f32x2){0.f, 0.f}; }
#pragma unroll
      for (int i = 0; i < 7; ++i)
#pragma unroll
        for (int k = 0; k < 4; ++k) { const u32 wi = hv[i][k]; const f32x2 v = {bf2f(wi & 0xffffu), bfhi(wi)}; sa[k] += v; sq[k] = __builtin_elementwise_fma(v, v, sq[k]); }
      f32x4* r1 = (f32x4*)(lds + (tid >> 3) * 528 + (tid & 7) * 64);
#pragma unroll
      for (int k = 0; k < 4; ++k) r1[k] = (f32x4){sa[k][0], sq[k][0], sa[k][1], sq[k][1]};
      __syncthreads();
      f32x2 t2 = {0.f, 0.f};
#pragma unroll
      for (int i = 0; i < 8; ++i) t2 += *(const f32x2*)(lds + ((tid >> 6) * 8 + i) * 528 + (tid & 63) * 8);
      *(f32x2*)(lds + 34816 + (tid >> 6) * 512 + (tid & 63) * 8) = t2;
      __syncthreads();
      if (tid < 64) {
        f32x2 tot = {0.f, 0.f};
#pragma unroll
        for (int i = 0; i < 8; ++i) tot += *(const f32x2*)(lds + 34816 + i * 512 + tid * 8);
        const float mean = tot[0] * (1.f / DM);
        float var = tot[1] * (1.f / DM) - mean * mean; var = var < 0.f ? 0.f : var;
        const float sd = rbf(__builtin_sqrtf(rbf(rbf(var) + 1e-5f)));
        __hip_atomic_store((gu64*)(ws + WS_GRAN2) + 64 * s + tid, ((u64)tag << 32) | pk_bf16(mean, sd), RLX_AGENT);
      }
    }
    STAMP_FLUSH(0);
    return;
  }
  *(u32x4*)(lds + A_T1 + tid * 16) = xt;
  const float dt = rbf(__builtin_amdgcn_exp2f(bf2f(p_ldt) * 1.44269504088896341f));
  const float zr = dt * bf2f(p_are), zi = dt * bf2f(p_aim);
  const f32x2 E8 = cexp_s(zr, zi, 8.f);
  const f32x2 E1 = cexp_s(zr, -zi, 1.f), F = cexp_s(zr, -zi, 256.f);
  {
    const int grp = lane >> 4, q = (lane & 15) >> 2, p = lane & 3;
#pragma unroll
    for (int i = 0; i < 2; ++i) {
      const int tb = 16 * wv + 8 * i + 4 * (grp >> 1);
      const s16x4 v = __builtin_amdgcn_ds_read_tr16_b64_v4i16((LAS s16x4*)(lds + A_T1 + (tb + q) * 64 + (grp & 1) * 32 + p * 8));
      const u32x2 vw = __builtin_bit_cast(u32x2, v);
      *(u32x4*)(lds + A_T2 + (16 * (grp & 1) + (lane & 15)) * 528 + tb * 4) = (u32x4){vw[0], tag, vw[1], tag};
    }
  }
  const u32 scb = lds_addr(lds) + A_SCR + wv * A_SCRW;
  const CM M8 = mk(E8);
  f32x2 p0 = {16384.f, 0.f}, p1 = cmul(p0, M8), p2 = cmul(p1, M8), p3 = cmul(p2, M8);
  const CM M32 = mk(cmul(p2, mk(p2 * (1.f / 16384.f))) * (1.f / 16384.f));
  __syncthreads();
  {
    __amdgpu_buffer_rsrc_t rs = __builtin_amdgcn_make_buffer_rsrc((void*)(ws + WS_XG), 0, DM * 8192, 0x00020000);
#pragma unroll
    for (int k = 0; k < 2; ++k) {
      const int id = tid + 512 * k, c = id >> 5, pos = id & 31;
      const u32x4 v = *(const u32x4*)(lds + A_T2 + c * 528 + pos * 16);
      __builtin_amdgcn_raw_buffer_store_b128(v, rs, (32 * (w >> 4) + c) * 8192 + (w & 15) * 512 + pos * 16, 0, 16);
    }
  }
  STAMP(0, 2);
  {
    const float br = dt * bf2f(p_bre), bi = dt * bf2f(p_bim);
    const float cr = bf2f(p_cre), ci = bf2f(p_cim);
    const float CBr = cr * br - ci * bi, CBi = cr * bi + ci * br;
    float mx = fmaxf(fabsf(CBr), fabsf(CBi)), sabs = fabsf(CBr) + fabsf(CBi);
    wave_max_sum(mx, sabs);
    const float scA = pow2_for(mx, 14);
    if (lane == 0) { ((float*)(lds + A_SW))[wv] = pow2_inv(scA) * (1.f / 16384.f); ((float*)(lds + A_SW))[8 + wv] = sabs; }
#define COL4(R) { put4<(R) * 272>(scb, pk2(p0), pk2(p1), pk2(p2), pk2(p3)); \
                  if ((R) < 28) { p0 = cmul(p0, M32); p1 = cmul(p1, M32); p2 = cmul(p2, M32); p3 = cmul(p3, M32); } }
    COL4(0) COL4(4) COL4(8) COL4(12) COL4(16) COL4(20) COL4(24) COL4(28)
#undef COL4
    const CM M1 = mk(E1), MF = mk(F);
    const f32x2 E2 = cmul(E1, M1);
    const CM M2 = mk(E2), M4 = mk(cmul(E2, M2));
    const f32x2 X0 = {CBr * scA, -CBi * scA};
    const f32x2 X1 = cmul(X0, M1), G1 = cmul(X0, MF);
    const f32x2 X2 = cmul(X0, M2), X3 = cmul(X1, M2), G2 = cmul(G1, MF), Y1 = cmul(G1, M2);
    put4<(32 + 0) * 272>(scb, pk2(X0), pk2(X1), pk2(X2), pk2(X3));
    const f32x2 X4 = cmul(X0, M4), X5 = cmul(X1, M4), X6 = cmul(X2, M4), X7 = cmul(X3, M4);
    const f32x2 G3 = cmul(G2, MF), Y2 = cmul(G1, M4), Y3 = cmul(Y1, M4);
    put4<(32 + 4) * 272>(scb, pk2(X4), pk2(X5), pk2(X6), pk2(X7));
    put4<(32 + 8) * 272>(scb, pk2(G1), pk2(Y1), pk2(Y2), pk2(Y3));
    const f32x2 G4 = cmul(G3, MF), Z2 = cmul(G2, M4), Z3 = cmul(G3, M4);
    put4<(32 + 12) * 272>(scb, pk2(G2), pk2(Z2), pk2(G3), pk2(Z3));
    const CM MF2 = mk(cmul(F, MF));
    const f32x2 G5 = cmul(G3, MF2), G6 = cmul(G4, MF2);
    const f32x2 G7 = cmul(G5, MF2), G8 = cmul(G6, MF2);
    put4<(32 + 16) * 272>(scb, pk2(G4), pk2(G5), pk2(G6), pk2(G7));
    {
      const u32 g8 = pk2(G8);
      asm volatile("s_mov_b32 m0, %0\n\ts_nop 0\n\tds_write_addtid_b32 %1 offset:%2" :: "s"(scb), "v"(g8), "n"((32 + 20) * 272) : "memory");
    }
    char* scr = lds + A_SCR + wv * A_SCRW;
    const char* fr = scr + (lane & 31) * 272 + (lane >> 5) * 16;
    f16x8 bf[8], af[8];
#pragma unroll
    for (int ks = 0; ks < 8; ++ks) { bf[ks] = *(const f16x8*)(fr + ks * 32); af[ks] = *(const f16x8*)(fr + 32 * 272 + ks * 32); }
    f32x16 acc0 = {}, acc1 = {};
#pragma unroll
    for (int ks = 0; ks < 8; ks += 2) {
      acc0 = __builtin_amdgcn_mfma_f32_32x32x16_f16(af[ks], bf[ks], acc0, 0, 0, 0);
      acc1 = __builtin_amdgcn_mfma_f32_32x32x16_f16(af[ks + 1], bf[ks + 1], acc1, 0, 0, 0);
    }
#pragma unroll
    for (int r = 0; r < 12; ++r) {
      int row = (r & 3) + 8 * (r >> 2) + 4 * (lane >> 5);
      *(float*)(scr + row * 128 + (lane & 31) * 4) = acc0[r] + acc1[r];
    }
  }
  STAMP(0, 3);
  const __amdgpu_buffer_rsrc_t rs_g = __builtin_amdgcn_make_buffer_rsrc((void*)(ws + WS_GRAN), 0, SL * 8, 0x00020000);
  const __amdgpu_buffer_rsrc_t rs_x = __builtin_amdgcn_make_buffer_rsrc((void*)(ws + WS_XG + (size_t)c0 * 8192), 0, 2 * 8192, 0x00020000);
  u32x4 g[4], xg[4];
  const int t8 = tid & 255;
  auto fetch = [&]() {
#pragma unroll
    for (int i = 0; i < 4; ++i) g[i] = __builtin_amdgcn_raw_buffer_load_b128(rs_g, t8 * 64 + i * 16, 0, 16);
#pragma unroll
    for (int i = 0; i < 4; ++i) xg[i] = __builtin_amdgcn_raw_buffer_load_b128(rs_x, (i >> 1) * 8192 + t8 * 32 + (i & 1) * 16, 0, 16);
  };
  if (wv < 4) fetch();
  asm volatile("s_waitcnt lgkmcnt(0)" ::: "memory");
  bar();
  float kinv0, kinv1;
  {
    const float* sw = (const float*)(lds + A_SW);
    const float k0 = pow2_for(sw[8] + sw[9] + sw[10] + sw[11], 15), k1 = pow2_for(sw[12] + sw[13] + sw[14] + sw[15], 15);
    kinv0 = pow2_inv(k0); kinv1 = pow2_inv(k1);
    const float ks = ch ? k1 : k0;
    const float i0 = sw[4 * ch] * ks, i1 = sw[4 * ch + 1] * ks, i2 = sw[4 * ch + 2] * ks, i3 = sw[4 * ch + 3] * ks;
    const char* p = lds + A_SCR + (4 * ch) * A_SCRW + (tid & 255) * 4;
    float v[3][4];
#pragma unroll
    for (int it = 0; it < 3; ++it)
#pragma unroll
      for (int k = 0; k < 4; ++k) v[it][k] = *(const float*)(p + it * 1024 + k * A_SCRW);
#pragma unroll
    for (int it = 0; it < 3; ++it) {
      const int e = (tid & 255) + 256 * it;
      const float t = v[it][0] * i0 + v[it][1] * i1 + v[it][2] * i2 + v[it][3] * i3;
      if (it < 2 || e < 672) *(_Float16*)(lds + A_TB + ch * A_TBC + e * 2) = (_Float16)t;
    }
  }
  asm volatile("s_waitcnt lgkmcnt(0)" ::: "memory");
  bar();
  if (wv >= 4) {
#pragma unroll
    for (int cc = 0; cc < 2; ++cc) {
      const char* tb = lds + A_TB + cc * A_TBC;
      u32 P[6];
#pragma unroll
      for (int k = 0; k < 6; ++k) {
        const u32 iw = k < 4 ? ix0[k] : ix1[k - 4];
        const u32 lo = *(const u16*)(tb + (iw & 0xffffu));
        const u32 hi = (k < 5) ? *(const u16*)(tb + (iw >> 16)) : 0u;
        P[k] = lo | (hi << 16);
      }
      u32 Q[5];
#pragma unroll
      for (int k = 0; k < 5; ++k) Q[k] = __builtin_amdgcn_alignbit(P[k + 1], P[k], 16);
      char* cp = lds + A_CP + cc * A_CPC + t8 * 16;
      *(u32x4*)(cp + 0 * A_CPS) = (u32x4){Q[1], Q[2], Q[3], Q[4]};
      *(u32x4*)(cp + 1 * A_CPS) = (u32x4){P[1], P[2], P[3], P[4]};
      *(u32x4*)(cp + 2 * A_CPS) = (u32x4){Q[0], Q[1], Q[2], Q[3]};
      *(u32x4*)(cp + 3 * A_CPS) = (u32x4){P[0], P[1], P[2], P[3]};
    }
    if (t8 < 32) *(u32x4*)(lds + A_CP + (t8 >> 2) * A_CPS + (256 + (t8 & 3)) * 16) = (u32x4){0, 0, 0, 0};
    STAMP(0, 4);
  } else {
    for (int spin = 0; spin < (1 << 16); ++spin) {
      u32 bad = 0;
#pragma unroll
      for (int i = 0; i < 4; ++i) bad |= (g[i][1] ^ tag) | (g[i][3] ^ tag) | (xg[i][1] ^ tag) | (xg[i][3] ^ tag);
      if (__all(bad == 0)) break;
      __builtin_amdgcn_s_sleep(2);
      fetch();
    }
    STAMP(0, 4);
    float uu[2][8];
#pragma unroll
    for (int i = 0; i < 8; ++i) {
      const u32 st = g[i >> 1][(i & 1) * 2];
      const float m = bf2f(st & 0xffffu), rs_ = __builtin_amdgcn_rcpf(bfhi(st));
#pragma unroll
      for (int c = 0; c < 2; ++c) {
        const u32 xw = xg[2 * c + (i >> 2)][(i & 2)];
        const float xv = (i & 1) ? bfhi(xw) : bf2f(xw & 0xffffu);
        uu[c][i] = rbf(rbf(xv - m) * rs_);
      }
    }
#pragma unroll
    for (int c = 0; c < 2; ++c) {
      *(u32x4*)(lds + A_UU + c * A_UUC + ((tid >> 2) + 31) * 80 + (tid & 3) * 16) =
          (u32x4){pk_f16(uu[c][0], uu[c][1]), pk_f16(uu[c][2], uu[c][3]), pk_f16(uu[c][4], uu[c][5]), pk_f16(uu[c][6], uu[c][7])};
      *(u32x4*)(lds + A_XX + c * 5120 + (tid >> 2) * 80 + (tid & 3) * 16) = (u32x4){xg[2 * c][0], xg[2 * c][2], xg[2 * c + 1][0], xg[2 * c + 1][2]};
    }
  }
  STAMP(0, 5);
  asm volatile("s_waitcnt lgkmcnt(0)" ::: "memory");
  bar();
  if (b == 0 && tid == 0) __hip_atomic_store(epoch_p, tag, RLX_AGENT);
  const int m = 4 * (xcd >> 1) + j / 7, nb = 7 * (xcd & 1) + j % 7;
  const int mm = 4 * xcd + j / 7, nn = j % 7;
  const int lw = wv & 3;
  const u32 ldsb = lds_addr(lds);
  const rsrc_t rg = mkrsrc(wg + 128 * nb), ru = mkrsrc(wu + 128 * nb), rh = mkrsrc((const u16*)(ws + WS_HT) + 128 * m);
  const int lk = lw & 1;
  const int wrow = 16 * lk + (lane >> 3), wcp = lane & 7, wkey = ((wrow >> 1) & 1) << 2;
  const u32 vg = (wrow * FF + (wcp ^ wkey) * 8) * 2, vu = (wrow * FF + (wcp ^ wkey ^ 2) * 8) * 2;
  auto issueW = [&](int t) {
    const int p_ = t >= 14, s_ = p_ ? 27 - t : t;
    const u32 d = ldsb + P_W + ((t + 2) % 7) * SLOT + lk * 2048, so = s_ * 32 * FF * 2 + (p_ ^ (m & 1)) * 128;
    bdma16(rg, vg, so, d); bdma16(rg, vg, so + 8 * FF * 2, d + 1024);
    bdma16(ru, vu, so, d + 4096); bdma16(ru, vu, so + 8 * FF * 2, d + 4096 + 1024);
  };
  const int hrow = 16 * lk + (lane >> 4), hcp = lane & 15;
  const u32 vh = (hrow * HS + (hcp ^ ((hrow & 3) << 2)) * 8) * 2;
  auto issueH = [&](int s_) {
    const u32 d = ldsb + (s_ % 12) * SLOT + lk * 4096, so = s_ * 32 * HS * 2;
#pragma unroll
    for (int k = 0; k < 4; ++k) bdma16_dev(rh, vh, so + 4 * k * HS * 2, d + 1024 * k);
  };
  if (wv == 4 || wv == 5) { issueW(0); issueW(1); issueW(2); }
  const int cw = (wv >> 1) & 1, hf = wv & 1;
  const int q = lane & 31, h = lane >> 5;
  if (wv < 4) {
    const int a4 = q >> 2, s4 = q & 3;
    const char* abase = lds + A_CP + cw * A_CPC + s4 * A_CPS + 4088 - 8 * a4 + 16 * h - 32 * (32 * hf + 30) - 2048;
    const char* bbase = lds + A_UU + cw * A_UUC + (q + 31 - (16 * hf + 15)) * 80 + 16 * h;
    typedef _Float16 f16x4v __attribute__((ext_vector_type(4)));
    auto lda = [&](const char* p_) {
      const f16x4v lo = *(volatile LAS const f16x4v*)(p_), hi = *(volatile LAS const f16x4v*)(p_ + 8);
      return __builtin_shufflevector(lo, hi, 0, 1, 2, 3, 4, 5, 6, 7);
    };
    f32x16 y0 = {}, y1 = {};
    f16x8 fa[2][8];
#pragma unroll
    for (int kk = 0; kk < 2; ++kk) {
      const int erel = 30 + kk;
      fa[0][4 * kk] = lda(abase + 2048 + 32 * erel);
      fa[0][4 * kk + 1] = *(const f16x8*)(bbase + 15 * 80 + kk * 32);
      fa[0][4 * kk + 2] = *(const f16x8*)(bbase + (15 + 32) * 80 + kk * 32);
      fa[0][4 * kk + 3] = lda(abase + 32 * erel);
    }
#pragma unroll
    for (int i = 0; i < 16; ++i) {
      const f16x8 (&f)[8] = fa[i & 1];
      f16x8 (&g)[8] = fa[(i + 1) & 1];
#pragma unroll
      for (int kk = 0; kk < 2; ++kk) {
        const int erel = 30 - (2 * (i + 1) - kk);
        y0 = __builtin_amdgcn_mfma_f32_32x32x16_f16(f[4 * kk], f[4 * kk + 1], y0, 0, 0, 0);
        if (i + 1 < 16) g[4 * kk] = lda(abase + 2048 + 32 * erel);
        FENCE();
        y1 = __builtin_amdgcn_mfma_f32_32x32x16_f16(f[4 * kk], f[4 * kk + 2], y1, 0, 0, 0);
        if (i + 1 < 16) { g[4 * kk + 1] = *(const f16x8*)(bbase + (15 - i - 1) * 80 + kk * 32); g[4 * kk + 2] = *(const f16x8*)(bbase + (15 - i - 1 + 32) * 80 + kk * 32); }
        FENCE();
        y1 = __builtin_amdgcn_mfma_f32_32x32x16_f16(f[4 * kk + 3], f[4 * kk + 1], y1, 0, 0, 0);
        if (i + 1 < 16) g[4 * kk + 3] = lda(abase + 32 * erel);
        FENCE();
      }
    }
    char* pp = lds + A_EX + wv * A_EXW + q * 144 + h * 16;
#pragma unroll
    for (int g = 0; g < 4; ++g) {
      *(f32x4*)(pp + g * 32) = (f32x4){y0[4 * g], y0[4 * g + 1], y0[4 * g + 2], y0[4 * g + 3]};
      *(f32x4*)(pp + 32 * 144 + g * 32) = (f32x4){y1[4 * g], y1[4 * g + 1], y1[4 * g + 2], y1[4 * g + 3]};
    }
  }
  STAMP(0, 6);
  asm volatile("s_waitcnt lgkmcnt(0)" ::: "memory");
  bar();
  {
    const int c = tid >> 8;
    const char* pp = lds + A_EX + (2 * c) * A_EXW + (t8 >> 2) * 144 + (t8 & 3) * 32;
    const f32x4 a0 = *(const f32x4*)pp, a1 = *(const f32x4*)(pp + 16), b0 = *(const f32x4*)(pp + A_EXW), b1 = *(const f32x4*)(pp + A_EXW + 16);
    const f16x8 u8 = *(const f16x8*)(lds + A_UU + c * A_UUC + ((t8 >> 2) + 31) * 80 + (t8 & 3) * 16);
    const u32x4 x8 = *(const u32x4*)(lds + A_XX + c * 5120 + (t8 >> 2) * 80 + (t8 & 3) * 16);
    const float ki = c ? kinv1 : kinv0, dk = c ? bfhi(p_d01) : bf2f(p_d01 & 0xffffu);
    u32x4 o;
#pragma unroll
    for (int i = 0; i < 4; ++i) {
      const float y0 = (i < 2 ? a0[2 * i] + b0[2 * i] : a1[2 * i - 4] + b1[2 * i - 4]) * ki;
      const float y1 = (i < 2 ? a0[2 * i + 1] + b0[2 * i + 1] : a1[2 * i - 3] + b1[2 * i - 3]) * ki;
      const u32 yb = pk_bf16(y0, y1), ub = pk_bf16((float)u8[2 * i] * dk, (float)u8[2 * i + 1] * dk);
      const u32 sb = pk_bf16(bf2f(yb & 0xffffu) + bf2f(ub & 0xffffu), bfhi(yb) + bfhi(ub));
      const u32 xw = x8[i];
      o[i] = pk_bf16(bf2f(xw & 0xffffu) + bf2f(sb & 0xffffu), bfhi(xw) + bfhi(sb));
    }
    __builtin_amdgcn_raw_buffer_store_b128(o, mkrsrc(ws + WS_HT), ((c0 + c) * HS + 8 * t8) * 2, 0, 16);
  }
  wait_vm<0>();
  bar();
  if (tid == 0) __hip_atomic_store((gu32*)(ws + WS_FLA) + w * 32, tag, RLX_AGENT);
  STAMP(0, 7);

  const rsrc_t rHc = mkrsrc(ws + WS_H + (size_t)(2 * mm + lk) * 112 * 1024);
  const rsrc_t rw = mkrsrc(wd + 64 * nn);
  const int crow = 32 * lk + (lane >> 3), ccp = lane & 7;
  const u32 cvh = lane * 16, cvw = (crow * DM + (ccp ^ (((crow >> 1) & 1) << 2)) * 8) * 2;
  auto cW = [&](int u) {
    const int p_ = u >= 14, n_ = p_ ? u - 14 : u;
    const u32 d = ldsb + (11 - u % 7) * SLOT + lk * 4096, so = (128 * n_ + 64 * (p_ ^ ((mm >> 1) & 1))) * DM * 2;
#pragma unroll
    for (int k = 0; k < 4; ++k) bdma16(rw, cvw, so + 8 * k * DM * 2, d + 1024 * k);
  };
  auto cH = [&](int u) {
    const int p_ = u >= 14, n_ = p_ ? u - 14 : u, v = u % 12;
    const u32 d = ldsb + (v < 7 ? 12 + v : v - 7) * SLOT + lk * 4096, so = (8 * n_ + 4 * (p_ ^ ((mm >> 1) & 1))) * 1024;
#pragma unroll
    for (int k = 0; k < 4; ++k) bdma16_dev(rHc, cvh, so + 1024 * k, d + 1024 * k);
  };

  if (wv >= 4) {
    if (lw < 2) {
      issueW(3); issueW(4);
      {
        int slot = 0;
        u32 so = 5 * 32 * FF * 2 + (m & 1) * 128;
#pragma unroll 1
        for (int t = 0; t <= 22; ++t) {
          wait_vm<16>(); bar();
          const u32 d = ldsb + P_W + slot * SLOT + lk * 2048;
          bdma16(rg, vg, so, d); bdma16(rg, vg, so + 8 * FF * 2, d + 1024);
          bdma16(ru, vu, so, d + 4096); bdma16(ru, vu, so + 8 * FF * 2, d + 4096 + 1024);
          slot = slot == 6 ? 0 : slot + 1;
          so = t + 6 < 14 ? so + 32 * FF * 2 : (t + 6 == 14 ? (so ^ 128u) : so - 32 * FF * 2);
        }
        wait_vm<16>(); bar(); wait_vm<12>(); bar(); wait_vm<8>(); bar(); wait_vm<4>(); bar(); wait_vm<0>(); bar();
      }
      bar();
      bar(); bar();
      {
        int slot = 11;
        u32 so = (128 * 7 + 64 * ((mm >> 1) & 1)) * DM * 2;
#pragma unroll 1
        for (int u = 2; u <= 22; ++u) {
          wait_vm<16>(); bar();
          const u32 d = ldsb + slot * SLOT + lk * 4096;
#pragma unroll
          for (int k = 0; k < 4; ++k) bdma16(rw, cvw, so + 8 * k * DM * 2, d + 1024 * k);
          slot = slot == 5 ? 11 : slot - 1;
          so = u + 6 == 14 ? (64 * (((mm >> 1) & 1) ^ 1)) * DM * 2 : so + 128 * DM * 2;
        }
        wait_vm<16>(); bar(); wait_vm<12>(); bar(); wait_vm<8>(); bar(); wait_vm<4>(); bar(); wait_vm<0>(); bar();
      }
    } else {
      {
        const rsrc_t rc = mkrsrc(ws + WS_CS + (lk * FF + 128 * nb) * 8);
        u32x4 cg;
        for (int spin = 0; spin < (1 << 16); ++spin) {
          cg = __builtin_amdgcn_raw_buffer_load_b128(rc, lane * 16, 0, 16);
          if (__all(cg[1] == tag && cg[3] == tag)) break;
          __builtin_amdgcn_s_sleep(2);
        }
        *(u32x2*)(lds + B_CS + lk * 512 + lane * 8) = (u32x2){cg[0], cg[2]};
      }
      {
        gu32* f0 = (gu32*)(ws + WS_FLA) + (lk * 112 + (lane < 56 ? lane : 0)) * 32;
        for (int spin = 0; spin < (1 << 16); ++spin) {
          const u32 a_ = __hip_atomic_load(f0, RLX_AGENT), b_ = __hip_atomic_load(f0 + 56 * 32, RLX_AGENT);
          if (__all(a_ == tag && b_ == tag)) break;
          __builtin_amdgcn_s_sleep(2);
        }
        LAS u32* cnt = (LAS u32*)(lds + B_CNT);
        if (lane == 0) __hip_atomic_fetch_add(cnt, 1u, __ATOMIC_RELAXED, __HIP_MEMORY_SCOPE_WORKGROUP);
        for (int spin = 0; spin < (1 << 20); ++spin) {
          if (__hip_atomic_load(cnt, __ATOMIC_RELAXED, __HIP_MEMORY_SCOPE_WORKGROUP) >= 2u) break;
          __builtin_amdgcn_s_sleep(1);
        }
      }
      STAMP(0, 8);
      issueH(0); issueH(1); issueH(2);
      wait_vm<8>(); bar(); issueH(3); issueH(4); issueH(5);
      wait_vm<16>(); bar(); issueH(6); issueH(7); issueH(8);
      wait_vm<24>(); bar(); issueH(9); issueH(10); issueH(11);
      wait_vm<32>(); bar(); issueH(12);
      wait_vm<32>(); bar(); issueH(13);
      wait_vm<32>(); bar();
      wait_vm<28>(); bar(); wait_vm<24>(); bar(); wait_vm<20>(); bar(); wait_vm<16>(); bar();
      wait_vm<12>(); bar(); wait_vm<8>(); bar(); wait_vm<4>(); bar(); wait_vm<0>(); bar();
      bar(); bar();
      bar(); issueH(1);
      bar(); issueH(0);
#pragma unroll 1
      for (int u = 0; u < 7; ++u) {
        bar();
        const u32 d = ldsb + (11 - u) * SLOT + lk * 4096, so = (128 * u + 64 * ((mm >> 1) & 1)) * DM * 2;
#pragma unroll
        for (int k = 0; k < 4; ++k) bdma16(rw, cvw, so + 8 * k * DM * 2, d + 1024 * k);
      }
      bar();
      wait_vm<32>(); bar();
      wait_vm<28>(); bar();
      STAMP(0, 9);
      bar();
      {
        gu32* fl = (gu32*)(ws + WS_FLB) + (((mm >> 1) * 14 + (lane < 14 ? lane : 0)) * 2) * 32;
        for (int spin = 0; spin < (1 << 16); ++spin) {
          if (__all(__hip_atomic_load(fl, RLX_AGENT) == tag)) break;
          __builtin_amdgcn_s_sleep(2);
        }
      }
      STAMP(0, 11);
      cH(0); cH(1); cH(2);
      wait_vm<8>(); bar(); cH(3); cH(4); cH(5);
      wait_vm<16>(); bar(); cH(6); cH(7); cH(8);
      wait_vm<24>(); bar(); cH(9); cH(10); cH(11);
      wait_vm<32>(); bar(); cH(12); cH(13);
      {
        int pos = 2;
        u32 so = (8 * 0 + 4 * (((mm >> 1) & 1) ^ 1)) * 1024;
#pragma unroll 1
        for (int u = 4; u <= 17; ++u) {
          wait_vm<36>(); bar();
          const u32 d = ldsb + (pos < 7 ? 12 + pos : pos - 7) * SLOT + lk * 4096;
#pragma unroll
          for (int k = 0; k < 4; ++k) bdma16_dev(rHc, cvh, so + 1024 * k, d + 1024 * k);
          pos = pos == 11 ? 0 : pos + 1; so += 8 * 1024;
        }
        wait_vm<36>(); bar(); wait_vm<32>(); bar(); wait_vm<28>(); bar(); wait_vm<24>(); bar(); wait_vm<20>(); bar();
        wait_vm<16>(); bar(); wait_vm<12>(); bar(); wait_vm<8>(); bar(); wait_vm<4>(); bar(); wait_vm<0>(); bar();
      }
    }
    STAMP(0, 13);
    STAMP_FLUSH(0);
    asm volatile(".Lfused_end2:");
    return;
  }

  const int tp = wv & 1, ag = wv >> 1;
  const int grp = lane >> 4, fq = (lane & 15) >> 2, fp = lane & 3, fh = grp >> 1;
  int ao[2], bo[2];
  {
    const int akey = (((fq >> 1) & 1) << 2) ^ ((grp & 1) << 1);
#pragma unroll
    for (int i = 0; i < 2; ++i) ao[i] = P_W + (grp & 1) * 4096 + (8 * fh + fq) * 128 + (((2 * (2 * ag + i) + (fp >> 1)) ^ akey) * 16) + (fp & 1) * 8;
#pragma unroll
    for (int t = 0; t < 2; ++t)
      bo[t] = (8 * fh + fq) * 256 + (((4 * (2 * tp + t) + 2 * (grp & 1) + (fp >> 1)) ^ (fq << 2)) * 16) + (fp & 1) * 8;
  }
  f32x16 accs[2][2][2];
  struct KF { s16x8 a[2][2], b[2][2]; };
  KF X, Y;
  const rsrc_t rH = mkrsrc(ws + WS_H);
  const int hh = lane >> 5;
  f32x2 st2[2];
  auto epilogue = [&](auto pass_) {
    constexpr int p_ = decltype(pass_)::value;
    auto& acc = accs[p_];
    const float* cs = (const float*)(lds + B_CS);
#pragma unroll
    for (int i = 0; i < 2; ++i) {
      const int tile = 4 * (p_ ^ (m & 1)) + 2 * ag + i, f0 = 16 * tile + 4 * hh;
      const f32x4 g0 = *(const f32x4*)(cs + f0), g1 = *(const f32x4*)(cs + f0 + 8);
      const f32x4 u0 = *(const f32x4*)(cs + 128 + f0), u1 = *(const f32x4*)(cs + 128 + f0 + 8);
#pragma unroll
      for (int t = 0; t < 2; ++t) {
        const f32x2 nm = {st2[t][0], st2[t][0]}, r2 = {st2[t][1], st2[t][1]};
        u32x4 o;
#pragma unroll
        for (int r = 0; r < 4; ++r) {
          const f32x4 cg = r < 2 ? g0 : g1, cu = r < 2 ? u0 : u1;
          const int e = 2 * (r & 1);
          const f32x2 g = __builtin_elementwise_fma(nm, (f32x2){cg[e], cg[e + 1]}, (f32x2){acc[i][t][2 * r], acc[i][t][2 * r + 1]});
          const f32x2 u = __builtin_elementwise_fma(nm, (f32x2){cu[e], cu[e + 1]}, (f32x2){acc[i][t][8 + 2 * r], acc[i][t][8 + 2 * r + 1]});
          const f32x2 hv = (g * r2) * u;
          o[r] = pk_bf16(hv[0], hv[1]);
        }
        __builtin_amdgcn_raw_buffer_store_b128(o, rH, (((4 * m + 2 * tp + t) * 112 + 8 * nb + tile) * 64 + lane) * 16, 0, 16);
      }
    }
  };
  auto publish = [&](int p_) {
    asm volatile("s_waitcnt vmcnt(0)" ::: "memory");
    u32 old = 0;
    if (lane == 0) old = __hip_atomic_fetch_add((LAS u32*)(lds + CNT0) + p_, 1u, __ATOMIC_RELAXED, __HIP_MEMORY_SCOPE_WORKGROUP);
    if (lane == 0 && old == 3u) __hip_atomic_store((gu32*)(ws + WS_FLB) + ((m * 14 + nb) * 2 + p_) * 32, tag, RLX_AGENT);
  };
  auto mm8 = [&](auto pass_, const KF& f) {
    auto& acc = accs[decltype(pass_)::value];
#pragma unroll
    for (int g = 0; g < 8; ++g) {
      const int ks = g >> 2, i = (g >> 1) & 1, t = g & 1;
      acc[i][t] = __builtin_amdgcn_mfma_f32_32x32x16_bf16(__builtin_bit_cast(bf16x8, f.a[ks][i]), __builtin_bit_cast(bf16x8, f.b[ks][t]), acc[i][t], 0, 0, 0);
    }
  };
  int hsl = 0, wsl = 2;
  typedef LAS const char* lptr;
  auto trf = [&](u32 a0, int off) {
    s16x4 x0 = __builtin_amdgcn_ds_read_tr16_b64_v4i16((LAS s16x4*)(a0 + off));
    return x0;
  };
  auto stage = [&](auto pass_, auto first, KF& cur, const KF& prev) {
    auto& acc = accs[decltype(pass_)::value];
    u32 hb0 = ldsb + hsl * SLOT + bo[0], hb1 = ldsb + hsl * SLOT + bo[1], wa0 = ldsb + wsl * SLOT + ao[0], wa1 = ldsb + wsl * SLOT + ao[1];
    asm volatile("" : "+v"(hb0), "+v"(hb1), "+v"(wa0), "+v"(wa1));
    auto rdb = [&](int rk, int rt_) {
      const u32 a0 = rt_ ? hb1 : hb0;
      cur.b[rk][rt_] = __builtin_shufflevector(trf(a0, rk * 16 * 256), trf(a0, (rk * 16 + 4) * 256), 0, 1, 2, 3, 4, 5, 6, 7);
    };
    auto rda = [&](int rk, int rt_) {
      const u32 a0 = rt_ ? wa1 : wa0;
      cur.a[rk][rt_] = __builtin_shufflevector(trf(a0, rk * 16 * 128), trf(a0, (rk * 16 + 4) * 128), 0, 1, 2, 3, 4, 5, 6, 7);
    };
#pragma unroll
    for (int g = 0; g < 8; ++g) {
      const int ks = g >> 2, i = (g >> 1) & 1, t = g & 1;
      if (!decltype(first)::value)
        acc[i][t] = __builtin_amdgcn_mfma_f32_32x32x16_bf16(__builtin_bit_cast(bf16x8, prev.a[ks][i]), __builtin_bit_cast(bf16x8, prev.b[ks][t]), acc[i][t], 0, 0, 0);
      if (g == 0) { FENCE(); bar(); }
      if (g == 1) { rdb(0, 0); rda(0, 0); }
      if (g == 2) { rdb(0, 1); }
      if (g == 3) { rda(0, 1); }
      if (g == 4) { rdb(1, 0); }
      if (g == 5) { rda(1, 0); }
      if (g == 6) { rdb(1, 1); }
      if (g == 7) { rda(1, 1); }
      FENCE();
    }
    wsl = wsl == 6 ? 0 : wsl + 1;
  };
#pragma unroll
  for (int g = 0; g < 8; ++g) accs[g >> 2][(g >> 1) & 1][g & 1] = (f32x16){};
  stage(IC<0>{}, IC<1>{}, X, Y); hsl = 1;
  STAMP(0, 8);
  stage(IC<0>{}, IC<0>{}, Y, X); hsl = 2;
#pragma unroll 1
  for (int t = 2; t < 14; t += 2) {
    stage(IC<0>{}, IC<0>{}, X, Y); hsl = hsl == 11 ? 0 : hsl + 1;
    stage(IC<0>{}, IC<0>{}, Y, X); hsl = hsl == 11 ? 0 : hsl + 1;
  }
  STAMP(0, 15);
  const rsrc_t rg2 = mkrsrc(ws + WS_GRAN2 + (size_t)(128 * m + 64 * tp) * 8);
  u32x2 sg[2];
#pragma unroll
  for (int t = 0; t < 2; ++t) sg[t] = __builtin_amdgcn_raw_buffer_load_b64(rg2, (32 * t + (lane & 31)) * 8, 0, 16);
  mm8(IC<0>{}, Y);
  hsl = 1;
  stage(IC<1>{}, IC<1>{}, X, Y); hsl = 0;
  stage(IC<1>{}, IC<0>{}, Y, X); hsl = 11;
  stage(IC<1>{}, IC<0>{}, X, Y); hsl = 10;
  for (int spin = 0; spin < (1 << 16); ++spin) {
    if (__all(sg[0][1] == tag && sg[1][1] == tag)) break;
    __builtin_amdgcn_s_sleep(2);
#pragma unroll
    for (int t = 0; t < 2; ++t) sg[t] = __builtin_amdgcn_raw_buffer_load_b64(rg2, (32 * t + (lane & 31)) * 8, 0, 16);
  }
#pragma unroll
  for (int t = 0; t < 2; ++t) { const float rs = __builtin_amdgcn_rcpf(bfhi(sg[t][0])); st2[t] = (f32x2){-bf2f(sg[t][0] & 0xffffu), rs * rs}; }
  STAMP(0, 11);
  epilogue(IC<0>{});
  STAMP(0, 10);
  stage(IC<1>{}, IC<0>{}, Y, X); hsl = 9;
  stage(IC<1>{}, IC<0>{}, X, Y); hsl = 8;
  stage(IC<1>{}, IC<0>{}, Y, X); hsl = 7;
  publish(0);
#pragma unroll 1
  for (int t = 20; t < 28; t += 2) {
    stage(IC<1>{}, IC<0>{}, X, Y); hsl = hsl - 1;
    stage(IC<1>{}, IC<0>{}, Y, X); hsl = hsl - 1;
  }
  mm8(IC<1>{}, Y);
  STAMP(0, 9);
  bar();
  epilogue(IC<1>{});
  publish(1);
  const int dtl = wv & 1, tt = wv >> 1;
  const int tok = 64 * mm + 32 * tt + (lane & 31);
  const rsrc_t rres = mkrsrc(ws + WS_HT);
  u32 res[16];
#pragma unroll
  for (int i = 0; i < 16; ++i) res[i] = __builtin_amdgcn_raw_buffer_load_b16(rres, ((64 * nn + 32 * dtl + 8 * (i >> 2) + 4 * hh + (i & 3)) * HS + tok) * 2, 0, 16);
  const int achunk = 4 * dtl + 2 * (grp & 1) + (fp >> 1);
  const int caoff = (4 * fh + fq) * 128 + ((achunk ^ (((fq >> 1) & 1) << 2)) * 16) + (fp & 1) * 8;
  const int cboff = tt * 4096 + lane * 16;
  f32x16 cacc = {};
  struct F4 { s16x8 a[4]; bf16x8 b[4]; };
  F4 Xc, Yc;
  int cwb = 11, chv = 0;
  auto cstage = [&](auto first, F4& cur, const F4& prev) {
    bar();
    const char* sth = lds + (chv < 7 ? 12 + chv : chv - 7) * SLOT;
    const char* stw = lds + cwb * SLOT;
#pragma unroll
    for (int k = 0; k < 4; ++k) {
      if (!decltype(first)::value) cacc = __builtin_amdgcn_mfma_f32_32x32x16_bf16(__builtin_bit_cast(bf16x8, prev.a[k]), prev.b[k], cacc, 0, 0, 0);
      cur.a[k] = tr_frag(stw + caoff + k * 16 * 128, stw + caoff + (k * 16 + 8) * 128);
      cur.b[k] = *(const bf16x8*)(sth + cboff + k * 1024);
      FENCE();
    }
    chv = chv == 11 ? 0 : chv + 1; cwb = cwb == 5 ? 11 : cwb - 1;
  };
  cstage(IC<1>{}, Xc, Yc);
  STAMP(0, 12);
  cstage(IC<0>{}, Yc, Xc);
  cstage(IC<0>{}, Xc, Yc);
  cstage(IC<0>{}, Yc, Xc);
  if (wv == 0) {
    gu32* fl = (gu32*)(ws + WS_FLB) + (((mm >> 1) * 14 + (lane < 14 ? lane : 0)) * 2 + 1) * 32;
    for (int spin = 0; spin < (1 << 16); ++spin) {
      if (__all(__hip_atomic_load(fl, RLX_AGENT) == tag)) break;
      __builtin_amdgcn_s_sleep(2);
    }
  }
#pragma unroll 1
  for (int u = 4; u < 28; u += 2) { cstage(IC<0>{}, Xc, Yc); cstage(IC<0>{}, Yc, Xc); }
#pragma unroll
  for (int k = 0; k < 4; ++k) cacc = __builtin_amdgcn_mfma_f32_32x32x16_bf16(__builtin_bit_cast(bf16x8, Yc.a[k]), Yc.b[k], cacc, 0, 0, 0);
  STAMP(0, 13);
  u16* op = out + tok * DM + 64 * nn + 32 * dtl + 4 * hh;
#pragma unroll
  for (int g = 0; g < 4; ++g)
    *(u32x2*)(op + 8 * g) = (u32x2){pk_bf16(bf2f(res[4 * g]) + rbf(cacc[4 * g]), bf2f(res[4 * g + 1]) + rbf(cacc[4 * g + 1])),
                                    pk_bf16(bf2f(res[4 * g + 2]) + rbf(cacc[4 * g + 2]), bf2f(res[4 * g + 3]) + rbf(cacc[4 * g + 3]))};
  STAMP(0, 14);
  STAMP_FLUSH(0);
  asm volatile(".Lfused_end:");
}

extern "C" void kernel_launch(void* const* d_in, const int* in_sizes, int n_in, void* d_out, int out_size, void* d_ws, size_t ws_size,
                              hipStream_t stream) {
  (void)in_sizes; (void)n_in; (void)out_size; (void)ws_size;
  (void)hipFuncSetAttribute((const void*)fused_kernel, hipFuncAttributeMaxDynamicSharedMemorySize, K_LDS);
  fused_kernel<<<256, 512, K_LDS, stream>>>((const u16*)d_in[0], (const u16*)d_in[1], (const u16*)d_in[2], (const u16*)d_in[3], (const u16*)d_in[4],
                                            (const u16*)d_in[5], (const u16*)d_in[6], (const u16*)d_in[7], (const u16*)d_in[8],
                                            (const u16*)d_in[10], (const u16*)d_in[11], (const u16*)d_in[12], (char*)d_ws, (u16*)d_out);
}
```

```cpp
#include <hip/hip_runtime.h>
#include <stdint.h>

typedef unsigned short u16;
typedef unsigned int u32;
typedef unsigned long long u64;
typedef float f32x2 __attribute__((ext_vector_type(2)));
typedef float f32x4 __attribute__((ext_vector_type(4)));
typedef float f32x16 __attribute__((ext_vector_type(16)));
typedef unsigned u32x2 __attribute__((ext_vector_type(2)));
typedef unsigned u32x4 __attribute__((ext_vector_type(4)));
typedef short s16x4 __attribute__((ext_vector_type(4)));
typedef short s16x8 __attribute__((ext_vector_type(8)));
typedef __bf16 bf16x8 __attribute__((ext_vector_type(8)));
typedef __bf16 bf16x2 __attribute__((ext_vector_type(2)));
typedef _Float16 f16x8 __attribute__((ext_vector_type(8)));
typedef _Float16 f16x4 __attribute__((ext_vector_type(4)));
typedef _Float16 f16x2 __attribute__((ext_vector_type(2)));
#define DI __device__ __forceinline__
#define LAS __attribute__((address_space(3)))
#define GAS __attribute__((address_space(1)))
typedef GAS unsigned gu32;
typedef GAS unsigned long long gu64;
#define RLX_AGENT __ATOMIC_RELAXED, __HIP_MEMORY_SCOPE_AGENT
#define FENCE() __builtin_amdgcn_sched_barrier(0)

constexpr int SL = 2048, DM = 448, NS = 256, FF = 1792;
constexpr int HS = SL + 64;

constexpr size_t WS_EPOCH = 0;
constexpr size_t WS_GRAN  = 4096;
constexpr size_t WS_CS    = 32768;
constexpr size_t WS_FLA   = 65536;
constexpr size_t WS_FLB   = WS_FLA + 32768;
constexpr size_t WS_GRAN2 = 163840;
constexpr size_t WS_XG    = 196608;
constexpr size_t WS_HT    = WS_XG + (size_t)DM * 8192;
constexpr size_t WS_H     = WS_HT + (size_t)DM * HS * 2;
constexpr size_t WS_DBG   = WS_H + (size_t)SL * FF * 2;

#define STAMP_DECL do {} while (0)
#define STAMP(k, i) do {} while (0)
#define STAMP_FLUSH(k) do {} while (0)

DI float bf2f(u32 v) { return __uint_as_float(v << 16); }
DI float bfhi(u32 v) { return __uint_as_float(v & 0xffff0000u); }
DI u32 pk_bf16(float a, float b) { f32x2 v = {a, b}; return __builtin_bit_cast(u32, __builtin_convertvector(v, bf16x2)); }
DI u32 pk_f16(float a, float b) { f32x2 v = {a, b}; return __builtin_bit_cast(u32, __builtin_convertvector(v, f16x2)); }
DI u32 pk2(f32x2 v) { return __builtin_bit_cast(u32, __builtin_convertvector(v, f16x2)); }
DI float rbf(float a) { return __uint_as_float(pk_bf16(a, a) << 16); }

template <int CTRL, int RM> DI float dpp0(float v) { return __int_as_float(__builtin_amdgcn_update_dpp(0, __float_as_int(v), CTRL, RM, 0xf, true)); }
DI float lane63(float v) { return __int_as_float(__builtin_amdgcn_readlane(__float_as_int(v), 63)); }
DI void wave_sum2(float& a, float& b) {
  a += dpp0<0x111, 0xf>(a); b += dpp0<0x111, 0xf>(b); a += dpp0<0x112, 0xf>(a); b += dpp0<0x112, 0xf>(b);
  a += dpp0<0x114, 0xf>(a); b += dpp0<0x114, 0xf>(b); a += dpp0<0x118, 0xf>(a); b += dpp0<0x118, 0xf>(b);
  a += dpp0<0x142, 0xa>(a); b += dpp0<0x142, 0xa>(b); a += dpp0<0x143, 0xc>(a); b += dpp0<0x143, 0xc>(b);
  a = lane63(a); b = lane63(b);
}
DI void wave_max_sum(float& m, float& s) {
  m = fmaxf(m, dpp0<0x111, 0xf>(m)); s += dpp0<0x111, 0xf>(s); m = fmaxf(m, dpp0<0x112, 0xf>(m)); s += dpp0<0x112, 0xf>(s);
  m = fmaxf(m, dpp0<0x114, 0xf>(m)); s += dpp0<0x114, 0xf>(s); m = fmaxf(m, dpp0<0x118, 0xf>(m)); s += dpp0<0x118, 0xf>(s);
  m = fmaxf(m, dpp0<0x142, 0xa>(m)); s += dpp0<0x142, 0xa>(s); m = fmaxf(m, dpp0<0x143, 0xc>(m)); s += dpp0<0x143, 0xc>(s);
  m = lane63(m); s = lane63(s);
}

DI void glds16(const void* g, u32 lds_dst) {
  u32 keep;
  asm volatile("s_mov_b32 %0, m0\n\ts_mov_b32 m0, %2\n\ts_nop 0\n\tglobal_load_lds_dwordx4 %1, off\n\ts_mov_b32 m0, %0"
               : "=&s"(keep) : "v"(g), "s"(lds_dst) : "memory");
}
typedef __amdgpu_buffer_rsrc_t rsrc_t;
DI rsrc_t mkrsrc(const void* p) { return __builtin_amdgcn_make_buffer_rsrc((void*)p, 0, 0x7fffffff, 0x00020000); }
DI void bdma16(rsrc_t rs, u32 voff, u32 soff, u32 lds_dst) {
  asm volatile("s_mov_b32 m0, %0\n\ts_nop 0\n\tbuffer_load_dwordx4 %1, %2, %3 offen lds" :: "s"(lds_dst), "v"(voff), "s"(rs), "s"(soff) : "memory");
}
DI void bdma16_dev(rsrc_t rs, u32 voff, u32 soff, u32 lds_dst) {
  asm volatile("s_mov_b32 m0, %0\n\ts_nop 0\n\tbuffer_load_dwordx4 %1, %2, %3 offen sc1 lds" :: "s"(lds_dst), "v"(voff), "s"(rs), "s"(soff) : "memory");
}
DI void bdma4(rsrc_t rs, u32 voff, u32 soff, u32 lds_dst) {
  asm volatile("s_mov_b32 m0, %0\n\ts_nop 0\n\tbuffer_load_dword %1, %2, %3 offen lds" :: "s"(lds_dst), "v"(voff), "s"(rs), "s"(soff) : "memory");
}
DI u32 lds_addr(const void* p) { return (u32)(size_t)(LAS const char*)p; }
DI s16x8 tr_frag(const char* p0, const char* p1) {
  s16x4 a = __builtin_amdgcn_ds_read_tr16_b64_v4i16((LAS s16x4*)p0);
  s16x4 b = __builtin_amdgcn_ds_read_tr16_b64_v4i16((LAS s16x4*)p1);
  return __builtin_shufflevector(a, b, 0, 1, 2, 3, 4, 5, 6, 7);
}

struct IdxTab { u16 v[2064]; };
constexpr int tap_entry(int l) {
  if (l < 0) return 21 * 32;
  int lam = l;
  if (l >= 256) {
    int sh = l >= 1024 ? 3 : (l >= 512 ? 2 : 1);
    int q = l >> sh, rem = l & ((1 << sh) - 1), half = 1 << (sh - 1);
    if (rem > half || (rem == half && (q & 1))) q += 1;
    lam = q << sh;
  }
  int hq = lam >> 8, lo = lam & 255, c = lo >> 3, r = 0;
  if (hq == 0) r = lo & 7;
  else if (hq == 1) r = 8 + ((lo & 7) >> 1);
  else if (hq == 2) r = 12 + ((lo & 7) >> 2);
  else if (hq == 3) r = 14 + ((lo & 7) >> 2);
  else if (hq < 8) r = 16 + hq - 4;
  else { r = 20; c = 0; }
  return r * 32 + c;
}
constexpr IdxTab make_tab() { IdxTab t{}; for (int y = 0; y < 2064; ++y) t.v[y] = (u16)(2 * tap_entry(2047 - y)); return t; }
__device__ const IdxTab RIDX __attribute__((aligned(16))) = make_tab();

DI float pow2_for(float mx, int target) {
  int e = (int)(__float_as_uint(mx) >> 23);
  int be = 127 + target - (e - 126);
  be = be > 254 ? 254 : (be < 1 ? 1 : be);
  return __uint_as_float((u32)be << 23);
}
DI float pow2_inv(float p) { return __uint_as_float((254u - (__float_as_uint(p) >> 23)) << 23); }

struct CM { f32x2 a, b; };
DI CM mk(f32x2 e) { return {e, (f32x2){-e.y, e.x}}; }
DI f32x2 cmul(f32x2 p, CM m) {
  f32x2 t = __builtin_shufflevector(p, p, 0, 0) * m.a;
  return __builtin_elementwise_fma(__builtin_shufflevector(p, p, 1, 1), m.b, t);
}
DI f32x2 cexp_s(float zr, float zi, float s) {
  float m = __builtin_amdgcn_exp2f(zr * (s * 1.44269504088896341f));
  float a = __builtin_amdgcn_fractf(zi * (s * 0.15915494309189535f));
  return (f32x2){m * __builtin_amdgcn_cosf(a), m * __builtin_amdgcn_sinf(a)};
}
template <int OFF>
DI void put4(u32 base, u32 v0, u32 v1, u32 v2, u32 v3) {
  asm volatile("s_mov_b32 m0, %0\n\ts_nop 0\n\tds_write_addtid_b32 %1 offset:%5\n\tds_write_addtid_b32 %2 offset:%6\n\t"
               "ds_write_addtid_b32 %3 offset:%7\n\tds_write_addtid_b32 %4 offset:%8"
               :: "s"(base), "v"(v0), "v"(v1), "v"(v2), "v"(v3), "n"(OFF), "n"(OFF + 272), "n"(OFF + 544), "n"(OFF + 816) : "memory");
}

template <int V> struct IC { static constexpr int value = V; };
template <int N> DI void wait_vm() { asm volatile("s_waitcnt vmcnt(%0)" :: "n"(N) : "memory"); }
DI void glds4(const void* g, u32 lds_dst) {
  u32 keep;
  asm volatile("s_mov_b32 %0, m0\n\ts_mov_b32 m0, %2\n\ts_nop 0\n\tglobal_load_lds_dword %1, off\n\ts_mov_b32 m0, %0"
               : "=&s"(keep) : "v"(g), "s"(lds_dst) : "memory");
}
DI void bar() { __builtin_amdgcn_s_barrier(); }

constexpr int A_T1 = 0, A_T2 = 8192;
constexpr int A_SCR = 25088, A_SCRW = 32 * 272 + 21 * 272;
constexpr int A_CP = 0, A_CPS = 4160, A_CPC = 4 * A_CPS;
constexpr int A_EX = 66560, A_EXW = 64 * 144, A_XX = A_EX + 4 * A_EXW;
constexpr int A_UU = A_SCR + 8 * A_SCRW, A_UUC = 95 * 80;
constexpr int A_TB = A_UU + 2 * A_UUC, A_TBC = 22 * 32 * 2;
constexpr int A_SW = A_TB + 2 * A_TBC;
constexpr int A_LDS = A_SW + 64;
static_assert(A_LDS <= 163840 && A_XX + 10240 <= A_UU && (A_SCR % 16) == 0 && (A_SCRW % 16) == 0 && (A_UU % 16) == 0, "lds");

constexpr int SLOT = 8192, P_W = 12 * SLOT;
constexpr int B_CS = 19 * SLOT, B_CNT = 163840 - 16, CNT0 = 163840 - 12, CNT1 = 163840 - 8;

constexpr int K_LDS = 163840;
constexpr int CODE_WARM_KB = 64;
static_assert(A_LDS <= K_LDS - 16, "lds");

__global__ __launch_bounds__(512, 2) void fused_kernel(const u16* __restrict__ x, const u16* __restrict__ Are, const u16* __restrict__ Aim,
    const u16* __restrict__ Bre, const u16* __restrict__ Bim, const u16* __restrict__ Cre, const u16* __restrict__ Cim,
    const u16* __restrict__ logdt, const u16* __restrict__ Dskip, const u16* __restrict__ wg, const u16* __restrict__ wu,
    const u16* __restrict__ wd, char* ws, u16* __restrict__ out) {
  extern __shared__ __attribute__((aligned(16))) char lds[];
  char* dbg = ws + WS_DBG; (void)dbg;
  STAMP_DECL;
  const int tid = threadIdx.x, lane = tid & 63;
  const int wv = __builtin_amdgcn_readfirstlane(tid >> 6);
  const int b = blockIdx.x, xcd = b & 7, j = b >> 3;
  const bool worker = j < 28;
  STAMP(0, 0);
  u32x4 cwarm[CODE_WARM_KB / 8];
  if (!worker) {
      u64 pc; u32 len, len2;
      asm volatile("s_getpc_b64 %0\n.Lfused_pc:\n\ts_mov_b32 %1, .Lfused_end-.Lfused_pc\n\ts_mov_b32 %2, .Lfused_end2-.Lfused_pc" : "=s"(pc), "=s"(len), "=s"(len2));
      len = (int)len2 > (int)len ? len2 : len;
      const char* code = (const char*)pc;
#pragma unroll
      for (int i = 0; i < CODE_WARM_KB / 8; ++i) {
        const u32 o = (u32)(i * 512 + tid) * 16u;
        cwarm[i] = *(const GAS u32x4*)(code + (((int)len > 16 && o + 16u <= len) ? o : 0u));
      }
  }
  asm volatile("" :: "s"(x), "s"(Are), "s"(Aim), "s"(Bre), "s"(Bim), "s"(Cre), "s"(Cim), "s"(logdt), "s"(Dskip), "s"(wg), "s"(wu), "s"(wd), "s"(ws), "s"(out));
  gu32* epoch_p = (gu32*)(ws + WS_EPOCH);
  gu64* gran = (gu64*)(ws + WS_GRAN);

  const int w = worker ? xcd * 28 + j : 0, c0 = 2 * w;
  const int trow = 8 * b + wv;
  const u32x4 xr = *(const u32x4*)(x + trow * DM + (lane < 56 ? lane : 0) * 8);
  const u32 tag = __hip_atomic_load(epoch_p, RLX_AGENT) + 1u;
  const int ch = tid >> 8, n = tid & 255;
  const int idx = (c0 + ch) * NS + n;
  const u32 p_ldt = logdt[c0 + ch];
  const u32 p_are = Are[idx], p_aim = Aim[idx];
  const u32x4 xt = *(const u32x4*)(x + (128 * (w & 15) + (tid >> 2)) * DM + 32 * (w >> 4) + (tid & 3) * 8);
  const u32 p_bre = Bre[idx], p_bim = Bim[idx], p_cre = Cre[idx], p_cim = Cim[idx];
  const u32 p_d01 = *(const u32*)(Dskip + c0);
  const u32x4 ix0 = *(const u32x4*)(RIDX.v + 8 * (tid & 255)), ix1 = *(const u32x4*)(RIDX.v + 8 * (tid & 255) + 8);
  if (tid == 0) { *(u32*)(lds + B_CNT) = 0u; *(u32*)(lds + CNT0) = 0u; *(u32*)(lds + CNT1) = 0u; }
  if (tid < 310) *(u32x4*)(lds + A_UU + (tid >= 155 ? A_UUC - 155 * 16 : 0) + tid * 16) = (u32x4){0, 0, 0, 0};
  if (tid >= 504) *(u32x4*)(lds + A_TB + ((tid >> 2) & 1) * A_TBC + 21 * 64 + (tid & 3) * 16) = (u32x4){0, 0, 0, 0};
  {
    const float piv = __uint_as_float((u32)__builtin_amdgcn_readfirstlane(xr[0]) << 16);
    float s = 0.f, q = 0.f, s2 = 0.f, q2 = 0.f;
#pragma unroll
    for (int i = 0; i < 4; ++i) {
      const float lo = bf2f(xr[i] & 0xffffu) - piv, hi = bfhi(xr[i]) - piv;
      s += lo; s2 += hi; q = fmaf(lo, lo, q); q2 = fmaf(hi, hi, q2);
    }
    s = lane < 56 ? s + s2 : 0.f; q = lane < 56 ? q + q2 : 0.f;
    wave_sum2(s, q);
    const float ms = s * (1.f / DM);
    float var = q * (1.f / DM) - ms * ms; var = var < 0.f ? 0.f : var;
    const float sd = rbf(__builtin_sqrtf(rbf(rbf(var) + 1e-5f)));
    if (lane == 0) __hip_atomic_store(gran + trow, ((u64)tag << 32) | pk_bf16(piv + ms, sd), RLX_AGENT);
  }
  STAMP(0, 1);
  if (!worker) {
    {
      u32 acc = 0;
#pragma unroll
      for (int i = 0; i < CODE_WARM_KB / 8; ++i) acc |= cwarm[i][0] & cwarm[i][1] & cwarm[i][2] & cwarm[i][3];
      asm volatile("" :: "v"(acc));
    }
    const int s = xcd * 4 + (j - 28);
    const u16* W = (s >> 4) ? wu : wg;
    const int colb = (s & 15) * 112;
    float a[8] = {0, 0, 0, 0, 0, 0, 0, 0};
    const int cc = tid % 14, rg = tid / 14;
    u32 warm = 0;
    {
      u32x4 wv4[7];
#pragma unroll
      for (int i = 0; i < 7; ++i) { const int c = tid + 512 * i; wv4[i] = *(const u32x4*)(wd + (size_t)s * 25088 + (c < 3136 ? c : 0) * 8); }
#pragma unroll
      for (int i = 0; i < 7; ++i) warm |= wv4[i][0] & wv4[i][1] & wv4[i][2] & wv4[i][3];
      asm volatile("" :: "v"(warm));
    }
    if (tid < 448) {
      u32x4 v[14];
#pragma unroll
      for (int r = 0; r < 14; ++r) v[r] = *(const u32x4*)(W + (rg * 14 + r) * FF + colb + cc * 8);
#pragma unroll
      for (int r = 0; r < 14; ++r)
#pragma unroll
        for (int i = 0; i < 4; ++i) { a[2 * i] += bf2f(v[r][i] & 0xffffu); a[2 * i + 1] += bfhi(v[r][i]); }
      float* red = (float*)lds;
#pragma unroll
      for (int i = 0; i < 8; ++i) red[rg * 112 + cc * 8 + i] = a[i];
    }
    __syncthreads();
    if (tid < 112) {
      float t = 0.f;
      for (int r = 0; r < 32; ++r) t += ((float*)lds)[r * 112 + tid];
      __hip_atomic_store((gu64*)(ws + WS_CS) + (s >> 4) * FF + colb + tid, ((u64)tag << 32) | __float_as_uint(t), RLX_AGENT);
    }
    if (wv < 4) {
      gu32* fl = (gu32*)(ws + WS_FLA) + (wv * 56 + (lane < 56 ? lane : 0)) * 32;
      for (int spin = 0; spin < (1 << 16); ++spin) {
        if (__all(__hip_atomic_load(fl, RLX_AGENT) == tag)) break;
        __builtin_amdgcn_s_sleep(2);
      }
    }
    __syncthreads();
    {
      const rsrc_t rh2 = mkrsrc((const u16*)(ws + WS_HT) + 64 * s);
      u32x4 hv[7];
#pragma unroll
      for (int i = 0; i < 7; ++i) hv[i] = __builtin_amdgcn_raw_buffer_load_b128(rh2, (((tid >> 3) + 64 * i) * HS + (tid & 7) * 8) * 2, 0, 16);
      f32x2 sa[4], sq[4];
#pragma unroll
      for (int k = 0; k < 4; ++k) { sa[k] = (f32x2){0.f, 0.f}; sq[k] = (f32x2){0.f, 0.f}; }
#pragma unroll
      for (int i = 0; i < 7; ++i)
#pragma unroll
        for (int k = 0; k < 4; ++k) { const u32 wi = hv[i][k]; const f32x2 v = {bf2f(wi & 0xffffu), bfhi(wi)}; sa[k] += v; sq[k] = __builtin_elementwise_fma(v, v, sq[k]); }
      f32x4* r1 = (f32x4*)(lds + (tid >> 3) * 528 + (tid & 7) * 64);
#pragma unroll
      for (int k = 0; k < 4; ++k) r1[k] = (f32x4){sa[k][0], sq[k][0], sa[k][1], sq[k][1]};
      __syncthreads();
      f32x2 t2 = {0.f, 0.f};
#pragma unroll
      for (int i = 0; i < 8; ++i) t2 += *(const f32x2*)(lds + ((tid >> 6) * 8 + i) * 528 + (tid & 63) * 8);
      *(f32x2*)(lds + 34816 + (tid >> 6) * 512 + (tid & 63) * 8) = t2;
      __syncthreads();
      if (tid < 64) {
        f32x2 tot = {0.f, 0.f};
#pragma unroll
        for (int i = 0; i < 8; ++i) tot += *(const f32x2*)(lds + 34816 + i * 512 + tid * 8);
        const float mean = tot[0] * (1.f / DM);
        float var = tot[1] * (1.f / DM) - mean * mean; var = var < 0.f ? 0.f : var;
        const float sd = rbf(__builtin_sqrtf(rbf(rbf(var) + 1e-5f)));
        __hip_atomic_store((gu64*)(ws + WS_GRAN2) + 64 * s + tid, ((u64)tag << 32) | pk_bf16(mean, sd), RLX_AGENT);
      }
    }
    STAMP_FLUSH(0);
    return;
  }
  *(u32x4*)(lds + A_T1 + tid * 16) = xt;
  {
    const int grp = lane >> 4, q = (lane & 15) >> 2, p = lane & 3;
#pragma unroll
    for (int i = 0; i < 2; ++i) {
      const int tb = 16 * wv + 8 * i + 4 * (grp >> 1);
      const s16x4 v = __builtin_amdgcn_ds_read_tr16_b64_v4i16((LAS s16x4*)(lds + A_T1 + (tb + q) * 64 + (grp & 1) * 32 + p * 8));
      const u32x2 vw = __builtin_bit_cast(u32x2, v);
      *(u32x4*)(lds + A_T2 + (16 * (grp & 1) + (lane & 15)) * 528 + tb * 4) = (u32x4){vw[0], tag, vw[1], tag};
    }
  }
  asm volatile("s_waitcnt lgkmcnt(0)" ::: "memory");
  bar();
  {
    __amdgpu_buffer_rsrc_t rs = __builtin_amdgcn_make_buffer_rsrc((void*)(ws + WS_XG), 0, DM * 8192, 0x00020000);
#pragma unroll
    for (int k = 0; k < 2; ++k) {
      const int id = tid + 512 * k, c = id >> 5, pos = id & 31;
      const u32x4 v = *(const u32x4*)(lds + A_T2 + c * 528 + pos * 16);
      __builtin_amdgcn_raw_buffer_store_b128(v, rs, (32 * (w >> 4) + c) * 8192 + (w & 15) * 512 + pos * 16, 0, 16);
    }
  }
  const float dt = rbf(__builtin_amdgcn_exp2f(bf2f(p_ldt) * 1.44269504088896341f));
  const float zr = dt * bf2f(p_are), zi = dt * bf2f(p_aim);
  const f32x2 E8 = cexp_s(zr, zi, 8.f);
  const f32x2 E1 = cexp_s(zr, -zi, 1.f), F = cexp_s(zr, -zi, 256.f);
  const u32 scb = lds_addr(lds) + A_SCR + wv * A_SCRW;
  const CM M8 = mk(E8);
  f32x2 p0 = {16384.f, 0.f}, p1 = cmul(p0, M8), p2 = cmul(p1, M8), p3 = cmul(p2, M8);
  const CM M32 = mk(cmul(p2, mk(p2 * (1.f / 16384.f))) * (1.f / 16384.f));
  const __amdgpu_buffer_rsrc_t rs_g = __builtin_amdgcn_make_buffer_rsrc((void*)(ws + WS_GRAN), 0, SL * 8, 0x00020000);
  const __amdgpu_buffer_rsrc_t rs_x = __builtin_amdgcn_make_buffer_rsrc((void*)(ws + WS_XG + (size_t)c0 * 8192), 0, 2 * 8192, 0x00020000);
  u32x4 g[4], xg[4];
  const int t8 = tid & 255;
  auto fetch = [&]() {
#pragma unroll
    for (int i = 0; i < 4; ++i) g[i] = __builtin_amdgcn_raw_buffer_load_b128(rs_g, t8 * 64 + i * 16, 0, 16);
#pragma unroll
    for (int i = 0; i < 4; ++i) xg[i] = __builtin_amdgcn_raw_buffer_load_b128(rs_x, (i >> 1) * 8192 + t8 * 32 + (i & 1) * 16, 0, 16);
  };
  STAMP(0, 2);
  {
    const float br = dt * bf2f(p_bre), bi = dt * bf2f(p_bim);
    const float cr = bf2f(p_cre), ci = bf2f(p_cim);
    const float CBr = cr * br - ci * bi, CBi = cr * bi + ci * br;
    float mx = fmaxf(fabsf(CBr), fabsf(CBi)), sabs = fabsf(CBr) + fabsf(CBi);
    wave_max_sum(mx, sabs);
    const float scA = pow2_for(mx, 14);
    if (lane == 0) { ((float*)(lds + A_SW))[wv] = pow2_inv(scA) * (1.f / 16384.f); ((float*)(lds + A_SW))[8 + wv] = sabs; }
#define COL4(R) { put4<(R) * 272>(scb, pk2(p0), pk2(p1), pk2(p2), pk2(p3)); \
                  if ((R) < 28) { p0 = cmul(p0, M32); p1 = cmul(p1, M32); p2 = cmul(p2, M32); p3 = cmul(p3, M32); } }
    COL4(0) COL4(4) COL4(8) COL4(12) COL4(16) COL4(20) COL4(24) COL4(28)
#undef COL4
    const CM M1 = mk(E1), MF = mk(F);
    const f32x2 E2 = cmul(E1, M1);
    const CM M2 = mk(E2), M4 = mk(cmul(E2, M2));
    const f32x2 X0 = {CBr * scA, -CBi * scA};
    const f32x2 X1 = cmul(X0, M1), G1 = cmul(X0, MF);
    const f32x2 X2 = cmul(X0, M2), X3 = cmul(X1, M2), G2 = cmul(G1, MF), Y1 = cmul(G1, M2);
    put4<(32 + 0) * 272>(scb, pk2(X0), pk2(X1), pk2(X2), pk2(X3));
    const f32x2 X4 = cmul(X0, M4), X5 = cmul(X1, M4), X6 = cmul(X2, M4), X7 = cmul(X3, M4);
    const f32x2 G3 = cmul(G2, MF), Y2 = cmul(G1, M4), Y3 = cmul(Y1, M4);
    put4<(32 + 4) * 272>(scb, pk2(X4), pk2(X5), pk2(X6), pk2(X7));
    put4<(32 + 8) * 272>(scb, pk2(G1), pk2(Y1), pk2(Y2), pk2(Y3));
    const f32x2 G4 = cmul(G3, MF), Z2 = cmul(G2, M4), Z3 = cmul(G3, M4);
    put4<(32 + 12) * 272>(scb, pk2(G2), pk2(Z2), pk2(G3), pk2(Z3));
    const CM MF2 = mk(cmul(F, MF));
    const f32x2 G5 = cmul(G3, MF2), G6 = cmul(G4, MF2);
    const f32x2 G7 = cmul(G5, MF2), G8 = cmul(G6, MF2);
    put4<(32 + 16) * 272>(scb, pk2(G4), pk2(G5), pk2(G6), pk2(G7));
    {
      const u32 g8 = pk2(G8);
      asm volatile("s_mov_b32 m0, %0\n\ts_nop 0\n\tds_write_addtid_b32 %1 offset:%2" :: "s"(scb), "v"(g8), "n"((32 + 20) * 272) : "memory");
    }
    if (wv < 4) fetch();
    char* scr = lds + A_SCR + wv * A_SCRW;
    const char* fr = scr + (lane & 31) * 272 + (lane >> 5) * 16;
    f16x8 bf[8], af[8];
#pragma unroll
    for (int ks = 0; ks < 8; ++ks) { bf[ks] = *(const f16x8*)(fr + ks * 32); af[ks] = *(const f16x8*)(fr + 32 * 272 + ks * 32); }
    f32x16 acc0 = {}, acc1 = {};
#pragma unroll
    for (int ks = 0; ks < 8; ks += 2) {
      acc0 = __builtin_amdgcn_mfma_f32_32x32x16_f16(af[ks], bf[ks], acc0, 0, 0, 0);
      acc1 = __builtin_amdgcn_mfma_f32_32x32x16_f16(af[ks + 1], bf[ks + 1], acc1, 0, 0, 0);
    }
#pragma unroll
    for (int r = 0; r < 12; ++r) {
      int row = (r & 3) + 8 * (r >> 2) + 4 * (lane >> 5);
      *(float*)(scr + row * 128 + (lane & 31) * 4) = acc0[r] + acc1[r];
    }
  }
  STAMP(0, 3);
  asm volatile("s_waitcnt lgkmcnt(0)" ::: "memory");
  bar();
  float kinv0, kinv1;
  {
    const float* sw = (const float*)(lds + A_SW);
    const float k0 = pow2_for(sw[8] + sw[9] + sw[10] + sw[11], 15), k1 = pow2_for(sw[12] + sw[13] + sw[14] + sw[15], 15);
    kinv0 = pow2_inv(k0); kinv1 = pow2_inv(k1);
    const float ks = ch ? k1 : k0;
    const float i0 = sw[4 * ch] * ks, i1 = sw[4 * ch + 1] * ks, i2 = sw[4 * ch + 2] * ks, i3 = sw[4 * ch + 3] * ks;
    const char* p = lds + A_SCR + (4 * ch) * A_SCRW + (tid & 255) * 4;
    float v[3][4];
#pragma unroll
    for (int it = 0; it < 3; ++it)
#pragma unroll
      for (int k = 0; k < 4; ++k) v[it][k] = *(const float*)(p + it * 1024 + k * A_SCRW);
#pragma unroll
    for (int it = 0; it < 3; ++it) {
      const int e = (tid & 255) + 256 * it;
      const float t = v[it][0] * i0 + v[it][1] * i1 + v[it][2] * i2 + v[it][3] * i3;
      if (it < 2 || e < 672) *(_Float16*)(lds + A_TB + ch * A_TBC + e * 2) = (_Float16)t;
    }
  }
  asm volatile("s_waitcnt lgkmcnt(0)" ::: "memory");
  bar();
  if (wv >= 4) {
#pragma unroll
    for (int cc = 0; cc < 2; ++cc) {
      const char* tb = lds + A_TB + cc * A_TBC;
      u32 P[6];
#pragma unroll
      for (int k = 0; k < 6; ++k) {
        const u32 iw = k < 4 ? ix0[k] : ix1[k - 4];
        const u32 lo = *(const u16*)(tb + (iw & 0xffffu));
        const u32 hi = (k < 5) ? *(const u16*)(tb + (iw >> 16)) : 0u;
        P[k] = lo | (hi << 16);
      }
      u32 Q[5];
#pragma unroll
      for (int k = 0; k < 5; ++k) Q[k] = __builtin_amdgcn_alignbit(P[k + 1], P[k], 16);
      char* cp = lds + A_CP + cc * A_CPC + t8 * 16;
      *(u32x4*)(cp + 0 * A_CPS) = (u32x4){Q[1], Q[2], Q[3], Q[4]};
      *(u32x4*)(cp + 1 * A_CPS) = (u32x4){P[1], P[2], P[3], P[4]};
      *(u32x4*)(cp + 2 * A_CPS) = (u32x4){Q[0], Q[1], Q[2], Q[3]};
      *(u32x4*)(cp + 3 * A_CPS) = (u32x4){P[0], P[1], P[2], P[3]};
    }
    if (t8 < 32) *(u32x4*)(lds + A_CP + (t8 >> 2) * A_CPS + (256 + (t8 & 3)) * 16) = (u32x4){0, 0, 0, 0};
    STAMP(0, 4);
  } else {
    for (int spin = 0; spin < (1 << 16); ++spin) {
      u32 bad = 0;
#pragma unroll
      for (int i = 0; i < 4; ++i) bad |= (g[i][1] ^ tag) | (g[i][3] ^ tag) | (xg[i][1] ^ tag) | (xg[i][3] ^ tag);
      if (__all(bad == 0)) break;
      __builtin_amdgcn_s_sleep(2);
      fetch();
    }
    STAMP(0, 4);
    float uu[2][8];
#pragma unroll
    for (int i = 0; i < 8; ++i) {
      const u32 st = g[i >> 1][(i & 1) * 2];
      const float m = bf2f(st & 0xffffu), rs_ = __builtin_amdgcn_rcpf(bfhi(st));
#pragma unroll
      for (int c = 0; c < 2; ++c) {
        const u32 xw = xg[2 * c + (i >> 2)][(i & 2)];
        const float xv = (i & 1) ? bfhi(xw) : bf2f(xw & 0xffffu);
        uu[c][i] = rbf(rbf(xv - m) * rs_);
      }
    }
#pragma unroll
    for (int c = 0; c < 2; ++c) {
      *(u32x4*)(lds + A_UU + c * A_UUC + ((tid >> 2) + 31) * 80 + (tid & 3) * 16) =
          (u32x4){pk_f16(uu[c][0], uu[c][1]), pk_f16(uu[c][2], uu[c][3]), pk_f16(uu[c][4], uu[c][5]), pk_f16(uu[c][6], uu[c][7])};
      *(u32x4*)(lds + A_XX + c * 5120 + (tid >> 2) * 80 + (tid & 3) * 16) = (u32x4){xg[2 * c][0], xg[2 * c][2], xg[2 * c + 1][0], xg[2 * c + 1][2]};
    }
  }
  STAMP(0, 5);
  asm volatile("s_waitcnt lgkmcnt(0)" ::: "memory");
  bar();
  if (b == 0 && tid == 0) __hip_atomic_store(epoch_p, tag, RLX_AGENT);
  const int m = 4 * (xcd >> 1) + j / 7, nb = 7 * (xcd & 1) + j % 7;
  const int mm = 4 * xcd + j / 7, nn = j % 7;
  const int lw = wv & 3;
  const u32 ldsb = lds_addr(lds);
  const rsrc_t rg = mkrsrc(wg + 128 * nb), ru = mkrsrc(wu + 128 * nb), rh = mkrsrc((const u16*)(ws + WS_HT) + 128 * m);
  const int wrow = 8 * lw + (lane >> 3), wcp = lane & 7, wkey = ((wrow >> 1) & 1) << 2;
  const u32 vg = (wrow * FF + (wcp ^ wkey) * 8) * 2, vu = (wrow * FF + (wcp ^ wkey ^ 2) * 8) * 2;
  auto issueWs = [&](u32 slot, u32 so) { const u32 d = ldsb + P_W + slot * SLOT + lw * 1024; bdma16(rg, vg, so, d); bdma16(ru, vu, so, d + 4096); };
  auto issueW = [&](int t) { const int p_ = t >= 14, s_ = p_ ? 27 - t : t; issueWs((t + 2) % 7, s_ * 32 * FF * 2 + (p_ ^ (m & 1)) * 128); };
  const int hrow = 4 * lw + (lane >> 4), hcp = lane & 15;
  const u32 vh = (hrow * HS + (hcp ^ ((hrow & 3) << 2)) * 8) * 2;
  auto issueH = [&](int s_) {
    const u32 d = ldsb + (s_ % 12) * SLOT + lw * 1024, so = s_ * 32 * HS * 2;
    bdma16_dev(rh, vh, so, d); bdma16_dev(rh, vh, so + 16 * HS * 2, d + 4096);
  };
  if (wv >= 4) { issueW(0); issueW(1); issueW(2); }
  const int cw = (wv >> 1) & 1, hf = wv & 1;
  const int q = lane & 31, h = lane >> 5;
  if (wv < 4) {
    const int a4 = q >> 2, s4 = q & 3;
    const char* abase = lds + A_CP + cw * A_CPC + s4 * A_CPS + 4088 - 8 * a4 + 16 * h - 32 * (32 * hf + 30) - 2048;
    const char* bbase = lds + A_UU + cw * A_UUC + (q + 31 - (16 * hf + 15)) * 80 + 16 * h;
    typedef _Float16 f16x4v __attribute__((ext_vector_type(4)));
    auto lda = [&](const char* p_) {
      const f16x4v lo = *(volatile LAS const f16x4v*)(p_), hi = *(volatile LAS const f16x4v*)(p_ + 8);
      return __builtin_shufflevector(lo, hi, 0, 1, 2, 3, 4, 5, 6, 7);
    };
    f32x16 y0 = {}, y1 = {};
    f16x8 fa[2][8];
#pragma unroll
    for (int kk = 0; kk < 2; ++kk) {
      const int erel = 30 + kk;
      fa[0][4 * kk] = lda(abase + 2048 + 32 * erel);
      fa[0][4 * kk + 1] = *(const f16x8*)(bbase + 15 * 80 + kk * 32);
      fa[0][4 * kk + 2] = *(const f16x8*)(bbase + (15 + 32) * 80 + kk * 32);
      fa[0][4 * kk + 3] = lda(abase + 32 * erel);
    }
#pragma unroll
    for (int i = 0; i < 16; ++i) {
      const f16x8 (&f)[8] = fa[i & 1];
      f16x8 (&g)[8] = fa[(i + 1) & 1];
#pragma unroll
      for (int kk = 0; kk < 2; ++kk) {
        const int erel = 30 - (2 * (i + 1) - kk);
        y0 = __builtin_amdgcn_mfma_f32_32x32x16_f16(f[4 * kk], f[4 * kk + 1], y0, 0, 0, 0);
        if (i + 1 < 16) g[4 * kk] = lda(abase + 2048 + 32 * erel);
        FENCE();
        y1 = __builtin_amdgcn_mfma_f32_32x32x16_f16(f[4 * kk], f[4 * kk + 2], y1, 0, 0, 0);
        if (i + 1 < 16) { g[4 * kk + 1] = *(const f16x8*)(bbase + (15 - i - 1) * 80 + kk * 32); g[4 * kk + 2] = *(const f16x8*)(bbase + (15 - i - 1 + 32) * 80 + kk * 32); }
        FENCE();
        y1 = __builtin_amdgcn_mfma_f32_32x32x16_f16(f[4 * kk + 3], f[4 * kk + 1], y1, 0, 0, 0);
        if (i + 1 < 16) g[4 * kk + 3] = lda(abase + 32 * erel);
        FENCE();
      }
    }
    char* pp = lds + A_EX + wv * A_EXW + q * 144 + h * 16;
#pragma unroll
    for (int g = 0; g < 4; ++g) {
      *(f32x4*)(pp + g * 32) = (f32x4){y0[4 * g], y0[4 * g + 1], y0[4 * g + 2], y0[4 * g + 3]};
      *(f32x4*)(pp + 32 * 144 + g * 32) = (f32x4){y1[4 * g], y1[4 * g + 1], y1[4 * g + 2], y1[4 * g + 3]};
    }
  }
  STAMP(0, 6);
  asm volatile("s_waitcnt lgkmcnt(0)" ::: "memory");
  bar();
  {
    const int c = tid >> 8;
    const char* pp = lds + A_EX + (2 * c) * A_EXW + (t8 >> 2) * 144 + (t8 & 3) * 32;
    const f32x4 a0 = *(const f32x4*)pp, a1 = *(const f32x4*)(pp + 16), b0 = *(const f32x4*)(pp + A_EXW), b1 = *(const f32x4*)(pp + A_EXW + 16);
    const f16x8 u8 = *(const f16x8*)(lds + A_UU + c * A_UUC + ((t8 >> 2) + 31) * 80 + (t8 & 3) * 16);
    const u32x4 x8 = *(const u32x4*)(lds + A_XX + c * 5120 + (t8 >> 2) * 80 + (t8 & 3) * 16);
    const float ki = c ? kinv1 : kinv0, dk = c ? bfhi(p_d01) : bf2f(p_d01 & 0xffffu);
    u32x4 o;
#pragma unroll
    for (int i = 0; i < 4; ++i) {
      const float y0 = (i < 2 ? a0[2 * i] + b0[2 * i] : a1[2 * i - 4] + b1[2 * i - 4]) * ki;
      const float y1 = (i < 2 ? a0[2 * i + 1] + b0[2 * i + 1] : a1[2 * i - 3] + b1[2 * i - 3]) * ki;
      const u32 yb = pk_bf16(y0, y1), ub = pk_bf16((float)u8[2 * i] * dk, (float)u8[2 * i + 1] * dk);
      const u32 sb = pk_bf16(bf2f(yb & 0xffffu) + bf2f(ub & 0xffffu), bfhi(yb) + bfhi(ub));
      const u32 xw = x8[i];
      o[i] = pk_bf16(bf2f(xw & 0xffffu) + bf2f(sb & 0xffffu), bfhi(xw) + bfhi(sb));
    }
    __builtin_amdgcn_raw_buffer_store_b128(o, mkrsrc(ws + WS_HT), ((c0 + c) * HS + 8 * t8) * 2, 0, 16);
  }
  wait_vm<0>();
  bar();
  if (tid == 0) __hip_atomic_store((gu32*)(ws + WS_FLA) + w * 32, tag, RLX_AGENT);
  STAMP(0, 7);

  const rsrc_t rw = mkrsrc(wd + 64 * nn);
  const u32 cvh = lane * 16;
  const rsrc_t rH2 = mkrsrc(ws + WS_H + ((size_t)(2 * mm + (lw >> 1)) * 112 + 2 * (lw & 1)) * 1024);
  const int drow = 16 * lw + (lane >> 3);
  const u32 dvw = (drow * DM + ((lane & 7) ^ (((drow >> 1) & 1) << 2)) * 8) * 2;
  auto gW = [&](u32 slot, u32 so) { const u32 d = ldsb + slot * SLOT + lw * 2048; bdma16(rw, dvw, so, d); bdma16(rw, dvw, so + 8 * DM * 2, d + 1024); };
  auto gH = [&](u32 pos, u32 so) { const u32 d = ldsb + (pos < 7 ? 12 + pos : pos - 7) * SLOT + lw * 2048; bdma16_dev(rH2, cvh, so, d); bdma16_dev(rH2, cvh, so + 1024, d + 1024); };
  const int hf0 = (mm >> 1) & 1;
  auto soW = [&](int u) { return (u32)((128 * (u >= 14 ? u - 14 : u) + 64 * ((u >= 14) ^ hf0)) * DM * 2); };
  auto soH = [&](int u) { return (u32)(u * 4096); };

  if (wv >= 4) {
    const int lk = lw & 1;
    issueW(3); issueW(4);
    if (lw >= 2) {
      {
        const rsrc_t rc = mkrsrc(ws + WS_CS + (lk * FF + 128 * nb) * 8);
        u32x4 cg;
        _Pragma("unroll 1") for (int spin = 0; spin < (1 << 16); ++spin) {
          cg = __builtin_amdgcn_raw_buffer_load_b128(rc, lane * 16, 0, 16);
          if (__all(cg[1] == tag && cg[3] == tag)) break;
          __builtin_amdgcn_s_sleep(2);
        }
        *(u32x2*)(lds + B_CS + lk * 512 + lane * 8) = (u32x2){cg[0], cg[2]};
      }
      gu32* f0 = (gu32*)(ws + WS_FLA) + (lk * 112 + (lane < 56 ? lane : 0)) * 32;
      _Pragma("unroll 1") for (int spin = 0; spin < (1 << 16); ++spin) {
        const u32 a_ = __hip_atomic_load(f0, RLX_AGENT), b_ = __hip_atomic_load(f0 + 56 * 32, RLX_AGENT);
        if (__all(a_ == tag && b_ == tag)) break;
        __builtin_amdgcn_s_sleep(2);
      }
      if (lane == 0) __hip_atomic_fetch_add((LAS u32*)(lds + B_CNT), 1u, __ATOMIC_RELAXED, __HIP_MEMORY_SCOPE_WORKGROUP);
    }
    _Pragma("unroll 1") for (int spin = 0; spin < (1 << 20); ++spin) {
      if (__hip_atomic_load((LAS u32*)(lds + B_CNT), __ATOMIC_RELAXED, __HIP_MEMORY_SCOPE_WORKGROUP) >= 2u) break;
      __builtin_amdgcn_s_sleep(1);
    }
    STAMP(0, 8);
    issueH(0); issueH(1); issueH(2); issueH(3); issueH(4); issueH(5); issueH(6); issueH(7); issueH(8); issueH(9);
    wait_vm<18>(); bar(); issueW(5); issueH(10);
    wait_vm<20>(); bar(); issueW(6); issueH(11);
    wait_vm<22>(); bar(); issueW(7); issueH(12);
    wait_vm<24>(); bar(); issueW(8); issueH(13);
    wait_vm<26>(); bar(); issueW(9);
    wait_vm<16>(); bar(); issueW(10);
    wait_vm<14>(); bar(); issueW(11);
    wait_vm<12>(); bar(); issueW(12);
    wait_vm<10>(); bar(); issueW(13);
    {
      u32 slot = 2, so = 13 * 32 * FF * 2 + ((m & 1) ^ 1) * 128;
#pragma unroll 1
      for (int t = 9; t <= 15; ++t) { wait_vm<8>(); bar(); issueWs(slot, so); slot = slot == 6 ? 0 : slot + 1; so -= 32 * FF * 2; }
    }
    wait_vm<8>(); bar(); issueW(21); issueH(1);
    wait_vm<10>(); bar(); issueW(22); issueH(0);
    wait_vm<12>(); bar(); issueW(23); gW(11, soW(0));
    wait_vm<14>(); bar(); issueW(24); gW(10, soW(1));
    wait_vm<16>(); bar(); issueW(25); gW(9, soW(2));
    wait_vm<18>(); bar(); issueW(26); gW(8, soW(3));
    wait_vm<18>(); bar(); issueW(27); gW(7, soW(4));
    wait_vm<18>(); bar(); gW(6, soW(5));
    wait_vm<16>(); bar(); gW(5, soW(6));
    wait_vm<14>(); bar(); wait_vm<10>(); bar(); wait_vm<6>(); bar();
    STAMP(0, 9);
    bar();
    {
      gu32* fl = (gu32*)(ws + WS_FLB) + (((mm >> 1) * 14 + (lane < 14 ? lane : 0)) * 2) * 32;
      _Pragma("unroll 1") for (int spin = 0; spin < (1 << 16); ++spin) {
        if (__all(__hip_atomic_load(fl, RLX_AGENT) == tag)) break;
        __builtin_amdgcn_s_sleep(2);
      }
    }
    STAMP(0, 11);
    wait_vm<0>();
#pragma unroll
    for (int u = 0; u < 10; ++u) gH(u, soH(u));
    wait_vm<18>(); bar(); gH(10, soH(10));
    wait_vm<18>(); bar(); gH(11, soH(11));
    wait_vm<18>(); bar(); gW(11, soW(7)); gH(0, soH(12));
    wait_vm<20>(); bar(); gW(10, soW(8)); gH(1, soH(13));
    wait_vm<22>(); bar(); gW(9, soW(9)); gH(2, soH(14));
    wait_vm<24>(); bar(); gW(8, soW(10)); gH(3, soH(15));
    wait_vm<26>(); bar(); gW(7, soW(11)); gH(4, soH(16));
    wait_vm<18>(); bar(); gW(6, soW(12)); gH(5, soH(17));
    wait_vm<18>(); bar(); gW(5, soW(13)); gH(6, soH(18));
    {
      u32 wslot = 11, hpos = 7, sw = soW(14), sh = soH(19);
#pragma unroll 1
      for (int u = 9; u <= 17; ++u) {
        wait_vm<18>(); bar(); gW(wslot, sw); gH(hpos, sh);
        wslot = wslot == 5 ? 11 : wslot - 1; hpos = hpos == 11 ? 0 : hpos + 1; sw += 128 * DM * 2; sh += 4096;
      }
      wait_vm<18>(); bar(); gW(wslot, sw); wslot = wslot == 5 ? 11 : wslot - 1; sw += 128 * DM * 2;
      wait_vm<16>(); bar(); gW(wslot, sw); wslot = wslot == 5 ? 11 : wslot - 1; sw += 128 * DM * 2;
      wait_vm<14>(); bar(); gW(wslot, sw); wslot = wslot == 5 ? 11 : wslot - 1; sw += 128 * DM * 2;
      wait_vm<12>(); bar(); gW(wslot, sw); wslot = wslot == 5 ? 11 : wslot - 1; sw += 128 * DM * 2;
      wait_vm<10>(); bar(); gW(wslot, sw);
    }
    wait_vm<8>(); bar(); wait_vm<6>(); bar(); wait_vm<4>(); bar(); wait_vm<2>(); bar(); wait_vm<0>(); bar();
    STAMP(0, 13);
    STAMP_FLUSH(0);
    asm volatile(".Lfused_end2:");
    return;
  }

  const int tp = wv & 1, ag = wv >> 1;
  const int grp = lane >> 4, fq = (lane & 15) >> 2, fp = lane & 3, fh = grp >> 1;
  int ao[2], bo[2];
  {
    const int akey = (((fq >> 1) & 1) << 2) ^ ((grp & 1) << 1);
#pragma unroll
    for (int i = 0; i < 2; ++i) ao[i] = P_W + (grp & 1) * 4096 + (8 * fh + fq) * 128 + (((2 * (2 * ag + i) + (fp >> 1)) ^ akey) * 16) + (fp & 1) * 8;
#pragma unroll
    for (int t = 0; t < 2; ++t)
      bo[t] = (8 * fh + fq) * 256 + (((4 * (2 * tp + t) + 2 * (grp & 1) + (fp >> 1)) ^ (fq << 2)) * 16) + (fp & 1) * 8;
  }
  f32x16 accs[2][2][2];
  struct KF { s16x8 a[2][2], b[2][2]; };
  KF X, Y;
  const rsrc_t rH = mkrsrc(ws + WS_H);
  const int hh = lane >> 5;
  f32x2 st2[2];
  auto epilogue = [&](auto pass_) {
    constexpr int p_ = decltype(pass_)::value;
    auto& acc = accs[p_];
    const float* cs = (const float*)(lds + B_CS);
#pragma unroll
    for (int i = 0; i < 2; ++i) {
      const int tile = 4 * (p_ ^ (m & 1)) + 2 * ag + i, f0 = 16 * tile + 4 * hh;
      const f32x4 g0 = *(const f32x4*)(cs + f0), g1 = *(const f32x4*)(cs + f0 + 8);
      const f32x4 u0 = *(const f32x4*)(cs + 128 + f0), u1 = *(const f32x4*)(cs + 128 + f0 + 8);
#pragma unroll
      for (int t = 0; t < 2; ++t) {
        const f32x2 nm = {st2[t][0], st2[t][0]}, r2 = {st2[t][1], st2[t][1]};
        u32x4 o;
#pragma unroll
        for (int r = 0; r < 4; ++r) {
          const f32x4 cg = r < 2 ? g0 : g1, cu = r < 2 ? u0 : u1;
          const int e = 2 * (r & 1);
          const f32x2 g = __builtin_elementwise_fma(nm, (f32x2){cg[e], cg[e + 1]}, (f32x2){acc[i][t][2 * r], acc[i][t][2 * r + 1]});
          const f32x2 u = __builtin_elementwise_fma(nm, (f32x2){cu[e], cu[e + 1]}, (f32x2){acc[i][t][8 + 2 * r], acc[i][t][8 + 2 * r + 1]});
          const f32x2 hv = (g * r2) * u;
          o[r] = pk_bf16(hv[0], hv[1]);
        }
        __builtin_amdgcn_raw_buffer_store_b128(o, rH, (((4 * m + 2 * tp + t) * 112 + 56 * p_ + 4 * nb + 2 * ag + i) * 64 + lane) * 16, 0, 16);
      }
    }
  };
  auto publish = [&](int p_) {
    asm volatile("s_waitcnt vmcnt(0)" ::: "memory");
    u32 old = 0;
    if (lane == 0) old = __hip_atomic_fetch_add((LAS u32*)(lds + CNT0) + p_, 1u, __ATOMIC_RELAXED, __HIP_MEMORY_SCOPE_WORKGROUP);
    if (lane == 0 && old == 3u) __hip_atomic_store((gu32*)(ws + WS_FLB) + ((m * 14 + nb) * 2 + p_) * 32, tag, RLX_AGENT);
  };
  auto mm8 = [&](auto pass_, const KF& f) {
    auto& acc = accs[decltype(pass_)::value];
#pragma unroll
    for (int g = 0; g < 8; ++g) {
      const int ks = g >> 2, i = (g >> 1) & 1, t = g & 1;
      acc[i][t] = __builtin_amdgcn_mfma_f32_32x32x16_bf16(__builtin_bit_cast(bf16x8, f.a[ks][i]), __builtin_bit_cast(bf16x8, f.b[ks][t]), acc[i][t], 0, 0, 0);
    }
  };
  int hsl = 0, wsl = 2;
  typedef LAS const char* lptr;
  auto trf = [&](u32 a0, int off) {
    s16x4 x0 = __builtin_amdgcn_ds_read_tr16_b64_v4i16((LAS s16x4*)(a0 + off));
    return x0;
  };
  auto stage = [&](auto pass_, auto first, KF& cur, const KF& prev) {
    auto& acc = accs[decltype(pass_)::value];
    u32 hb0 = ldsb + hsl * SLOT + bo[0], hb1 = ldsb + hsl * SLOT + bo[1], wa0 = ldsb + wsl * SLOT + ao[0], wa1 = ldsb + wsl * SLOT + ao[1];
    asm volatile("" : "+v"(hb0), "+v"(hb1), "+v"(wa0), "+v"(wa1));
    auto rdb = [&](int rk, int rt_) {
      const u32 a0 = rt_ ? hb1 : hb0;
      cur.b[rk][rt_] = __builtin_shufflevector(trf(a0, rk * 16 * 256), trf(a0, (rk * 16 + 4) * 256), 0, 1, 2, 3, 4, 5, 6, 7);
    };
    auto rda = [&](int rk, int rt_) {
      const u32 a0 = rt_ ? wa1 : wa0;
      cur.a[rk][rt_] = __builtin_shufflevector(trf(a0, rk * 16 * 128), trf(a0, (rk * 16 + 4) * 128), 0, 1, 2, 3, 4, 5, 6, 7);
    };
#pragma unroll
    for (int g = 0; g < 8; ++g) {
      const int ks = g >> 2, i = (g >> 1) & 1, t = g & 1;
      if (!decltype(first)::value)
        acc[i][t] = __builtin_amdgcn_mfma_f32_32x32x16_bf16(__builtin_bit_cast(bf16x8, prev.a[ks][i]), __builtin_bit_cast(bf16x8, prev.b[ks][t]), acc[i][t], 0, 0, 0);
      if (g == 0) { FENCE(); bar(); }
      if (g == 1) { rdb(0, 0); rda(0, 0); }
      if (g == 2) { rdb(0, 1); }
      if (g == 3) { rda(0, 1); }
      if (g == 4) { rdb(1, 0); }
      if (g == 5) { rda(1, 0); }
      if (g == 6) { rdb(1, 1); }
      if (g == 7) { rda(1, 1); }
      FENCE();
    }
    wsl = wsl == 6 ? 0 : wsl + 1;
  };
#pragma unroll
  for (int g = 0; g < 8; ++g) accs[g >> 2][(g >> 1) & 1][g & 1] = (f32x16){};
  stage(IC<0>{}, IC<1>{}, X, Y); hsl = 1;
  STAMP(0, 8);
  stage(IC<0>{}, IC<0>{}, Y, X); hsl = 2;
#pragma unroll 1
  for (int t = 2; t < 14; t += 2) {
    stage(IC<0>{}, IC<0>{}, X, Y); hsl = hsl == 11 ? 0 : hsl + 1;
    stage(IC<0>{}, IC<0>{}, Y, X); hsl = hsl == 11 ? 0 : hsl + 1;
  }
  STAMP(0, 15);
  const rsrc_t rg2 = mkrsrc(ws + WS_GRAN2 + (size_t)(128 * m + 64 * tp) * 8);
  u32x2 sg[2];
#pragma unroll
  for (int t = 0; t < 2; ++t) sg[t] = __builtin_amdgcn_raw_buffer_load_b64(rg2, (32 * t + (lane & 31)) * 8, 0, 16);
  mm8(IC<0>{}, Y);
  hsl = 1;
  stage(IC<1>{}, IC<1>{}, X, Y); hsl = 0;
  stage(IC<1>{}, IC<0>{}, Y, X); hsl = 11;
  stage(IC<1>{}, IC<0>{}, X, Y); hsl = 10;
  for (int spin = 0; spin < (1 << 16); ++spin) {
    if (__all(sg[0][1] == tag && sg[1][1] == tag)) break;
    __builtin_amdgcn_s_sleep(2);
#pragma unroll
    for (int t = 0; t < 2; ++t) sg[t] = __builtin_amdgcn_raw_buffer_load_b64(rg2, (32 * t + (lane & 31)) * 8, 0, 16);
  }
#pragma unroll
  for (int t = 0; t < 2; ++t) { const float rs = __builtin_amdgcn_rcpf(bfhi(sg[t][0])); st2[t] = (f32x2){-bf2f(sg[t][0] & 0xffffu), rs * rs}; }
  STAMP(0, 11);
  epilogue(IC<0>{});
  STAMP(0, 10);
  stage(IC<1>{}, IC<0>{}, Y, X); hsl = 9;
  stage(IC<1>{}, IC<0>{}, X, Y); hsl = 8;
  stage(IC<1>{}, IC<0>{}, Y, X); hsl = 7;
  publish(0);
#pragma unroll 1
  for (int t = 20; t < 28; t += 2) {
    stage(IC<1>{}, IC<0>{}, X, Y); hsl = hsl - 1;
    stage(IC<1>{}, IC<0>{}, Y, X); hsl = hsl - 1;
  }
  mm8(IC<1>{}, Y);
  STAMP(0, 9);
  bar();
  epilogue(IC<1>{});
  publish(1);
  const int dtl = wv & 1, tt = wv >> 1;
  const int tok = 64 * mm + 32 * tt + (lane & 31);
  const rsrc_t rres = mkrsrc(ws + WS_HT);
  u32 res[16];
#pragma unroll
  for (int i = 0; i < 16; ++i) res[i] = __builtin_amdgcn_raw_buffer_load_b16(rres, ((64 * nn + 32 * dtl + 8 * (i >> 2) + 4 * hh + (i & 3)) * HS + tok) * 2, 0, 16);
  const int achunk = 4 * dtl + 2 * (grp & 1) + (fp >> 1);
  const int caoff = (4 * fh + fq) * 128 + ((achunk ^ (((fq >> 1) & 1) << 2)) * 16) + (fp & 1) * 8;
  const int cboff = tt * 4096 + lane * 16;
  f32x16 cacc = {};
  struct F4 { s16x8 a[4]; bf16x8 b[4]; };
  F4 Xc, Yc;
  int cwb = 11, chv = 0;
  auto cstage = [&](auto first, F4& cur, const F4& prev) {
    bar();
    const char* sth = lds + (chv < 7 ? 12 + chv : chv - 7) * SLOT;
    const char* stw = lds + cwb * SLOT;
#pragma unroll
    for (int k = 0; k < 4; ++k) {
      if (!decltype(first)::value) cacc = __builtin_amdgcn_mfma_f32_32x32x16_bf16(__builtin_bit_cast(bf16x8, prev.a[k]), prev.b[k], cacc, 0, 0, 0);
      cur.a[k] = tr_frag(stw + caoff + k * 16 * 128, stw + caoff + (k * 16 + 8) * 128);
      cur.b[k] = *(const bf16x8*)(sth + cboff + k * 1024);
      FENCE();
    }
    chv = chv == 11 ? 0 : chv + 1; cwb = cwb == 5 ? 11 : cwb - 1;
  };
  cstage(IC<1>{}, Xc, Yc);
  STAMP(0, 12);
  cstage(IC<0>{}, Yc, Xc);
  cstage(IC<0>{}, Xc, Yc);
  cstage(IC<0>{}, Yc, Xc);
  if (wv == 0) {
    gu32* fl = (gu32*)(ws + WS_FLB) + (((mm >> 1) * 14 + (lane < 14 ? lane : 0)) * 2 + 1) * 32;
    for (int spin = 0; spin < (1 << 16); ++spin) {
      if (__all(__hip_atomic_load(fl, RLX_AGENT) == tag)) break;
      __builtin_amdgcn_s_sleep(2);
    }
  }
#pragma unroll 1
  for (int u = 4; u < 28; u += 2) { cstage(IC<0>{}, Xc, Yc); cstage(IC<0>{}, Yc, Xc); }
#pragma unroll
  for (int k = 0; k < 4; ++k) cacc = __builtin_amdgcn_mfma_f32_32x32x16_bf16(__builtin_bit_cast(bf16x8, Yc.a[k]), Yc.b[k], cacc, 0, 0, 0);
  STAMP(0, 13);
  u16* op = out + tok * DM + 64 * nn + 32 * dtl + 4 * hh;
#pragma unroll
  for (int g = 0; g < 4; ++g)
    *(u32x2*)(op + 8 * g) = (u32x2){pk_bf16(bf2f(res[4 * g]) + rbf(cacc[4 * g]), bf2f(res[4 * g + 1]) + rbf(cacc[4 * g + 1])),
                                    pk_bf16(bf2f(res[4 * g + 2]) + rbf(cacc[4 * g + 2]), bf2f(res[4 * g + 3]) + rbf(cacc[4 * g + 3]))};
  STAMP(0, 14);
  STAMP_FLUSH(0);
  asm volatile(".Lfused_end:");
}

extern "C" void kernel_launch(void* const* d_in, const int* in_sizes, int n_in, void* d_out, int out_size, void* d_ws, size_t ws_size,
                              hipStream_t stream) {
  (void)in_sizes; (void)n_in; (void)out_size; (void)ws_size;
  (void)hipFuncSetAttribute((const void*)fused_kernel, hipFuncAttributeMaxDynamicSharedMemorySize, K_LDS);
  fused_kernel<<<256, 512, K_LDS, stream>>>((const u16*)d_in[0], (const u16*)d_in[1], (const u16*)d_in[2], (const u16*)d_in[3], (const u16*)d_in[4],
                                            (const u16*)d_in[5], (const u16*)d_in[6], (const u16*)d_in[7], (const u16*)d_in[8],
                                            (const u16*)d_in[10], (const u16*)d_in[11], (const u16*)d_in[12], (char*)d_ws, (u16*)d_out);
}
```

```cpp
#include <hip/hip_runtime.h>
#include <stdint.h>

typedef unsigned short u16;
typedef unsigned int u32;
typedef unsigned long long u64;
typedef float f32x2 __attribute__((ext_vector_type(2)));
typedef float f32x4 __attribute__((ext_vector_type(4)));
typedef float f32x16 __attribute__((ext_vector_type(16)));
typedef unsigned u32x2 __attribute__((ext_vector_type(2)));
typedef unsigned u32x4 __attribute__((ext_vector_type(4)));
typedef short s16x4 __attribute__((ext_vector_type(4)));
typedef short s16x8 __attribute__((ext_vector_type(8)));
typedef __bf16 bf16x8 __attribute__((ext_vector_type(8)));
typedef __bf16 bf16x2 __attribute__((ext_vector_type(2)));
typedef _Float16 f16x8 __attribute__((ext_vector_type(8)));
typedef _Float16 f16x4 __attribute__((ext_vector_type(4)));
typedef _Float16 f16x2 __attribute__((ext_vector_type(2)));
#define DI __device__ __forceinline__
#define LAS __attribute__((address_space(3)))
#define GAS __attribute__((address_space(1)))
typedef GAS unsigned gu32;
typedef GAS unsigned long long gu64;
#define RLX_AGENT __ATOMIC_RELAXED, __HIP_MEMORY_SCOPE_AGENT
#define FENCE() __builtin_amdgcn_sched_barrier(0)

constexpr int SL = 2048, DM = 448, NS = 256, FF = 1792;
constexpr int HS = SL + 64;

constexpr size_t WS_EPOCH = 0;
constexpr size_t WS_GRAN  = 4096;
constexpr size_t WS_CS    = 32768;
constexpr size_t WS_FLA   = 65536;
constexpr size_t WS_FLB   = WS_FLA + 32768;
constexpr size_t WS_GRAN2 = 163840;
constexpr size_t WS_XG    = 196608;
constexpr size_t WS_HT    = WS_XG + (size_t)DM * 8192;
constexpr size_t WS_H     = WS_HT + (size_t)DM * HS * 2;


DI float bf2f(u32 v) { return __uint_as_float(v << 16); }
DI float bfhi(u32 v) { return __uint_as_float(v & 0xffff0000u); }
DI u32 pk_bf16(float a, float b) { f32x2 v = {a, b}; return __builtin_bit_cast(u32, __builtin_convertvector(v, bf16x2)); }
DI u32 pk_f16(float a, float b) { f32x2 v = {a, b}; return __builtin_bit_cast(u32, __builtin_convertvector(v, f16x2)); }
DI u32 pk2(f32x2 v) { return __builtin_bit_cast(u32, __builtin_convertvector(v, f16x2)); }
DI float rbf(float a) { return __uint_as_float(pk_bf16(a, a) << 16); }

template <int CTRL, int RM> DI float dpp0(float v) { return __int_as_float(__builtin_amdgcn_update_dpp(0, __float_as_int(v), CTRL, RM, 0xf, true)); }
DI float lane63(float v) { return __int_as_float(__builtin_amdgcn_readlane(__float_as_int(v), 63)); }
DI void wave_sum2(float& a, float& b) {
  a += dpp0<0x111, 0xf>(a); b += dpp0<0x111, 0xf>(b); a += dpp0<0x112, 0xf>(a); b += dpp0<0x112, 0xf>(b);
  a += dpp0<0x114, 0xf>(a); b += dpp0<0x114, 0xf>(b); a += dpp0<0x118, 0xf>(a); b += dpp0<0x118, 0xf>(b);
  a += dpp0<0x142, 0xa>(a); b += dpp0<0x142, 0xa>(b); a += dpp0<0x143, 0xc>(a); b += dpp0<0x143, 0xc>(b);
  a = lane63(a); b = lane63(b);
}
DI void wave_max_sum(float& m, float& s) {
  m = fmaxf(m, dpp0<0x111, 0xf>(m)); s += dpp0<0x111, 0xf>(s); m = fmaxf(m, dpp0<0x112, 0xf>(m)); s += dpp0<0x112, 0xf>(s);
  m = fmaxf(m, dpp0<0x114, 0xf>(m)); s += dpp0<0x114, 0xf>(s); m = fmaxf(m, dpp0<0x118, 0xf>(m)); s += dpp0<0x118, 0xf>(s);
  m = fmaxf(m, dpp0<0x142, 0xa>(m)); s += dpp0<0x142, 0xa>(s); m = fmaxf(m, dpp0<0x143, 0xc>(m)); s += dpp0<0x143, 0xc>(s);
  m = lane63(m); s = lane63(s);
}

typedef __amdgpu_buffer_rsrc_t rsrc_t;
DI rsrc_t mkrsrc(const void* p) { return __builtin_amdgcn_make_buffer_rsrc((void*)p, 0, 0x7fffffff, 0x00020000); }
DI void bdma16(rsrc_t rs, u32 voff, u32 soff, u32 lds_dst) {
  asm volatile("s_mov_b32 m0, %0\n\ts_nop 0\n\tbuffer_load_dwordx4 %1, %2, %3 offen lds" :: "s"(lds_dst), "v"(voff), "s"(rs), "s"(soff) : "memory");
}
DI void bdma16_dev(rsrc_t rs, u32 voff, u32 soff, u32 lds_dst) {
  asm volatile("s_mov_b32 m0, %0\n\ts_nop 0\n\tbuffer_load_dwordx4 %1, %2, %3 offen sc1 lds" :: "s"(lds_dst), "v"(voff), "s"(rs), "s"(soff) : "memory");
}
DI u32 lds_addr(const void* p) { return (u32)(size_t)(LAS const char*)p; }
DI s16x8 tr_frag(const char* p0, const char* p1) {
  s16x4 a = __builtin_amdgcn_ds_read_tr16_b64_v4i16((LAS s16x4*)p0);
  s16x4 b = __builtin_amdgcn_ds_read_tr16_b64_v4i16((LAS s16x4*)p1);
  return __builtin_shufflevector(a, b, 0, 1, 2, 3, 4, 5, 6, 7);
}

struct IdxTab { u16 v[2064]; };
constexpr int tap_entry(int l) {
  if (l < 0) return 21 * 32;
  int lam = l;
  if (l >= 256) {
    int sh = l >= 1024 ? 3 : (l >= 512 ? 2 : 1);
    int q = l >> sh, rem = l & ((1 << sh) - 1), half = 1 << (sh - 1);
    if (rem > half || (rem == half && (q & 1))) q += 1;
    lam = q << sh;
  }
  int hq = lam >> 8, lo = lam & 255, c = lo >> 3, r = 0;
  if (hq == 0) r = lo & 7;
  else if (hq == 1) r = 8 + ((lo & 7) >> 1);
  else if (hq == 2) r = 12 + ((lo & 7) >> 2);
  else if (hq == 3) r = 14 + ((lo & 7) >> 2);
  else if (hq < 8) r = 16 + hq - 4;
  else { r = 20; c = 0; }
  return r * 32 + c;
}
constexpr IdxTab make_tab() { IdxTab t{}; for (int y = 0; y < 2064; ++y) t.v[y] = (u16)(2 * tap_entry(2047 - y)); return t; }
__device__ const IdxTab RIDX __attribute__((aligned(16))) = make_tab();

DI float pow2_for(float mx, int target) {
  int e = (int)(__float_as_uint(mx) >> 23);
  int be = 127 + target - (e - 126);
  be = be > 254 ? 254 : (be < 1 ? 1 : be);
  return __uint_as_float((u32)be << 23);
}
DI float pow2_inv(float p) { return __uint_as_float((254u - (__float_as_uint(p) >> 23)) << 23); }

struct CM { f32x2 a, b; };
DI CM mk(f32x2 e) { return {e, (f32x2){-e.y, e.x}}; }
DI f32x2 cmul(f32x2 p, CM m) {
  f32x2 t = __builtin_shufflevector(p, p, 0, 0) * m.a;
  return __builtin_elementwise_fma(__builtin_shufflevector(p, p, 1, 1), m.b, t);
}
DI f32x2 cexp_s(float zr, float zi, float s) {
  float m = __builtin_amdgcn_exp2f(zr * (s * 1.44269504088896341f));
  float a = __builtin_amdgcn_fractf(zi * (s * 0.15915494309189535f));
  return (f32x2){m * __builtin_amdgcn_cosf(a), m * __builtin_amdgcn_sinf(a)};
}
template <int OFF>
DI void put4(u32 base, u32 v0, u32 v1, u32 v2, u32 v3) {
  asm volatile("s_mov_b32 m0, %0\n\ts_nop 0\n\tds_write_addtid_b32 %1 offset:%5\n\tds_write_addtid_b32 %2 offset:%6\n\t"
               "ds_write_addtid_b32 %3 offset:%7\n\tds_write_addtid_b32 %4 offset:%8"
               :: "s"(base), "v"(v0), "v"(v1), "v"(v2), "v"(v3), "n"(OFF), "n"(OFF + 272), "n"(OFF + 544), "n"(OFF + 816) : "memory");
}

template <int V> struct IC { static constexpr int value = V; };
template <int N> DI void wait_vm() { asm volatile("s_waitcnt vmcnt(%0)" :: "n"(N) : "memory"); }
DI void bar() { __builtin_amdgcn_s_barrier(); }


constexpr int A_T1 = 0, A_T2 = 8192;
constexpr int A_SCR = 25088, A_SCRW = 32 * 272 + 21 * 272;
constexpr int A_CP = 0, A_CPS = 4160, A_CPC = 4 * A_CPS;
constexpr int A_EX = 66560, A_EXW = 64 * 144, A_XX = A_EX + 4 * A_EXW;
constexpr int A_UU = A_SCR + 8 * A_SCRW, A_UUC = 95 * 80;
constexpr int A_TB = A_UU + 2 * A_UUC, A_TBC = 22 * 32 * 2;
constexpr int A_SW = A_TB + 2 * A_TBC;
constexpr int A_LDS = A_SW + 64;
static_assert(A_LDS <= 163840 && A_XX + 10240 <= A_UU && (A_SCR % 16) == 0 && (A_SCRW % 16) == 0 && (A_UU % 16) == 0, "lds");

constexpr int SLOT = 8192, P_W = 12 * SLOT;
constexpr int B_CS = 19 * SLOT, B_CNT = 163840 - 16, CNT0 = 163840 - 12, CNT1 = 163840 - 8;

constexpr int K_LDS = 163840;
constexpr int CODE_WARM_KB = 64;
static_assert(A_LDS <= K_LDS - 16, "lds");

__global__ __launch_bounds__(512, 2) void fused_kernel(const u16* __restrict__ x, const u16* __restrict__ Are, const u16* __restrict__ Aim,
    const u16* __restrict__ Bre, const u16* __restrict__ Bim, const u16* __restrict__ Cre, const u16* __restrict__ Cim,
    const u16* __restrict__ logdt, const u16* __restrict__ Dskip, const u16* __restrict__ wg, const u16* __restrict__ wu,
    const u16* __restrict__ wd, char* ws, u16* __restrict__ out) {
  extern __shared__ __attribute__((aligned(16))) char lds[];
  const int tid = threadIdx.x, lane = tid & 63;
  const int wv = __builtin_amdgcn_readfirstlane(tid >> 6);
  const int b = blockIdx.x, xcd = b & 7, j = b >> 3;
  const bool worker = j < 28;
  u32x4 cwarm[CODE_WARM_KB / 8];
  if (!worker) {
      u64 pc; u32 len, len2;
      asm volatile("s_getpc_b64 %0\n.Lfused_pc:\n\ts_mov_b32 %1, .Lfused_end-.Lfused_pc\n\ts_mov_b32 %2, .Lfused_end2-.Lfused_pc" : "=s"(pc), "=s"(len), "=s"(len2));
      len = (int)len2 > (int)len ? len2 : len;
      const char* code = (const char*)pc;
#pragma unroll
      for (int i = 0; i < CODE_WARM_KB / 8; ++i) {
        const u32 o = (u32)(i * 512 + tid) * 16u;
        cwarm[i] = *(const GAS u32x4*)(code + (((int)len > 16 && o + 16u <= len) ? o : 0u));
      }
  }
  asm volatile("" :: "s"(x), "s"(Are), "s"(Aim), "s"(Bre), "s"(Bim), "s"(Cre), "s"(Cim), "s"(logdt), "s"(Dskip), "s"(wg), "s"(wu), "s"(wd), "s"(ws), "s"(out));
  gu32* epoch_p = (gu32*)(ws + WS_EPOCH);
  gu64* gran = (gu64*)(ws + WS_GRAN);

  const int w = worker ? xcd * 28 + j : 0, c0 = 2 * w;
  const int trow = 8 * b + wv;
  const u32x4 xr = *(const u32x4*)(x + trow * DM + (lane < 56 ? lane : 0) * 8);
  const u32 tag = __hip_atomic_load(epoch_p, RLX_AGENT) + 1u;
  const int ch = tid >> 8, n = tid & 255;
  const int idx = (c0 + ch) * NS + n;
  const u32 p_ldt = logdt[c0 + ch];
  const u32 p_are = Are[idx], p_aim = Aim[idx];
  const u32x4 xt = *(const u32x4*)(x + (128 * (w & 15) + (tid >> 2)) * DM + 32 * (w >> 4) + (tid & 3) * 8);
  const u32 p_bre = Bre[idx], p_bim = Bim[idx], p_cre = Cre[idx], p_cim = Cim[idx];
  const u32 p_d01 = *(const u32*)(Dskip + c0);
  const u32x4 ix0 = *(const u32x4*)(RIDX.v + 8 * (tid & 255)), ix1 = *(const u32x4*)(RIDX.v + 8 * (tid & 255) + 8);
  if (tid == 0) { *(u32*)(lds + B_CNT) = 0u; *(u32*)(lds + CNT0) = 0u; *(u32*)(lds + CNT1) = 0u; }
  if (tid < 310) *(u32x4*)(lds + A_UU + (tid >= 155 ? A_UUC - 155 * 16 : 0) + tid * 16) = (u32x4){0, 0, 0, 0};
  if (tid >= 504) *(u32x4*)(lds + A_TB + ((tid >> 2) & 1) * A_TBC + 21 * 64 + (tid & 3) * 16) = (u32x4){0, 0, 0, 0};
  {
    const float piv = __uint_as_float((u32)__builtin_amdgcn_readfirstlane(xr[0]) << 16);
    float s = 0.f, q = 0.f, s2 = 0.f, q2 = 0.f;
#pragma unroll
    for (int i = 0; i < 4; ++i) {
      const float lo = bf2f(xr[i] & 0xffffu) - piv, hi = bfhi(xr[i]) - piv;
      s += lo; s2 += hi; q = fmaf(lo, lo, q); q2 = fmaf(hi, hi, q2);
    }
    s = lane < 56 ? s + s2 : 0.f; q = lane < 56 ? q + q2 : 0.f;
    wave_sum2(s, q);
    const float ms = s * (1.f / DM);
    float var = q * (1.f / DM) - ms * ms; var = var < 0.f ? 0.f : var;
    const float sd = rbf(__builtin_sqrtf(rbf(rbf(var) + 1e-5f)));
    if (lane == 0) __hip_atomic_store(gran + trow, ((u64)tag << 32) | pk_bf16(piv + ms, sd), RLX_AGENT);
  }
  if (!worker) {
    {
      u32 acc = 0;
#pragma unroll
      for (int i = 0; i < CODE_WARM_KB / 8; ++i) acc |= cwarm[i][0] & cwarm[i][1] & cwarm[i][2] & cwarm[i][3];
      asm volatile("" :: "v"(acc));
    }
    const int s = xcd * 4 + (j - 28);
    const u16* W = (s >> 4) ? wu : wg;
    const int colb = (s & 15) * 112;
    float a[8] = {0, 0, 0, 0, 0, 0, 0, 0};
    const int cc = tid % 14, rg = tid / 14;
    u32 warm = 0;
    {
      u32x4 wv4[7];
#pragma unroll
      for (int i = 0; i < 7; ++i) { const int c = tid + 512 * i; wv4[i] = *(const u32x4*)(wd + (size_t)s * 25088 + (c < 3136 ? c : 0) * 8); }
#pragma unroll
      for (int i = 0; i < 7; ++i) warm |= wv4[i][0] & wv4[i][1] & wv4[i][2] & wv4[i][3];
      asm volatile("" :: "v"(warm));
    }
    if (tid < 448) {
      u32x4 v[14];
#pragma unroll
      for (int r = 0; r < 14; ++r) v[r] = *(const u32x4*)(W + (rg * 14 + r) * FF + colb + cc * 8);
#pragma unroll
      for (int r = 0; r < 14; ++r)
#pragma unroll
        for (int i = 0; i < 4; ++i) { a[2 * i] += bf2f(v[r][i] & 0xffffu); a[2 * i + 1] += bfhi(v[r][i]); }
      float* red = (float*)lds;
#pragma unroll
      for (int i = 0; i < 8; ++i) red[rg * 112 + cc * 8 + i] = a[i];
    }
    __syncthreads();
    if (tid < 112) {
      float t = 0.f;
      for (int r = 0; r < 32; ++r) t += ((float*)lds)[r * 112 + tid];
      __hip_atomic_store((gu64*)(ws + WS_CS) + (s >> 4) * FF + colb + tid, ((u64)tag << 32) | __float_as_uint(t), RLX_AGENT);
    }
    if (wv < 4) {
      gu32* fl = (gu32*)(ws + WS_FLA) + (wv * 56 + (lane < 56 ? lane : 0)) * 32;
      _Pragma("unroll 1") for (int spin = 0; spin < (1 << 16); ++spin) {
        if (__all(__hip_atomic_load(fl, RLX_AGENT) == tag)) break;
        __builtin_amdgcn_s_sleep(2);
      }
    }
    __syncthreads();
    {
      const rsrc_t rh2 = mkrsrc((const u16*)(ws + WS_HT) + 64 * s);
      u32x4 hv[7];
#pragma unroll
      for (int i = 0; i < 7; ++i) hv[i] = __builtin_amdgcn_raw_buffer_load_b128(rh2, (((tid >> 3) + 64 * i) * HS + (tid & 7) * 8) * 2, 0, 16);
      if (tid < 8) *(u32x4*)(lds + 40960 + tid * 16) = hv[0];
      __syncthreads();
      const u32x4 pw = *(const u32x4*)(lds + 40960 + (tid & 7) * 16);
      f32x2 sa[4], sq[4];
#pragma unroll
      for (int k = 0; k < 4; ++k) { sa[k] = (f32x2){0.f, 0.f}; sq[k] = (f32x2){0.f, 0.f}; }
#pragma unroll
      for (int i = 0; i < 7; ++i)
#pragma unroll
        for (int k = 0; k < 4; ++k) {
          const u32 wi = hv[i][k], pi = pw[k];
          const f32x2 v = (f32x2){bf2f(wi & 0xffffu), bfhi(wi)} - (f32x2){bf2f(pi & 0xffffu), bfhi(pi)};
          sa[k] += v; sq[k] = __builtin_elementwise_fma(v, v, sq[k]);
        }
      f32x4* r1 = (f32x4*)(lds + (tid >> 3) * 528 + (tid & 7) * 64);
#pragma unroll
      for (int k = 0; k < 4; ++k) r1[k] = (f32x4){sa[k][0], sq[k][0], sa[k][1], sq[k][1]};
      __syncthreads();
      f32x2 t2 = {0.f, 0.f};
#pragma unroll
      for (int i = 0; i < 8; ++i) t2 += *(const f32x2*)(lds + ((tid >> 6) * 8 + i) * 528 + (tid & 63) * 8);
      *(f32x2*)(lds + 34816 + (tid >> 6) * 512 + (tid & 63) * 8) = t2;
      __syncthreads();
      if (tid < 64) {
        f32x2 tot = {0.f, 0.f};
#pragma unroll
        for (int i = 0; i < 8; ++i) tot += *(const f32x2*)(lds + 34816 + i * 512 + tid * 8);
        const u32 pword = *(const u32*)(lds + 40960 + (tid >> 1) * 4);
        const float piv2 = (tid & 1) ? bfhi(pword) : bf2f(pword & 0xffffu);
        const float ms = tot[0] * (1.f / DM), mean = piv2 + ms;
        float var = tot[1] * (1.f / DM) - ms * ms; var = var < 0.f ? 0.f : var;
        const float sd = rbf(__builtin_sqrtf(rbf(rbf(var) + 1e-5f)));
        __hip_atomic_store((gu64*)(ws + WS_GRAN2) + 64 * s + tid, ((u64)tag << 32) | pk_bf16(mean, sd), RLX_AGENT);
      }
    }
    return;
  }
  *(u32x4*)(lds + A_T1 + tid * 16) = xt;
  {
    const int grp = lane >> 4, q = (lane & 15) >> 2, p = lane & 3;
#pragma unroll
    for (int i = 0; i < 2; ++i) {
      const int tb = 16 * wv + 8 * i + 4 * (grp >> 1);
      const s16x4 v = __builtin_amdgcn_ds_read_tr16_b64_v4i16((LAS s16x4*)(lds + A_T1 + (tb + q) * 64 + (grp & 1) * 32 + p * 8));
      const u32x2 vw = __builtin_bit_cast(u32x2, v);
      *(u32x4*)(lds + A_T2 + (16 * (grp & 1) + (lane & 15)) * 528 + tb * 4) = (u32x4){vw[0], tag, vw[1], tag};
    }
  }
  asm volatile("s_waitcnt lgkmcnt(0)" ::: "memory");
  bar();
  {
    __amdgpu_buffer_rsrc_t rs = __builtin_amdgcn_make_buffer_rsrc((void*)(ws + WS_XG), 0, DM * 8192, 0x00020000);
#pragma unroll
    for (int k = 0; k < 2; ++k) {
      const int id = tid + 512 * k, c = id >> 5, pos = id & 31;
      const u32x4 v = *(const u32x4*)(lds + A_T2 + c * 528 + pos * 16);
      __builtin_amdgcn_raw_buffer_store_b128(v, rs, (32 * (w >> 4) + c) * 8192 + (w & 15) * 512 + pos * 16, 0, 16);
    }
  }
  const float dt = rbf(__builtin_amdgcn_exp2f(bf2f(p_ldt) * 1.44269504088896341f));
  const float zr = dt * bf2f(p_are), zi = dt * bf2f(p_aim);
  const f32x2 E8 = cexp_s(zr, zi, 8.f);
  const f32x2 E1 = cexp_s(zr, -zi, 1.f), F = cexp_s(zr, -zi, 256.f);
  const u32 scb = lds_addr(lds) + A_SCR + wv * A_SCRW;
  const CM M8 = mk(E8);
  f32x2 p0 = {16384.f, 0.f}, p1 = cmul(p0, M8), p2 = cmul(p1, M8), p3 = cmul(p2, M8);
  const CM M32 = mk(cmul(p2, mk(p2 * (1.f / 16384.f))) * (1.f / 16384.f));
  const __amdgpu_buffer_rsrc_t rs_g = __builtin_amdgcn_make_buffer_rsrc((void*)(ws + WS_GRAN), 0, SL * 8, 0x00020000);
  const __amdgpu_buffer_rsrc_t rs_x = __builtin_amdgcn_make_buffer_rsrc((void*)(ws + WS_XG + (size_t)c0 * 8192), 0, 2 * 8192, 0x00020000);
  u32x4 g[4], xg[4];
  const int t8 = tid & 255;
  auto fetch = [&]() {
#pragma unroll
    for (int i = 0; i < 4; ++i) g[i] = __builtin_amdgcn_raw_buffer_load_b128(rs_g, t8 * 64 + i * 16, 0, 16);
#pragma unroll
    for (int i = 0; i < 4; ++i) xg[i] = __builtin_amdgcn_raw_buffer_load_b128(rs_x, (i >> 1) * 8192 + t8 * 32 + (i & 1) * 16, 0, 16);
  };
  {
    const float br = dt * bf2f(p_bre), bi = dt * bf2f(p_bim);
    const float cr = bf2f(p_cre), ci = bf2f(p_cim);
    const float CBr = cr * br - ci * bi, CBi = cr * bi + ci * br;
    float mx = fmaxf(fabsf(CBr), fabsf(CBi)), sabs = fabsf(CBr) + fabsf(CBi);
    wave_max_sum(mx, sabs);
    const float scA = pow2_for(mx, 14);
    if (lane == 0) { ((float*)(lds + A_SW))[wv] = pow2_inv(scA) * (1.f / 16384.f); ((float*)(lds + A_SW))[8 + wv] = sabs; }
#define COL4(R) { put4<(R) * 272>(scb, pk2(p0), pk2(p1), pk2(p2), pk2(p3)); \
                  if ((R) < 28) { p0 = cmul(p0, M32); p1 = cmul(p1, M32); p2 = cmul(p2, M32); p3 = cmul(p3, M32); } }
    COL4(0) COL4(4) COL4(8) COL4(12) COL4(16) COL4(20) COL4(24) COL4(28)
#undef COL4
    const CM M1 = mk(E1), MF = mk(F);
    const f32x2 E2 = cmul(E1, M1);
    const CM M2 = mk(E2), M4 = mk(cmul(E2, M2));
    const f32x2 X0 = {CBr * scA, -CBi * scA};
    const f32x2 X1 = cmul(X0, M1), G1 = cmul(X0, MF);
    const f32x2 X2 = cmul(X0, M2), X3 = cmul(X1, M2), G2 = cmul(G1, MF), Y1 = cmul(G1, M2);
    put4<(32 + 0) * 272>(scb, pk2(X0), pk2(X1), pk2(X2), pk2(X3));
    const f32x2 X4 = cmul(X0, M4), X5 = cmul(X1, M4), X6 = cmul(X2, M4), X7 = cmul(X3, M4);
    const f32x2 G3 = cmul(G2, MF), Y2 = cmul(G1, M4), Y3 = cmul(Y1, M4);
    put4<(32 + 4) * 272>(scb, pk2(X4), pk2(X5), pk2(X6), pk2(X7));
    put4<(32 + 8) * 272>(scb, pk2(G1), pk2(Y1), pk2(Y2), pk2(Y3));
    const f32x2 G4 = cmul(G3, MF), Z2 = cmul(G2, M4), Z3 = cmul(G3, M4);
    put4<(32 + 12) * 272>(scb, pk2(G2), pk2(Z2), pk2(G3), pk2(Z3));
    const CM MF2 = mk(cmul(F, MF));
    const f32x2 G5 = cmul(G3, MF2), G6 = cmul(G4, MF2);
    const f32x2 G7 = cmul(G5, MF2), G8 = cmul(G6, MF2);
    put4<(32 + 16) * 272>(scb, pk2(G4), pk2(G5), pk2(G6), pk2(G7));
    {
      const u32 g8 = pk2(G8);
      asm volatile("s_mov_b32 m0, %0\n\ts_nop 0\n\tds_write_addtid_b32 %1 offset:%2" :: "s"(scb), "v"(g8), "n"((32 + 20) * 272) : "memory");
    }
    if (wv < 4) fetch();
    char* scr = lds + A_SCR + wv * A_SCRW;
    const char* fr = scr + (lane & 31) * 272 + (lane >> 5) * 16;
    f16x8 bf[8], af[8];
#pragma unroll
    for (int ks = 0; ks < 8; ++ks) { bf[ks] = *(const f16x8*)(fr + ks * 32); af[ks] = *(const f16x8*)(fr + 32 * 272 + ks * 32); }
    f32x16 acc0 = {}, acc1 = {};
#pragma unroll
    for (int ks = 0; ks < 8; ks += 2) {
      acc0 = __builtin_amdgcn_mfma_f32_32x32x16_f16(af[ks], bf[ks], acc0, 0, 0, 0);
      acc1 = __builtin_amdgcn_mfma_f32_32x32x16_f16(af[ks + 1], bf[ks + 1], acc1, 0, 0, 0);
    }
#pragma unroll
    for (int r = 0; r < 12; ++r) {
      int row = (r & 3) + 8 * (r >> 2) + 4 * (lane >> 5);
      *(float*)(scr + row * 128 + (lane & 31) * 4) = acc0[r] + acc1[r];
    }
  }
  asm volatile("s_waitcnt lgkmcnt(0)" ::: "memory");
  bar();
  float kinv0, kinv1;
  {
    const float* sw = (const float*)(lds + A_SW);
    const float k0 = pow2_for(sw[8] + sw[9] + sw[10] + sw[11], 15), k1 = pow2_for(sw[12] + sw[13] + sw[14] + sw[15], 15);
    kinv0 = pow2_inv(k0); kinv1 = pow2_inv(k1);
    const float ks = ch ? k1 : k0;
    const float i0 = sw[4 * ch] * ks, i1 = sw[4 * ch + 1] * ks, i2 = sw[4 * ch + 2] * ks, i3 = sw[4 * ch + 3] * ks;
    const char* p = lds + A_SCR + (4 * ch) * A_SCRW + (tid & 255) * 4;
    float v[3][4];
#pragma unroll
    for (int it = 0; it < 3; ++it)
#pragma unroll
      for (int k = 0; k < 4; ++k) v[it][k] = *(const float*)(p + it * 1024 + k * A_SCRW);
#pragma unroll
    for (int it = 0; it < 3; ++it) {
      const int e = (tid & 255) + 256 * it;
      const float t = v[it][0] * i0 + v[it][1] * i1 + v[it][2] * i2 + v[it][3] * i3;
      if (it < 2 || e < 672) *(_Float16*)(lds + A_TB + ch * A_TBC + e * 2) = (_Float16)t;
    }
  }
  asm volatile("s_waitcnt lgkmcnt(0)" ::: "memory");
  bar();
  if (wv >= 4) {
#pragma unroll
    for (int cc = 0; cc < 2; ++cc) {
      const char* tb = lds + A_TB + cc * A_TBC;
      u32 P[6];
#pragma unroll
      for (int k = 0; k < 6; ++k) {
        const u32 iw = k < 4 ? ix0[k] : ix1[k - 4];
        const u32 lo = *(const u16*)(tb + (iw & 0xffffu));
        const u32 hi = (k < 5) ? *(const u16*)(tb + (iw >> 16)) : 0u;
        P[k] = lo | (hi << 16);
      }
      u32 Q[5];
#pragma unroll
      for (int k = 0; k < 5; ++k) Q[k] = __builtin_amdgcn_alignbit(P[k + 1], P[k], 16);
      char* cp = lds + A_CP + cc * A_CPC + t8 * 16;
      *(u32x4*)(cp + 0 * A_CPS) = (u32x4){Q[1], Q[2], Q[3], Q[4]};
      *(u32x4*)(cp + 1 * A_CPS) = (u32x4){P[1], P[2], P[3], P[4]};
      *(u32x4*)(cp + 2 * A_CPS) = (u32x4){Q[0], Q[1], Q[2], Q[3]};
      *(u32x4*)(cp + 3 * A_CPS) = (u32x4){P[0], P[1], P[2], P[3]};
    }
    if (t8 < 32) *(u32x4*)(lds + A_CP + (t8 >> 2) * A_CPS + (256 + (t8 & 3)) * 16) = (u32x4){0, 0, 0, 0};
  } else {
    _Pragma("unroll 1") for (int spin = 0; spin < (1 << 16); ++spin) {
      u32 bad = 0;
#pragma unroll
      for (int i = 0; i < 4; ++i) bad |= (g[i][1] ^ tag) | (g[i][3] ^ tag) | (xg[i][1] ^ tag) | (xg[i][3] ^ tag);
      if (__all(bad == 0)) break;
      __builtin_amdgcn_s_sleep(2);
      fetch();
    }
    float uu[2][8];
#pragma unroll
    for (int i = 0; i < 8; ++i) {
      const u32 st = g[i >> 1][(i & 1) * 2];
      const float m = bf2f(st & 0xffffu), rs_ = __builtin_amdgcn_rcpf(bfhi(st));
#pragma unroll
      for (int c = 0; c < 2; ++c) {
        const u32 xw = xg[2 * c + (i >> 2)][(i & 2)];
        const float xv = (i & 1) ? bfhi(xw) : bf2f(xw & 0xffffu);
        uu[c][i] = rbf(rbf(xv - m) * rs_);
      }
    }
#pragma unroll
    for (int c = 0; c < 2; ++c) {
      *(u32x4*)(lds + A_UU + c * A_UUC + ((tid >> 2) + 31) * 80 + (tid & 3) * 16) =
          (u32x4){pk_f16(uu[c][0], uu[c][1]), pk_f16(uu[c][2], uu[c][3]), pk_f16(uu[c][4], uu[c][5]), pk_f16(uu[c][6], uu[c][7])};
      *(u32x4*)(lds + A_XX + c * 5120 + (tid >> 2) * 80 + (tid & 3) * 16) = (u32x4){xg[2 * c][0], xg[2 * c][2], xg[2 * c + 1][0], xg[2 * c + 1][2]};
    }
  }
  asm volatile("s_waitcnt lgkmcnt(0)" ::: "memory");
  bar();
  if (b == 0 && tid == 0) __hip_atomic_store(epoch_p, tag, RLX_AGENT);
  const int m = 4 * (xcd >> 1) + j / 7, nb = 7 * (xcd & 1) + j % 7;
  const int mm = 4 * xcd + j / 7, nn = j % 7;
  const int lw = wv & 3;
  const u32 ldsb = lds_addr(lds);
  const rsrc_t rg = mkrsrc(wg + 128 * nb), ru = mkrsrc(wu + 128 * nb), rh = mkrsrc((const u16*)(ws + WS_HT) + 128 * m);
  const int wrow = 8 * lw + (lane >> 3), wcp = lane & 7, wkey = ((wrow >> 1) & 1) << 2;
  const u32 vg = (wrow * FF + (wcp ^ wkey) * 8) * 2, vu = (wrow * FF + (wcp ^ wkey ^ 2) * 8) * 2;
  auto issueWs = [&](u32 slot, u32 so) { const u32 d = ldsb + P_W + slot * SLOT + lw * 1024; bdma16(rg, vg, so, d); bdma16(ru, vu, so, d + 4096); };
  auto issueW = [&](int t) { const int p_ = t >= 14, s_ = p_ ? 27 - t : t; issueWs((t + 2) % 7, s_ * 32 * FF * 2 + (p_ ^ (m & 1)) * 128); };
  const int hrow = 4 * lw + (lane >> 4), hcp = lane & 15;
  const u32 vh = (hrow * HS + (hcp ^ ((hrow & 3) << 2)) * 8) * 2;
  auto issueH = [&](int s_) {
    const u32 d = ldsb + (s_ % 12) * SLOT + lw * 1024, so = s_ * 32 * HS * 2;
    bdma16_dev(rh, vh, so, d); bdma16_dev(rh, vh, so + 16 * HS * 2, d + 4096);
  };
  if (wv >= 4) { issueW(0); issueW(1); issueW(2); }
  const int cw = (wv >> 1) & 1, hf = wv & 1;
  const int q = lane & 31, h = lane >> 5;
  if (wv < 4) {
    const int a4 = q >> 2, s4 = q & 3;
    const char* abase = lds + A_CP + cw * A_CPC + s4 * A_CPS + 4088 - 8 * a4 + 16 * h - 32 * (32 * hf + 30) - 2048;
    const char* bbase = lds + A_UU + cw * A_UUC + (q + 31 - (16 * hf + 15)) * 80 + 16 * h;
    typedef _Float16 f16x4v __attribute__((ext_vector_type(4)));
    auto lda = [&](const char* p_) {
      const f16x4v lo = *(volatile LAS const f16x4v*)(p_), hi = *(volatile LAS const f16x4v*)(p_ + 8);
      return __builtin_shufflevector(lo, hi, 0, 1, 2, 3, 4, 5, 6, 7);
    };
    f32x16 y0 = {}, y1 = {};
    f16x8 fa[2][8];
#pragma unroll
    for (int kk = 0; kk < 2; ++kk) {
      const int erel = 30 + kk;
      fa[0][4 * kk] = lda(abase + 2048 + 32 * erel);
      fa[0][4 * kk + 1] = *(const f16x8*)(bbase + 15 * 80 + kk * 32);
      fa[0][4 * kk + 2] = *(const f16x8*)(bbase + (15 + 32) * 80 + kk * 32);
      fa[0][4 * kk + 3] = lda(abase + 32 * erel);
    }
#pragma unroll
    for (int i = 0; i < 16; ++i) {
      const f16x8 (&f)[8] = fa[i & 1];
      f16x8 (&g)[8] = fa[(i + 1) & 1];
#pragma unroll
      for (int kk = 0; kk < 2; ++kk) {
        const int erel = 30 - (2 * (i + 1) - kk);
        y0 = __builtin_amdgcn_mfma_f32_32x32x16_f16(f[4 * kk], f[4 * kk + 1], y0, 0, 0, 0);
        if (i + 1 < 16) g[4 * kk] = lda(abase + 2048 + 32 * erel);
        FENCE();
        y1 = __builtin_amdgcn_mfma_f32_32x32x16_f16(f[4 * kk], f[4 * kk + 2], y1, 0, 0, 0);
        if (i + 1 < 16) { g[4 * kk + 1] = *(const f16x8*)(bbase + (15 - i - 1) * 80 + kk * 32); g[4 * kk + 2] = *(const f16x8*)(bbase + (15 - i - 1 + 32) * 80 + kk * 32); }
        FENCE();
        y1 = __builtin_amdgcn_mfma_f32_32x32x16_f16(f[4 * kk + 3], f[4 * kk + 1], y1, 0, 0, 0);
        if (i + 1 < 16) g[4 * kk + 3] = lda(abase + 32 * erel);
        FENCE();
      }
    }
    char* pp = lds + A_EX + wv * A_EXW + q * 144 + h * 16;
#pragma unroll
    for (int g = 0; g < 4; ++g) {
      *(f32x4*)(pp + g * 32) = (f32x4){y0[4 * g], y0[4 * g + 1], y0[4 * g + 2], y0[4 * g + 3]};
      *(f32x4*)(pp + 32 * 144 + g * 32) = (f32x4){y1[4 * g], y1[4 * g + 1], y1[4 * g + 2], y1[4 * g + 3]};
    }
  }
  asm volatile("s_waitcnt lgkmcnt(0)" ::: "memory");
  bar();
  {
    const int c = tid >> 8;
    const char* pp = lds + A_EX + (2 * c) * A_EXW + (t8 >> 2) * 144 + (t8 & 3) * 32;
    const f32x4 a0 = *(const f32x4*)pp, a1 = *(const f32x4*)(pp + 16), b0 = *(const f32x4*)(pp + A_EXW), b1 = *(const f32x4*)(pp + A_EXW + 16);
    const f16x8 u8 = *(const f16x8*)(lds + A_UU + c * A_UUC + ((t8 >> 2) + 31) * 80 + (t8 & 3) * 16);
    const u32x4 x8 = *(const u32x4*)(lds + A_XX + c * 5120 + (t8 >> 2) * 80 + (t8 & 3) * 16);
    const float ki = c ? kinv1 : kinv0, dk = c ? bfhi(p_d01) : bf2f(p_d01 & 0xffffu);
    u32x4 o;
#pragma unroll
    for (int i = 0; i < 4; ++i) {
      const float y0 = (i < 2 ? a0[2 * i] + b0[2 * i] : a1[2 * i - 4] + b1[2 * i - 4]) * ki;
      const float y1 = (i < 2 ? a0[2 * i + 1] + b0[2 * i + 1] : a1[2 * i - 3] + b1[2 * i - 3]) * ki;
      const u32 yb = pk_bf16(y0, y1), ub = pk_bf16((float)u8[2 * i] * dk, (float)u8[2 * i + 1] * dk);
      const u32 sb = pk_bf16(bf2f(yb & 0xffffu) + bf2f(ub & 0xffffu), bfhi(yb) + bfhi(ub));
      const u32 xw = x8[i];
      o[i] = pk_bf16(bf2f(xw & 0xffffu) + bf2f(sb & 0xffffu), bfhi(xw) + bfhi(sb));
    }
    __builtin_amdgcn_raw_buffer_store_b128(o, mkrsrc(ws + WS_HT), ((c0 + c) * HS + 8 * t8) * 2, 0, 16);
  }
  wait_vm<0>();
  bar();
  if (tid == 0) __hip_atomic_store((gu32*)(ws + WS_FLA) + w * 32, tag, RLX_AGENT);

  const rsrc_t rw = mkrsrc(wd + 64 * nn);
  const u32 cvh = lane * 16;
  const rsrc_t rH2 = mkrsrc(ws + WS_H + ((size_t)(2 * mm + (lw >> 1)) * 112 + 2 * (lw & 1)) * 1024);
  const int drow = 16 * lw + (lane >> 3);
  const u32 dvw = (drow * DM + ((lane & 7) ^ (((drow >> 1) & 1) << 2)) * 8) * 2;
  auto gW = [&](u32 slot, u32 so) { const u32 d = ldsb + slot * SLOT + lw * 2048; bdma16(rw, dvw, so, d); bdma16(rw, dvw, so + 8 * DM * 2, d + 1024); };
  auto gH = [&](u32 pos, u32 so) { const u32 d = ldsb + (pos < 7 ? 12 + pos : pos - 7) * SLOT + lw * 2048; bdma16_dev(rH2, cvh, so, d); bdma16_dev(rH2, cvh, so + 1024, d + 1024); };
  const int hf0 = (mm >> 1) & 1;
  auto soW = [&](int u) { return (u32)((128 * (u >= 14 ? u - 14 : u) + 64 * ((u >= 14) ^ hf0)) * DM * 2); };
  auto soH = [&](int u) { return (u32)(u * 4096); };

  if (wv >= 4) {
    const int lk = lw & 1;
    issueW(3); issueW(4);
    if (lw >= 2) {
      {
        const rsrc_t rc = mkrsrc(ws + WS_CS + (lk * FF + 128 * nb) * 8);
        u32x4 cg;
        _Pragma("unroll 1") for (int spin = 0; spin < (1 << 16); ++spin) {
          cg = __builtin_amdgcn_raw_buffer_load_b128(rc, lane * 16, 0, 16);
          if (__all(cg[1] == tag && cg[3] == tag)) break;
          __builtin_amdgcn_s_sleep(2);
        }
        *(u32x2*)(lds + B_CS + lk * 512 + lane * 8) = (u32x2){cg[0], cg[2]};
      }
      gu32* f0 = (gu32*)(ws + WS_FLA) + (lk * 112 + (lane < 56 ? lane : 0)) * 32;
      _Pragma("unroll 1") for (int spin = 0; spin < (1 << 16); ++spin) {
        const u32 a_ = __hip_atomic_load(f0, RLX_AGENT), b_ = __hip_atomic_load(f0 + 56 * 32, RLX_AGENT);
        if (__all(a_ == tag && b_ == tag)) break;
        __builtin_amdgcn_s_sleep(2);
      }
      if (lane == 0) __hip_atomic_fetch_add((LAS u32*)(lds + B_CNT), 1u, __ATOMIC_RELAXED, __HIP_MEMORY_SCOPE_WORKGROUP);
    }
    _Pragma("unroll 1") for (int spin = 0; spin < (1 << 20); ++spin) {
      if (__hip_atomic_load((LAS u32*)(lds + B_CNT), __ATOMIC_RELAXED, __HIP_MEMORY_SCOPE_WORKGROUP) >= 2u) break;
      __builtin_amdgcn_s_sleep(1);
    }
    issueH(0); issueH(1); issueH(2); issueH(3); issueH(4); issueH(5); issueH(6); issueH(7); issueH(8); issueH(9);
    wait_vm<18>(); bar(); issueW(5); issueH(10);
    wait_vm<20>(); bar(); issueW(6); issueH(11);
    wait_vm<22>(); bar(); issueW(7); issueH(12);
    wait_vm<24>(); bar(); issueW(8); issueH(13);
    wait_vm<26>(); bar(); issueW(9);
    wait_vm<16>(); bar(); issueW(10);
    wait_vm<14>(); bar(); issueW(11);
    wait_vm<12>(); bar(); issueW(12);
    wait_vm<10>(); bar(); issueW(13);
    {
      u32 slot = 2, so = 13 * 32 * FF * 2 + ((m & 1) ^ 1) * 128;
#pragma unroll 1
      for (int t = 9; t <= 15; ++t) { wait_vm<8>(); bar(); issueWs(slot, so); slot = slot == 6 ? 0 : slot + 1; so -= 32 * FF * 2; }
    }
    wait_vm<8>(); bar(); issueW(21); issueH(1);
    wait_vm<10>(); bar(); issueW(22); issueH(0);
    wait_vm<12>(); bar(); issueW(23); gW(11, soW(0));
    wait_vm<14>(); bar(); issueW(24); gW(10, soW(1));
    wait_vm<16>(); bar(); issueW(25); gW(9, soW(2));
    wait_vm<18>(); bar(); issueW(26); gW(8, soW(3));
    wait_vm<18>(); bar(); issueW(27); gW(7, soW(4));
    wait_vm<18>(); bar(); gW(6, soW(5));
    wait_vm<16>(); bar(); gW(5, soW(6));
    wait_vm<14>(); bar(); wait_vm<10>(); bar(); wait_vm<6>(); bar();
    bar();
    {
      gu32* fl = (gu32*)(ws + WS_FLB) + (((mm >> 1) * 14 + (lane < 14 ? lane : 0)) * 2) * 32;
      _Pragma("unroll 1") for (int spin = 0; spin < (1 << 16); ++spin) {
        if (__all(__hip_atomic_load(fl, RLX_AGENT) == tag)) break;
        __builtin_amdgcn_s_sleep(2);
      }
    }
    wait_vm<0>();
#pragma unroll
    for (int u = 0; u < 10; ++u) gH(u, soH(u));
    wait_vm<18>(); bar(); gH(10, soH(10));
    wait_vm<18>(); bar(); gH(11, soH(11));
    wait_vm<18>(); bar(); gW(11, soW(7)); gH(0, soH(12));
    wait_vm<20>(); bar(); gW(10, soW(8)); gH(1, soH(13));
    wait_vm<22>(); bar(); gW(9, soW(9)); gH(2, soH(14));
    wait_vm<24>(); bar(); gW(8, soW(10)); gH(3, soH(15));
    wait_vm<26>(); bar(); gW(7, soW(11)); gH(4, soH(16));
    wait_vm<18>(); bar(); gW(6, soW(12)); gH(5, soH(17));
    wait_vm<18>(); bar(); gW(5, soW(13)); gH(6, soH(18));
    {
      u32 wslot = 11, hpos = 7, sw = soW(14), sh = soH(19);
#pragma unroll 1
      for (int u = 9; u <= 17; ++u) {
        wait_vm<18>(); bar(); gW(wslot, sw); gH(hpos, sh);
        wslot = wslot == 5 ? 11 : wslot - 1; hpos = hpos == 11 ? 0 : hpos + 1; sw += 128 * DM * 2; sh += 4096;
      }
      wait_vm<18>(); bar(); gW(wslot, sw); wslot = wslot == 5 ? 11 : wslot - 1; sw += 128 * DM * 2;
      wait_vm<16>(); bar(); gW(wslot, sw); wslot = wslot == 5 ? 11 : wslot - 1; sw += 128 * DM * 2;
      wait_vm<14>(); bar(); gW(wslot, sw); wslot = wslot == 5 ? 11 : wslot - 1; sw += 128 * DM * 2;
      wait_vm<12>(); bar(); gW(wslot, sw); wslot = wslot == 5 ? 11 : wslot - 1; sw += 128 * DM * 2;
      wait_vm<10>(); bar(); gW(wslot, sw);
    }
    wait_vm<8>(); bar(); wait_vm<6>(); bar(); wait_vm<4>(); bar(); wait_vm<2>(); bar(); wait_vm<0>(); bar();
    asm volatile(".Lfused_end2:");
    return;
  }

  const int tp = wv & 1, ag = wv >> 1;
  const int grp = lane >> 4, fq = (lane & 15) >> 2, fp = lane & 3, fh = grp >> 1;
  int ao[2], bo[2];
  {
    const int akey = (((fq >> 1) & 1) << 2) ^ ((grp & 1) << 1);
#pragma unroll
    for (int i = 0; i < 2; ++i) ao[i] = P_W + (grp & 1) * 4096 + (8 * fh + fq) * 128 + (((2 * (2 * ag + i) + (fp >> 1)) ^ akey) * 16) + (fp & 1) * 8;
#pragma unroll
    for (int t = 0; t < 2; ++t)
      bo[t] = (8 * fh + fq) * 256 + (((4 * (2 * tp + t) + 2 * (grp & 1) + (fp >> 1)) ^ (fq << 2)) * 16) + (fp & 1) * 8;
  }
  f32x16 accs[2][2][2];
  struct KF { s16x8 a[2][2], b[2][2]; };
  KF X, Y;
  const rsrc_t rH = mkrsrc(ws + WS_H);
  const int hh = lane >> 5;
  f32x2 st2[2];
  auto epilogue = [&](auto pass_) {
    constexpr int p_ = decltype(pass_)::value;
    auto& acc = accs[p_];
    const float* cs = (const float*)(lds + B_CS);
#pragma unroll
    for (int i = 0; i < 2; ++i) {
      const int tile = 4 * (p_ ^ (m & 1)) + 2 * ag + i, f0 = 16 * tile + 4 * hh;
      const f32x4 g0 = *(const f32x4*)(cs + f0), g1 = *(const f32x4*)(cs + f0 + 8);
      const f32x4 u0 = *(const f32x4*)(cs + 128 + f0), u1 = *(const f32x4*)(cs + 128 + f0 + 8);
#pragma unroll
      for (int t = 0; t < 2; ++t) {
        const f32x2 nm = {st2[t][0], st2[t][0]}, r2 = {st2[t][1], st2[t][1]};
        u32x4 o;
#pragma unroll
        for (int r = 0; r < 4; ++r) {
          const f32x4 cg = r < 2 ? g0 : g1, cu = r < 2 ? u0 : u1;
          const int e = 2 * (r & 1);
          const f32x2 g = __builtin_elementwise_fma(nm, (f32x2){cg[e], cg[e + 1]}, (f32x2){acc[i][t][2 * r], acc[i][t][2 * r + 1]});
          const f32x2 u = __builtin_elementwise_fma(nm, (f32x2){cu[e], cu[e + 1]}, (f32x2){acc[i][t][8 + 2 * r], acc[i][t][8 + 2 * r + 1]});
          const f32x2 hv = (g * r2) * u;
          o[r] = pk_bf16(hv[0], hv[1]);
        }
        __builtin_amdgcn_raw_buffer_store_b128(o, rH, (((4 * m + 2 * tp + t) * 112 + 56 * p_ + 4 * nb + 2 * ag + i) * 64 + lane) * 16, 0, 16);
      }
    }
  };
  auto publish = [&](int p_) {
    asm volatile("s_waitcnt vmcnt(0)" ::: "memory");
    u32 old = 0;
    if (lane == 0) old = __hip_atomic_fetch_add((LAS u32*)(lds + CNT0) + p_, 1u, __ATOMIC_RELAXED, __HIP_MEMORY_SCOPE_WORKGROUP);
    if (lane == 0 && old == 3u) __hip_atomic_store((gu32*)(ws + WS_FLB) + ((m * 14 + nb) * 2 + p_) * 32, tag, RLX_AGENT);
  };
  auto mm8 = [&](auto pass_, const KF& f) {
    auto& acc = accs[decltype(pass_)::value];
#pragma unroll
    for (int g = 0; g < 8; ++g) {
      const int ks = g >> 2, i = (g >> 1) & 1, t = g & 1;
      acc[i][t] = __builtin_amdgcn_mfma_f32_32x32x16_bf16(__builtin_bit_cast(bf16x8, f.a[ks][i]), __builtin_bit_cast(bf16x8, f.b[ks][t]), acc[i][t], 0, 0, 0);
    }
  };
  int hsl = 0, wsl = 2;
  auto trf = [&](u32 a0, int off) {
    s16x4 x0 = __builtin_amdgcn_ds_read_tr16_b64_v4i16((LAS s16x4*)(size_t)(a0 + off));
    return x0;
  };
  auto stage = [&](auto pass_, auto first, KF& cur, const KF& prev) {
    auto& acc = accs[decltype(pass_)::value];
    u32 hb0 = ldsb + hsl * SLOT + bo[0], hb1 = ldsb + hsl * SLOT + bo[1], wa0 = ldsb + wsl * SLOT + ao[0], wa1 = ldsb + wsl * SLOT + ao[1];
    asm volatile("" : "+v"(hb0), "+v"(hb1), "+v"(wa0), "+v"(wa1));
    auto rdb = [&](int rk, int rt_) {
      const u32 a0 = rt_ ? hb1 : hb0;
      cur.b[rk][rt_] = __builtin_shufflevector(trf(a0, rk * 16 * 256), trf(a0, (rk * 16 + 4) * 256), 0, 1, 2, 3, 4, 5, 6, 7);
    };
    auto rda = [&](int rk, int rt_) {
      const u32 a0 = rt_ ? wa1 : wa0;
      cur.a[rk][rt_] = __builtin_shufflevector(trf(a0, rk * 16 * 128), trf(a0, (rk * 16 + 4) * 128), 0, 1, 2, 3, 4, 5, 6, 7);
    };
#pragma unroll
    for (int g = 0; g < 8; ++g) {
      const int ks = g >> 2, i = (g >> 1) & 1, t = g & 1;
      if (!decltype(first)::value)
        acc[i][t] = __builtin_amdgcn_mfma_f32_32x32x16_bf16(__builtin_bit_cast(bf16x8, prev.a[ks][i]), __builtin_bit_cast(bf16x8, prev.b[ks][t]), acc[i][t], 0, 0, 0);
      if (g == 0) { FENCE(); bar(); }
      if (g == 1) { rdb(0, 0); rda(0, 0); }
      if (g == 2) { rdb(0, 1); }
      if (g == 3) { rda(0, 1); }
      if (g == 4) { rdb(1, 0); }
      if (g == 5) { rda(1, 0); }
      if (g == 6) { rdb(1, 1); }
      if (g == 7) { rda(1, 1); }
      FENCE();
    }
    wsl = wsl == 6 ? 0 : wsl + 1;
  };
#pragma unroll
  for (int g = 0; g < 8; ++g) accs[g >> 2][(g >> 1) & 1][g & 1] = (f32x16){};
  stage(IC<0>{}, IC<1>{}, X, Y); hsl = 1;
  stage(IC<0>{}, IC<0>{}, Y, X); hsl = 2;
#pragma unroll 1
  for (int t = 2; t < 14; t += 2) {
    stage(IC<0>{}, IC<0>{}, X, Y); hsl = hsl == 11 ? 0 : hsl + 1;
    stage(IC<0>{}, IC<0>{}, Y, X); hsl = hsl == 11 ? 0 : hsl + 1;
  }
  const rsrc_t rg2 = mkrsrc(ws + WS_GRAN2 + (size_t)(128 * m + 64 * tp) * 8);
  u32x2 sg[2];
#pragma unroll
  for (int t = 0; t < 2; ++t) sg[t] = __builtin_amdgcn_raw_buffer_load_b64(rg2, (32 * t + (lane & 31)) * 8, 0, 16);
  mm8(IC<0>{}, Y);
  hsl = 1;
  stage(IC<1>{}, IC<1>{}, X, Y); hsl = 0;
  stage(IC<1>{}, IC<0>{}, Y, X); hsl = 11;
  stage(IC<1>{}, IC<0>{}, X, Y); hsl = 10;
  _Pragma("unroll 1") for (int spin = 0; spin < (1 << 16); ++spin) {
    if (__all(sg[0][1] == tag && sg[1][1] == tag)) break;
    __builtin_amdgcn_s_sleep(2);
#pragma unroll
    for (int t = 0; t < 2; ++t) sg[t] = __builtin_amdgcn_raw_buffer_load_b64(rg2, (32 * t + (lane & 31)) * 8, 0, 16);
  }
#pragma unroll
  for (int t = 0; t < 2; ++t) { const float rs = __builtin_amdgcn_rcpf(bfhi(sg[t][0])); st2[t] = (f32x2){-bf2f(sg[t][0] & 0xffffu), rs * rs}; }
  epilogue(IC<0>{});
  stage(IC<1>{}, IC<0>{}, Y, X); hsl = 9;
  stage(IC<1>{}, IC<0>{}, X, Y); hsl = 8;
  stage(IC<1>{}, IC<0>{}, Y, X); hsl = 7;
  publish(0);
#pragma unroll 1
  for (int t = 20; t < 28; t += 2) {
    stage(IC<1>{}, IC<0>{}, X, Y); hsl = hsl - 1;
    stage(IC<1>{}, IC<0>{}, Y, X); hsl = hsl - 1;
  }
  mm8(IC<1>{}, Y);
  bar();
  epilogue(IC<1>{});
  publish(1);
  const int dtl = wv & 1, tt = wv >> 1;
  const int tok = 64 * mm + 32 * tt + (lane & 31);
  const rsrc_t rres = mkrsrc(ws + WS_HT);
  u32 res[16];
#pragma unroll
  for (int i = 0; i < 16; ++i) res[i] = __builtin_amdgcn_raw_buffer_load_b16(rres, ((64 * nn + 32 * dtl + 8 * (i >> 2) + 4 * hh + (i & 3)) * HS + tok) * 2, 0, 16);
  const int achunk = 4 * dtl + 2 * (grp & 1) + (fp >> 1);
  const int caoff = (4 * fh + fq) * 128 + ((achunk ^ (((fq >> 1) & 1) << 2)) * 16) + (fp & 1) * 8;
  const int cboff = tt * 4096 + lane * 16;
  f32x16 cacc = {};
  struct F4 { s16x8 a[4]; bf16x8 b[4]; };
  F4 Xc, Yc;
  int cwb = 11, chv = 0;
  auto cstage = [&](auto first, F4& cur, const F4& prev) {
    bar();
    const char* sth = lds + (chv < 7 ? 12 + chv : chv - 7) * SLOT;
    const char* stw = lds + cwb * SLOT;
#pragma unroll
    for (int k = 0; k < 4; ++k) {
      if (!decltype(first)::value) cacc = __builtin_amdgcn_mfma_f32_32x32x16_bf16(__builtin_bit_cast(bf16x8, prev.a[k]), prev.b[k], cacc, 0, 0, 0);
      cur.a[k] = tr_frag(stw + caoff + k * 16 * 128, stw + caoff + (k * 16 + 8) * 128);
      cur.b[k] = *(const bf16x8*)(sth + cboff + k * 1024);
      FENCE();
    }
    chv = chv == 11 ? 0 : chv + 1; cwb = cwb == 5 ? 11 : cwb - 1;
  };
  cstage(IC<1>{}, Xc, Yc);
  cstage(IC<0>{}, Yc, Xc);
  cstage(IC<0>{}, Xc, Yc);
  cstage(IC<0>{}, Yc, Xc);
  if (wv == 0) {
    gu32* fl = (gu32*)(ws + WS_FLB) + (((mm >> 1) * 14 + (lane < 14 ? lane : 0)) * 2 + 1) * 32;
    _Pragma("unroll 1") for (int spin = 0; spin < (1 << 16); ++spin) {
      if (__all(__hip_atomic_load(fl, RLX_AGENT) == tag)) break;
      __builtin_amdgcn_s_sleep(2);
    }
  }
#pragma unroll 1
  for (int u = 4; u < 28; u += 2) { cstage(IC<0>{}, Xc, Yc); cstage(IC<0>{}, Yc, Xc); }
#pragma unroll
  for (int k = 0; k < 4; ++k) cacc = __builtin_amdgcn_mfma_f32_32x32x16_bf16(__builtin_bit_cast(bf16x8, Yc.a[k]), Yc.b[k], cacc, 0, 0, 0);
  u16* op = out + tok * DM + 64 * nn + 32 * dtl + 4 * hh;
#pragma unroll
  for (int g = 0; g < 4; ++g)
    *(u32x2*)(op + 8 * g) = (u32x2){pk_bf16(bf2f(res[4 * g]) + rbf(cacc[4 * g]), bf2f(res[4 * g + 1]) + rbf(cacc[4 * g + 1])),
                                    pk_bf16(bf2f(res[4 * g + 2]) + rbf(cacc[4 * g + 2]), bf2f(res[4 * g + 3]) + rbf(cacc[4 * g + 3]))};
  asm volatile(".Lfused_end:");
}

extern "C" void kernel_launch(void* const* d_in, const int* in_sizes, int n_in, void* d_out, int out_size, void* d_ws, size_t ws_size,
                              hipStream_t stream) {
  (void)in_sizes; (void)n_in; (void)out_size; (void)ws_size;
  (void)hipFuncSetAttribute((const void*)fused_kernel, hipFuncAttributeMaxDynamicSharedMemorySize, K_LDS);
  fused_kernel<<<256, 512, K_LDS, stream>>>((const u16*)d_in[0], (const u16*)d_in[1], (const u16*)d_in[2], (const u16*)d_in[3], (const u16*)d_in[4],
                                            (const u16*)d_in[5], (const u16*)d_in[6], (const u16*)d_in[7], (const u16*)d_in[8],
                                            (const u16*)d_in[10], (const u16*)d_in[11], (const u16*)d_in[12], (char*)d_ws, (u16*)d_out);
}
```
